```python
import torch

def _softmax(x, axis=-1):
    return torch.softmax(x, dim=axis)
def _gelu(x):
    return torch.nn.functional.gelu(x, approximate="tanh")
def _silu(x):
    return torch.nn.functional.silu(x)
def _sigmoid(x):
    return torch.sigmoid(x)
def _layernorm(x, axis=-1, eps=1e-5):
    m = x.mean(dim=axis, keepdim=True)
    v = x.var(dim=axis, keepdim=True, unbiased=False)
    return (x - m) / torch.sqrt(v + eps)
def _scatter_add(tgt, idx, src, axis=0):
    return tgt.clone().index_add_(axis, idx, src)
def _segment_sum(src, seg, n):
    out = torch.zeros((n,) + src.shape[1:], dtype=src.dtype)
    return out.index_add_(0, seg, src)
def _pad(x, pw):
    flat = tuple(v for pair in reversed(pw) for v in pair)
    return torch.nn.functional.pad(x, flat)

kernel_name = 'sequential_preLN__ssm_conv_s4d__mixer_s_mlp__prompt'

def setup_inputs():

    _w = (lambda a: (lambda t: t.to(torch.bfloat16) if t.is_floating_point() else t)(torch.as_tensor(a)))
    def _A_setup():
        Bsz, L, D, N = 1, 2048, 640, 128
        return {
            "u": _w((lambda *s: torch.randn(*s, dtype=torch.bfloat16) * 0.05)(Bsz, L, D)),
            "log_A_real": _w((lambda *s: torch.randn(*s, dtype=torch.bfloat16) * 6.4)(D, N) * 0.5),
            "A_imag": _w((lambda *s: torch.randn(*s, dtype=torch.bfloat16) * 0.05)(D, N) * 3.14),
            "C_real": _w((lambda *s: torch.randn(*s, dtype=torch.bfloat16) * 0.05)(D, N) / N ** 0.5),
            "C_imag": _w((lambda *s: torch.randn(*s, dtype=torch.bfloat16) * 0.05)(D, N) / N ** 0.5),
            "log_dt": _w((lambda *s: torch.randn(*s, dtype=torch.bfloat16) * 0.05)(D) * 0.5 - 4.0),
            "D_skip": _w((lambda *s: torch.randn(*s, dtype=torch.bfloat16) * 0.05)(D)),
        }
    _A_in = _A_setup()

    def _B_setup():
        B, S, D, F = 1, 2048, 640, 1728
        return {
            "x":  _w((lambda *s: torch.randn(*s, dtype=torch.bfloat16) * 0.05)(B, S, D)),
            "w1": _w((lambda *s: torch.randn(*s, dtype=torch.bfloat16) * 6.4)(D, D) * (D ** -0.5)),
            "w2": _w((lambda *s: torch.randn(*s, dtype=torch.bfloat16) * 6.4)(D, D) * (D ** -0.5)),
            "w3": _w((lambda *s: torch.randn(*s, dtype=torch.bfloat16) * 6.4)(D, F) * (D ** -0.5)),
            "w4": _w((lambda *s: torch.randn(*s, dtype=torch.bfloat16) * 6.4)(F, D) * (F ** -0.5)),
        }
    _B_in = _B_setup()

    return {
        "A_u": _A_in["u"],
        "A_log_A_real": _A_in["log_A_real"],
        "A_A_imag": _A_in["A_imag"],
        "A_C_real": _A_in["C_real"],
        "A_C_imag": _A_in["C_imag"],
        "A_log_dt": _A_in["log_dt"],
        "A_D_skip": _A_in["D_skip"],
        "B_x": _B_in["x"],
        "B_w1": _B_in["w1"],
        "B_w2": _B_in["w2"],
        "B_w3": _B_in["w3"],
        "B_w4": _B_in["w4"],
    }

def reference(A_u, A_log_A_real, A_A_imag, A_C_real, A_C_imag, A_log_dt, A_D_skip, B_x, B_w1, B_w2, B_w3, B_w4):
    def _A_ref(u, log_A_real, A_imag, C_real, C_imag, log_dt, D_skip):
        L = u.shape[1]
        dt = torch.exp(log_dt)
        A = -torch.exp(log_A_real) + 1j * A_imag
        C = (C_real + 1j * C_imag) * dt[:, None]
        pos = (lambda x, dt: x.to(dt))(torch.arange(L), log_dt.dtype)
        P = torch.exp((A * dt[:, None])[:, :, None] * pos[None, None, :])
        K = 2.0 * torch.einsum("dn,dnl->dl", C, P).real
        L2 = 2 * L
        u_t = (lambda x, *d: x.permute(*d))(u, 0, 2, 1)
        Kf = (lambda x, *a, **k: torch.fft.rfft(x.float(), *a, **k))(K, L2)
        Uf = (lambda x, *a, **k: torch.fft.rfft(x.float(), *a, **k))(u_t, L2)
        y = (lambda x, *a, **k: torch.fft.irfft(x, *a, **k).to(torch.bfloat16))(Uf * Kf[None, :, :], L2)[:, :, :L]
        y = (lambda x, *d: x.permute(*d))(y, 0, 2, 1)
        return y + u * D_skip[None, None, :]

    def _B_ref(x, w1, w2, w3, w4):
        B, S, D = x.shape
        h = _gelu(_layernorm(x, axis=-1) @ w1)
        c = D // 4
        g0 = _pad(h[:, :, 0*c:1*c], ((0, 0), (1, 0), (0, 0)))[:, :S, :]
        g1 = _pad(h[:, :, 1*c:2*c], ((0, 0), (0, 1), (0, 0)))[:, 1:, :]
        g2 = _pad(h[:, :, 2*c:3*c], ((0, 0), (2, 0), (0, 0)))[:, :S, :]
        g3 = _pad(h[:, :, 3*c:   ], ((0, 0), (0, 2), (0, 0)))[:, 2:, :]
        hs = torch.concatenate([g0, g1, g2, g3], axis=-1) @ w2
        x1 = x + hs
        h2 = _gelu(_layernorm(x1, axis=-1) @ w3) @ w4
        return x1 + h2

    x = A_u
    h = x + _A_ref(_layernorm(x, axis=-1), A_log_A_real, A_A_imag, A_C_real, A_C_imag, A_log_dt, A_D_skip)
    h2 = h + _B_ref(_layernorm(h, axis=-1), B_w1, B_w2, B_w3, B_w4)
    return h2
```

```cpp
#include <hip/hip_runtime.h>
#include <hip/hip_bf16.h>
#include <stdint.h>

#define POSBF16 1

typedef unsigned short u16;
__device__ __forceinline__ float bf2f(u16 v){ return __uint_as_float(((unsigned)v)<<16); }
__device__ __forceinline__ u16 f2bf(float f){ unsigned u=__float_as_uint(f); return (u16)((u + 0x7FFF + ((u>>16)&1))>>16); }
__device__ __forceinline__ float rbf(float f){ return bf2f(f2bf(f)); }

constexpr int L=2048, D=640, N=128, F=1728;

__global__ void k_ln(const u16* xin, const float* fin, float* out){
  int l=blockIdx.x; __shared__ float red[2][256];
  float v[3]; float s=0;
  for(int i=0;i<3;i++){int d=threadIdx.x+i*256; v[i]= d<D ? (xin? bf2f(xin[l*D+d]) : fin[l*D+d]) : 0.f; s+=v[i];}
  red[0][threadIdx.x]=s; __syncthreads();
  for(int o=128;o>0;o>>=1){ if(threadIdx.x<o) red[0][threadIdx.x]+=red[0][threadIdx.x+o]; __syncthreads(); }
  float m=red[0][0]/D; float q=0;
  for(int i=0;i<3;i++){int d=threadIdx.x+i*256; if(d<D){float t=v[i]-m; q+=t*t;}}
  red[1][threadIdx.x]=q; __syncthreads();
  for(int o=128;o>0;o>>=1){ if(threadIdx.x<o) red[1][threadIdx.x]+=red[1][threadIdx.x+o]; __syncthreads(); }
  float r=rsqrtf(red[1][0]/D+1e-5f);
  for(int i=0;i<3;i++){int d=threadIdx.x+i*256; if(d<D) out[l*D+d]=(v[i]-m)*r;}
}
__global__ void k_kgen(const u16* lar,const u16* aim,const u16* cr,const u16* ci,const u16* ldt,float* K){
  int d=blockIdx.x; int l=blockIdx.y*256+threadIdx.x;
  float dt=rbf(expf(bf2f(ldt[d])));
  float pos = POSBF16 ? rbf((float)l) : (float)l;
  float acc=0;
  for(int n=0;n<N;n++){
    float ar=-rbf(expf(bf2f(lar[d*N+n])))*dt, ai=bf2f(aim[d*N+n])*dt;
    float c_r=bf2f(cr[d*N+n])*dt, c_i=bf2f(ci[d*N+n])*dt;
    float e=expf(ar*pos); float s,c; sincosf(ai*pos,&s,&c);
    acc += e*(c_r*c - c_i*s);
  }
  K[d*L+l]=2.f*acc;
}
__global__ void k_conv(const float* K,const float* u,const u16* x,const u16* dsk,float* h){
  int l=blockIdx.x; int d=blockIdx.y*64+threadIdx.x;
  float acc=0;
  for(int j=0;j<=l;j++) acc+=K[d*L+(l-j)]*u[j*D+d];
  h[l*D+d]=bf2f(x[l*D+d])+acc+u[l*D+d]*bf2f(dsk[d]);
}
__device__ __forceinline__ float gelu(float x){ return 0.5f*x*(1.f+tanhf(0.7978845608028654f*(x+0.044715f*x*x*x))); }
__global__ void k_gemm(const float* A,const u16* W,float* C,u16* Cb,const float* r1,const float* r2,int Kk,int Nn,int mode){
  int l=blockIdx.x; int n=blockIdx.y*64+threadIdx.x;
  float acc=0;
  for(int k=0;k<Kk;k++){
    int ls=l;
    if(mode==1){ int g=k/160; int sh = g==0?1: g==1?-1: g==2?2:-2; ls=l-sh; if(ls<0||ls>=L) continue; }
    acc+=A[ls*Kk+k]*bf2f(W[k*Nn+n]);
  }
  if(mode==0) C[l*Nn+n]=gelu(acc);
  else if(mode==1) C[l*Nn+n]=acc+r1[l*Nn+n];
  else Cb[l*Nn+n]=f2bf(acc+r1[l*Nn+n]+r2[l*Nn+n]);
}

extern "C" void kernel_launch(void* const* d_in, const int* in_sizes, int n_in,
                              void* d_out, int out_size, void* d_ws, size_t ws_size,
                              hipStream_t stream) {
  const u16* x=(const u16*)d_in[0]; const u16* lar=(const u16*)d_in[1]; const u16* aim=(const u16*)d_in[2];
  const u16* cr=(const u16*)d_in[3]; const u16* ci=(const u16*)d_in[4]; const u16* ldt=(const u16*)d_in[5]; const u16* dsk=(const u16*)d_in[6];
  const u16* w1=(const u16*)d_in[8]; const u16* w2=(const u16*)d_in[9]; const u16* w3=(const u16*)d_in[10]; const u16* w4=(const u16*)d_in[11];
  float* ws=(float*)d_ws;
  float* u=ws; float* K=u+L*D; float* h=K+L*D; float* xb=h+L*D; float* z=xb+L*D; float* g1=z+L*D; float* x1=g1+L*D; float* z1=x1+L*D; float* g3=z1+L*D;
  k_ln<<<L,256,0,stream>>>(x,nullptr,u);
  k_kgen<<<dim3(D,L/256),256,0,stream>>>(lar,aim,cr,ci,ldt,K);
  k_conv<<<dim3(L,D/64),64,0,stream>>>(K,u,x,dsk,h);
  k_ln<<<L,256,0,stream>>>(nullptr,h,xb);
  k_ln<<<L,256,0,stream>>>(nullptr,xb,z);
  k_gemm<<<dim3(L,D/64),64,0,stream>>>(z,w1,g1,nullptr,nullptr,nullptr,D,D,0);
  k_gemm<<<dim3(L,D/64),64,0,stream>>>(g1,w2,x1,nullptr,xb,nullptr,D,D,1);
  k_ln<<<L,256,0,stream>>>(nullptr,x1,z1);
  k_gemm<<<dim3(L,F/64),64,0,stream>>>(z1,w3,g3,nullptr,nullptr,nullptr,D,F,0);
  k_gemm<<<dim3(L,D/64),64,0,stream>>>(g3,w4,nullptr,(u16*)d_out,x1,h,F,D,2);
}
```

```cpp
#include <hip/hip_runtime.h>
#include <hip/hip_bf16.h>
#include <stdint.h>
#include <utility>

typedef unsigned short u16;
typedef unsigned u32;
typedef _Float16 f16;
typedef __attribute__((ext_vector_type(2))) _Float16 f16x2;
typedef __attribute__((ext_vector_type(8))) _Float16 f16x8;
typedef __attribute__((ext_vector_type(2))) __bf16 bf16x2;
typedef __attribute__((ext_vector_type(8))) __bf16 bf16x8;
typedef __attribute__((ext_vector_type(2))) float f32x2;
typedef __attribute__((ext_vector_type(4))) float f32x4;
typedef __attribute__((ext_vector_type(2))) unsigned u32x2;
typedef __attribute__((ext_vector_type(4))) unsigned u32x4;
typedef __attribute__((ext_vector_type(4))) short s16x4;
#define LDS_AS __attribute__((address_space(3)))

namespace {

constexpr int L = 2048, D = 640, NM = 128, F = 1728;
constexpr size_t O_UT  = 0;
constexpr size_t O_H   = O_UT  + (size_t)D * L * 2;
constexpr size_t O_G1  = O_H   + (size_t)L * D * 2 + 20480;
constexpr size_t O_X1  = O_G1  + (size_t)L * D * 2 + 20480;
constexpr size_t O_G3  = O_X1  + (size_t)L * D * 2;
constexpr size_t O_W2  = O_G3  + (size_t)L * F * 2;
constexpr size_t O_W3  = O_W2  + (size_t)D * D * 2;
constexpr size_t O_W4  = O_W3  + (size_t)16 * 20 * 7 * 1024;
constexpr size_t O_CS1 = O_W4  + (size_t)D * F * 2 + 16384;
constexpr size_t O_CS3 = O_CS1 + D * 4;
constexpr size_t O_ST0 = O_CS3 + 1792 * 4;
constexpr size_t O_PS  = O_ST0 + L * 8;
constexpr size_t O_FL  = O_PS + 128 * 2048;
constexpr size_t O_EP  = O_FL + 6 * 2048;
constexpr size_t O_CNT = O_EP + 256;
constexpr int FL_LN = 0, FL_W = 1, FL_C = 2, FL_1 = 3, FL_2 = 4, FL_3 = 5;

__device__ __forceinline__ float bf2f(u16 v) { return __uint_as_float(((u32)v) << 16); }
__device__ __forceinline__ float bflo(u32 v) { return __uint_as_float(v << 16); }
__device__ __forceinline__ float bfhi(u32 v) { return __uint_as_float(v & 0xffff0000u); }
__device__ __forceinline__ u32 pkbf(float a, float b) {
  f32x2 v = {a, b};
  return __builtin_bit_cast(u32, __builtin_convertvector(v, bf16x2));
}
__device__ __forceinline__ float rbf(float f) { return bflo(pkbf(f, 0.f)); }
__device__ __forceinline__ u32 pkh(float a, float b) {
  f32x2 v = {a, b};
  return __builtin_bit_cast(u32, __builtin_convertvector(v, f16x2));
}
__device__ __forceinline__ u32 umin(u32 a, u32 b) { return a < b ? a : b; }
__device__ __forceinline__ u32 umax(u32 a, u32 b) { return a > b ? a : b; }
__device__ __forceinline__ bf16x2 as_bf2(u32 v) { return __builtin_bit_cast(bf16x2, v); }
__device__ __forceinline__ f16x2 as_h2(u32 v) { return __builtin_bit_cast(f16x2, v); }
__device__ __forceinline__ u32 lds_addr(const void* p) { return (u32)(size_t)(LDS_AS const void*)p; }
template <class T> __device__ __forceinline__ T lds_ld(u32 a) { return *(LDS_AS const T*)(size_t)a; }
template <class T> __device__ __forceinline__ void lds_st(u32 a, T v) { *(LDS_AS T*)(size_t)a = v; }

template <bool SC1>
__device__ __forceinline__ void glds16(const void* sbase, u32 voff, int goff, u32 lbase, int loff) {
  u32 t;
  if (SC1) asm volatile("s_add_i32 m0, %4, %5\n\tv_add_u32 %0, %3, %1\n\tglobal_load_lds_dwordx4 %0, %2 sc1"
               : "=&v"(t) : "v"(voff), "s"(sbase), "i"(goff), "s"(lbase), "i"(loff) : "memory", "scc");
  else asm volatile("s_add_i32 m0, %4, %5\n\tv_add_u32 %0, %3, %1\n\tglobal_load_lds_dwordx4 %0, %2"
               : "=&v"(t) : "v"(voff), "s"(sbase), "i"(goff), "s"(lbase), "i"(loff) : "memory", "scc");
}
__device__ __forceinline__ void glds16r(const void* sbase, u32 voff, u32 goff, u32 laddr) {
  u32 t;
  asm volatile("s_nop 4\n\ts_mov_b32 m0, %4\n\tv_add_u32 %0, %3, %1\n\tglobal_load_lds_dwordx4 %0, %2"
               : "=&v"(t) : "v"(voff), "s"(sbase), "s"(goff), "s"(laddr) : "memory");
}
template <bool SC1>
__device__ __forceinline__ void glds16_half(const void* sbase, u32 voff, int goff, u32 lbase, int loff) {
  u32 t;
  if (SC1) asm volatile("s_add_i32 m0, %4, %5\n\tv_add_u32 %0, %3, %1\n\ts_mov_b32 exec_hi, 0\n\tglobal_load_lds_dwordx4 %0, %2 sc1\n\ts_mov_b32 exec_hi, -1"
               : "=&v"(t) : "v"(voff), "s"(sbase), "i"(goff), "s"(lbase), "i"(loff) : "memory", "scc");
  else asm volatile("s_add_i32 m0, %4, %5\n\tv_add_u32 %0, %3, %1\n\ts_mov_b32 exec_hi, 0\n\tglobal_load_lds_dwordx4 %0, %2\n\ts_mov_b32 exec_hi, -1"
               : "=&v"(t) : "v"(voff), "s"(sbase), "i"(goff), "s"(lbase), "i"(loff) : "memory", "scc");
}
__device__ __forceinline__ u32x4 gload16(const void* sbase, u32 voff, int goff) {
  u32x4 r; u32 t;
  asm volatile("v_add_u32 %1, %4, %2\n\tglobal_load_dwordx4 %0, %1, %3 sc1" : "=v"(r), "=&v"(t) : "v"(voff), "s"(sbase), "i"(goff) : "memory");
  return r;
}
__device__ __forceinline__ u32x2 gload8(const void* sbase, u32 voff, int goff) {
  u32x2 r; u32 t;
  asm volatile("v_add_u32 %1, %4, %2\n\tglobal_load_dwordx2 %0, %1, %3 sc1" : "=v"(r), "=&v"(t) : "v"(voff), "s"(sbase), "i"(goff) : "memory");
  return r;
}
__device__ __forceinline__ u32x4 gload16a(const void* sbase, u32 voff, int goff) {
  u32x4 r; u32 t;
  asm volatile("v_add_u32 %1, %4, %2\n\tglobal_load_dwordx4 %0, %1, %3 sc1" : "=a"(r), "=&v"(t) : "v"(voff), "s"(sbase), "i"(goff) : "memory");
  return r;
}
__device__ __forceinline__ u32x2 gload8a(const void* sbase, u32 voff, int goff) {
  u32x2 r; u32 t;
  asm volatile("v_add_u32 %1, %4, %2\n\tglobal_load_dwordx2 %0, %1, %3 sc1" : "=a"(r), "=&v"(t) : "v"(voff), "s"(sbase), "i"(goff) : "memory");
  return r;
}
template <bool HALF>
__device__ __forceinline__ void glds16s(const void* sbase, u32 voff, u32 soff, u32 lbase, int loff) {
  u32 t;
  if (HALF) asm volatile("s_add_i32 m0, %4, %5\n\tv_add_u32 %0, %3, %1\n\ts_mov_b32 exec_hi, 0\n\tglobal_load_lds_dwordx4 %0, %2 sc1\n\ts_mov_b32 exec_hi, -1"
               : "=&v"(t) : "v"(voff), "s"(sbase), "s"(soff), "s"(lbase), "i"(loff) : "memory", "scc");
  else asm volatile("s_add_i32 m0, %4, %5\n\tv_add_u32 %0, %3, %1\n\tglobal_load_lds_dwordx4 %0, %2 sc1"
               : "=&v"(t) : "v"(voff), "s"(sbase), "s"(soff), "s"(lbase), "i"(loff) : "memory", "scc");
}
__device__ __forceinline__ u32x4 gload16as(const void* sbase, u32 voff, u32 soff, int ioff) {
  u32x4 r; u32 t;
  asm volatile("v_add_u32 %1, %4, %2\n\tglobal_load_dwordx4 %0, %1, %3 offset:%5 sc1" : "=a"(r), "=&v"(t) : "v"(voff), "s"(sbase), "s"(soff), "i"(ioff) : "memory");
  return r;
}
__device__ __forceinline__ u32x2 gload8s(const void* sbase, u32 voff, u32 soff, int ioff) {
  u32x2 r; u32 t;
  asm volatile("v_add_u32 %1, %4, %2\n\tglobal_load_dwordx2 %0, %1, %3 offset:%5 sc1" : "=v"(r), "=&v"(t) : "v"(voff), "s"(sbase), "s"(soff), "i"(ioff) : "memory");
  return r;
}
__device__ __forceinline__ void gstore16(void* sbase, u32 voff, u32x4 v) {
  asm volatile("global_store_dwordx4 %0, %1, %2 sc1\n\ts_nop 1" :: "v"(voff), "v"(v), "s"(sbase) : "memory");
}
__device__ __forceinline__ void gstore8(void* sbase, u32 voff, u32x2 v) {
  asm volatile("global_store_dwordx2 %0, %1, %2 sc1\n\ts_nop 0" :: "v"(voff), "v"(v), "s"(sbase) : "memory");
}
__device__ __forceinline__ void gstore4(void* sbase, u32 voff, u32 v) {
  asm volatile("global_store_dword %0, %1, %2 sc1\n\ts_nop 0" :: "v"(voff), "v"(v), "s"(sbase) : "memory");
}
__device__ __forceinline__ void reload16(u32x4& r, const void* sbase, u32 voff, int goff) {
  u32 t;
  asm volatile("v_add_u32 %1, %4, %2\n\tglobal_load_dwordx4 %0, %1, %3 sc1\n\ts_waitcnt vmcnt(0)" : "+v"(r), "=&v"(t) : "v"(voff), "s"(sbase), "i"(goff) : "memory");
}
__device__ __forceinline__ void reload8(u32x2& r, const void* sbase, u32 voff, int goff) {
  u32 t;
  asm volatile("v_add_u32 %1, %4, %2\n\tglobal_load_dwordx2 %0, %1, %3 sc1\n\ts_waitcnt vmcnt(0)" : "+v"(r), "=&v"(t) : "v"(voff), "s"(sbase), "i"(goff) : "memory");
}
__device__ __forceinline__ u32 mx4(u32x4 v) { return umax(umax(v[0], v[1]), umax(v[2], v[3])); }
__device__ __forceinline__ u32 mx2(u32x2 v) { return umax(v[0], v[1]); }
__device__ __forceinline__ bool unset(u32 m) { return __any(m == 0xffffffffu); }
__device__ __forceinline__ u32 san(u32 p) { return umin(p, 0xfffffffeu); }
constexpr int SPIN = 1 << 16;
__device__ __forceinline__ float qsum(float a) {
  auto r = __builtin_amdgcn_permlane16_swap(__float_as_uint(a), __float_as_uint(a), false, false);
  a = __uint_as_float(r[0]) + __uint_as_float(r[1]);
  auto r2 = __builtin_amdgcn_permlane32_swap(__float_as_uint(a), __float_as_uint(a), false, false);
  return __uint_as_float(r2[0]) + __uint_as_float(r2[1]);
}
template <class T> __device__ __forceinline__ void pin(T& x) { asm volatile("" : "+v"(x)); }
template <class T> __device__ __forceinline__ void pina(T& x) { asm volatile("" : "+a"(x)); }
#define STAMP(kid, idx)
template <int N> __device__ __forceinline__ void wait_vm() { asm volatile("s_waitcnt vmcnt(%0)" ::"i"(N) : "memory"); }
#define SCHED_FENCE() __builtin_amdgcn_sched_barrier(0)
template <class Fn, int... I> __device__ __forceinline__ void sfor_impl(std::integer_sequence<int, I...>, Fn&& f) {
  (f(std::integral_constant<int, I>{}), ...);
}
template <int N, class Fn> __device__ __forceinline__ void sfor(Fn&& f) { sfor_impl(std::make_integer_sequence<int, N>{}, f); }
template <int N> __device__ __forceinline__ void wait_vm_bar() { asm volatile("s_waitcnt vmcnt(%0)\n\ts_barrier" ::"i"(N) : "memory"); }

__device__ __forceinline__ f32x2 gelu2(f32x2 x) {
  const float c0 = -2.0f * 0.7978845608028654f * 1.4426950408889634f;
  const float c1 = c0 * 0.044715f;
  const f32x2 z = x * __builtin_elementwise_fma(x * x, f32x2{c1, c1}, f32x2{c0, c0});
  const f32x2 d = f32x2{__builtin_amdgcn_exp2f(z.x), __builtin_amdgcn_exp2f(z.y)} + 1.0f;
  return x * f32x2{__builtin_amdgcn_rcpf(d.x), __builtin_amdgcn_rcpf(d.y)};
}
__device__ __forceinline__ u32x4 widen2(u32x2 p0, u32x2 p1);
__device__ __forceinline__ u32x4 widen(u32x2 p) {
  auto a = __builtin_amdgcn_permlane16_swap(p.x, p.x, false, false);
  auto b = __builtin_amdgcn_permlane16_swap(p.y, p.y, false, false);
  return u32x4{(u32)a[0], (u32)b[0], (u32)a[1], (u32)b[1]};
}

__device__ __forceinline__ u32x4 widen2(u32x2 p0, u32x2 p1) {
  auto a = __builtin_amdgcn_permlane16_swap(p0.x, p1.x, false, false);
  auto b = __builtin_amdgcn_permlane16_swap(p0.y, p1.y, false, false);
  return u32x4{(u32)a[0], (u32)b[0], (u32)a[1], (u32)b[1]};
}
__device__ __forceinline__ f32x2 cmul(f32x2 a, f32x2 b) {
  f32x2 r;
  asm("v_pk_mul_f32 %0, %1, %2 op_sel_hi:[1,0]\n\t"
      "v_pk_fma_f32 %0, %1, %2, %0 op_sel:[1,1,0] op_sel_hi:[0,1,1] neg_lo:[1,0,0]" : "=&v"(r) : "v"(a), "v"(b));
  return r;
}
__device__ __forceinline__ u32 cmulh(u32 a, u32 b) {
  u32 r;
  asm("v_pk_mul_f16 %0, %1, %2 op_sel_hi:[1,0]\n\t"
      "v_pk_fma_f16 %0, %1, %2, %0 op_sel:[1,1,0] op_sel_hi:[0,1,1] neg_lo:[1,0,0]" : "=&v"(r) : "v"(a), "v"(b));
  return r;
}
template <int CTRL> __device__ __forceinline__ u32 dpp(u32 v) { return (u32)__builtin_amdgcn_update_dpp(0, (int)v, CTRL, 0xf, 0xf, true); }
__device__ __forceinline__ float rowsum16(float v) {
  v += __uint_as_float(dpp<0xB1>(__float_as_uint(v)));
  v += __uint_as_float(dpp<0x4E>(__float_as_uint(v)));
  v += __uint_as_float(dpp<0x141>(__float_as_uint(v)));
  v += __uint_as_float(dpp<0x140>(__float_as_uint(v)));
  return v;
}
__device__ __forceinline__ u32 wavemax(u32 v) {
  v = umax(v, dpp<0xB1>(v)); v = umax(v, dpp<0x4E>(v)); v = umax(v, dpp<0x141>(v)); v = umax(v, dpp<0x140>(v));
  auto r = __builtin_amdgcn_permlane16_swap(v, v, false, false);
  v = umax((u32)r[0], (u32)r[1]);
  auto r2 = __builtin_amdgcn_permlane32_swap(v, v, false, false);
  return umax((u32)r2[0], (u32)r2[1]);
}
__device__ __forceinline__ f32x2 cexpp(float rho, float th, float p) {
  float mag = __builtin_amdgcn_exp2f(rho * p);
  float ph = __builtin_amdgcn_fractf(th * p);
  return f32x2{mag * __builtin_amdgcn_cosf(ph), mag * __builtin_amdgcn_sinf(ph)};
}

constexpr int CV_KC = 4160;
constexpr int CV_U = 4608;
constexpr int CV_CH = 4 * CV_KC + CV_U + 32;
constexpr int K2_LDS = 160 * 1024;
constexpr int KB = 1024;

__device__ __forceinline__ u32x2 dsr64(u32 a, int off) {
  u32x2 r;
  asm volatile("ds_read_b64 %0, %1 offset:%2" : "=v"(r) : "v"(a), "i"(off));
  return r;
}
__device__ __forceinline__ u32x4 dsr128(u32 a, int off) {
  u32x4 r;
  asm volatile("ds_read_b128 %0, %1 offset:%2" : "=a"(r) : "v"(a), "i"(off));
  return r;
}

constexpr int wpos(int m, int ch) {
  return m == 1 ? (ch < 3 ? (100 + 20 * ch) * KB : 20 * (ch - 3) * KB)
       : m == 2 ? (40 + 20 * ch) * KB
       : m == 3 ? 28 * ch * KB
       : ((ch + 7) & 7) * 20 * KB;
}

struct Ctx {
  char* ws; u16* out;
  u32 sb, vo;
  int lane, w, xcd, bi;
  u32 tlo, thi;
  const char* Wg[5];
  u32 wo[3];

  float m, r;
  u32x2 hv[3], xv[3];
};

template <int M> __device__ __forceinline__ void pf_piece(const Ctx& c, int ch, int i) {
  constexpr int CHB = (M == 3 ? 28 : 20) * KB, NW = M == 3 ? 4 : 3;
  const u32 wl = c.sb + c.w * (CHB / 8);
  const u32 v = M == 1 ? c.wo[i] : c.vo;
  const int go = M == 1 ? ch * 128 * D * 2 : ch * CHB + i * 1024;
  if (i == NW - 1) glds16_half<(M != 1)>(c.Wg[M], v, go, wl, wpos(M, ch) + i * 1024);
  else glds16<(M != 1)>(c.Wg[M], v, go, wl, wpos(M, ch) + i * 1024);
}
template <int M> __device__ __forceinline__ void pf_chunk(const Ctx& c, int ch) {
#pragma unroll
  for (int i = 0; i < (M == 3 ? 4 : 3); ++i) pf_piece<M>(c, ch, i);
}

template <int M> __device__ __forceinline__ void pf7_chunk(const Ctx& c, int ch) {
  constexpr int CHB = (M == 3 ? 28 : 20) * KB, NU = M == 3 ? 4 : 3;
  const char* base = c.Wg[M] - (M == 1 ? c.w * 16 * D * 2 : c.w * (CHB / 8));
#pragma unroll
  for (int i = 0; i < NU; ++i) {
    int u = (c.w - 1) * NU + i; u = u < CHB / 1024 ? u : CHB / 1024 - 1;
    u32 v = c.vo + u * 1024;
    if (M == 1) { const int x = u * 64 + c.lane, r = x / 10, cc = x - r * 10; v = (u32)((r ^ ((r >> 1) & 4)) * (D * 2) + cc * 16); }
    glds16<(M != 1)>(base, v, M == 1 ? ch * 128 * D * 2 : ch * CHB, c.sb + u * 1024, wpos(M, ch));
  }
}
__device__ __forceinline__ void post(const Ctx& c, int arr, int idx) {
  if (threadIdx.x == 0) gstore8(c.ws + O_FL + arr * 2048, (u32)idx * 8u, u32x2{c.tlo, c.thi});
}
__device__ __forceinline__ void publish(const Ctx& c, int arr, int idx) {
  asm volatile("s_waitcnt vmcnt(0) lgkmcnt(0)\n\ts_barrier" ::: "memory");
  post(c, arr, idx);
}
__device__ __forceinline__ void await1(const Ctx& c, int arr, int idx_of_lane) {
  if (c.w == 0) {
    for (int sp = 0; sp < SPIN; ++sp) {
      u32x2 v;
      asm volatile("global_load_dwordx2 %0, %1, %2 sc1\n\ts_waitcnt vmcnt(0)" : "=v"(v) : "v"((u32)(arr * 2048 + idx_of_lane * 8)), "s"(c.ws + O_FL) : "memory");
      if (__all(v.x == c.tlo && v.y == c.thi)) break;
      __builtin_amdgcn_s_sleep(1);
    }
  }
}
__device__ __forceinline__ void await2(const Ctx& c, int arr, int n) {
  if (c.w == 0) {
    const u32 off = (u32)(2 * c.lane < n ? c.lane : 0) * 16u;
    for (int sp = 0; sp < SPIN; ++sp) {
      u32x4 a;
      asm volatile("global_load_dwordx4 %0, %1, %2 sc1\n\ts_waitcnt vmcnt(0)" : "=v"(a) : "v"(off), "s"(c.ws + O_FL + arr * 2048) : "memory");
      if (__all(((a[0] ^ c.tlo) | (a[1] ^ c.thi) | (a[2] ^ c.tlo) | (a[3] ^ c.thi)) == 0u)) break;
      __builtin_amdgcn_s_sleep(4);
    }
  }
}
__device__ __forceinline__ void arrive(const Ctx& c, int cnt) {
  asm volatile("s_waitcnt vmcnt(0) lgkmcnt(0)\n\ts_barrier" ::: "memory");
  if (threadIdx.x == 0) __hip_atomic_fetch_add((u32*)(c.ws + O_CNT) + cnt * 64, 1u, __ATOMIC_RELAXED, __HIP_MEMORY_SCOPE_AGENT);
}
__device__ __forceinline__ void await_cnt(const Ctx& c, int cnt_of_lane, u32 want) {
  if (c.w == 0) {
    for (int sp = 0; sp < SPIN; ++sp) {
      u32 v;
      asm volatile("global_load_dword %0, %1, %2 sc1\n\ts_waitcnt vmcnt(0)" : "=v"(v) : "v"((u32)cnt_of_lane * 256u), "s"(c.ws + O_CNT) : "memory");
      if (__all(v >= want)) break;
      __builtin_amdgcn_s_sleep(2);
    }
  }
}
#define WG_BARRIER() asm volatile("s_waitcnt lgkmcnt(0)\n\ts_barrier" ::: "memory")

struct In { const u16 *x, *lar, *aim, *cre, *cim, *ldt, *dsk, *w1, *w2, *w3, *w4; };

constexpr int LN_T = 100 * KB, LN_P = 1280 + 16, LN_ST = LN_T + 32 * LN_P;
__device__ __forceinline__ void ln_compute(const Ctx& c, u32x4 (&v)[5]) {
  const int row = c.w * 4 + (c.lane >> 4), i = c.lane & 15;
  const bf16x2 ones = {(__bf16)1.0f, (__bf16)1.0f};
  float s1 = 0.f, s2 = 0.f;
#pragma unroll
  for (int s = 0; s < 5; ++s)
#pragma unroll
    for (int k = 0; k < 4; ++k) {
      s1 = __builtin_amdgcn_fdot2_f32_bf16(as_bf2(v[s][k]), ones, s1, false);
      s2 = __builtin_amdgcn_fdot2_f32_bf16(as_bf2(v[s][k]), as_bf2(v[s][k]), s2, false);
    }
  const float m = rowsum16(s1) * (1.0f / D);
  const float ve = fmaxf(rowsum16(s2) * (1.0f / D) - m * m, 0.f) + 1e-5f;
  const float r = __builtin_amdgcn_rsqf(ve), mr = -m * r;
  if (i == 0) lds_st(c.sb + LN_ST + row * 8, f32x2{m, ve * r});
#pragma unroll
  for (int s = 0; s < 5; ++s) {
    u32x4 o;
#pragma unroll
    for (int k = 0; k < 4; ++k) o[k] = pkh(bflo(v[s][k]) * r + mr, bfhi(v[s][k]) * r + mr);
    lds_st(c.sb + LN_T + row * LN_P + (i + 16 * s) * 16, o);
  }
}
__device__ __forceinline__ void ln_store(const Ctx& c, int j) {
  const int lane = c.lane, gi = lane >> 4, qq = (lane & 15) >> 2, p = lane & 3;
  char* dst = c.ws + O_UT + (size_t)(252 - 4 * j) * 64;
#pragma unroll
  for (int it = 0; it < 5; ++it) {
    const int cgp = (it * 8 + c.w) * 4 + gi;
    const u32 base = c.sb + LN_T + (31 - 8 * p - qq) * LN_P + cgp * 8;
    s16x4 a = __builtin_amdgcn_ds_read_tr16_b64_v4i16((LDS_AS s16x4*)(size_t)base);
    s16x4 b = __builtin_amdgcn_ds_read_tr16_b64_v4i16((LDS_AS s16x4*)(size_t)(base - 4 * LN_P));
    u32x2 ua = __builtin_bit_cast(u32x2, a), ub = __builtin_bit_cast(u32x2, b);
    gstore16(dst, (u32)(cgp * 16384 + (lane & 15) * 16), u32x4{ua.x, ua.y, ub.x, ub.y});
  }
  if (threadIdx.x < 16) gstore16(c.ws + O_ST0 + j * 256, threadIdx.x * 16, lds_ld<u32x4>(c.sb + LN_ST + threadIdx.x * 16));
}

struct TU { const u16* W; char* Wt; int N, KS, sb2, strip, ks0, nks, wk; };
__device__ __forceinline__ TU tu_of(const In& in, char* ws, int u) {
  TU t;
  if (u < 20)      t = TU{in.w2, ws + O_W2, D, 20, 2, u >> 1, (u & 1) * 10, 10, 0};
  else if (u < 74) t = TU{in.w3, ws + O_W3, F, 20, 1, u - 20, 0, 20, 1};
  else if (u < 134) t = TU{in.w4, ws + O_W4, D, 54, 2, (u - 74) / 6, ((u - 74) % 6) * 9, 9, 0};
  else             t = TU{in.w1, nullptr, D, 20, 1, u - 134, 0, 20, 2};
  return t;
}
__device__ __forceinline__ void tu_load(const Ctx& c, const TU& t, u32 lds) {
  const int lpr2 = t.sb2 + 1;
  const char* src = (const char*)(t.W + (size_t)t.ks0 * 32 * t.N + (t.strip << (t.sb2 + 4)));
  const u32 voff = (u32)(c.lane >> lpr2) * (u32)t.N * 2u + (u32)(c.lane & ((1 << lpr2) - 1)) * 16u;
  const int ni = t.nks << t.sb2;
  const u32 step = (u32)(64 >> lpr2) * (u32)t.N * 2u;
#pragma unroll
  for (int j = 0; j < 5; ++j) {
    int i = c.w + j * 8; i = i < ni ? i : c.w;
    glds16r(src, voff, (u32)i * step, lds + i * 1024);
  }
}
__device__ __forceinline__ void tu_emit(const Ctx& c, const TU& t, u32 lds, u32 red) {
  const int lane = c.lane, w = c.w;
  const int RB = 32 << t.sb2;
  const int e = w & ((1 << t.sb2) - 1);
  const int g = lane >> 4, q = (lane & 15) >> 2, p = lane & 3;
  const int B = (t.strip << t.sb2) + e;
  int ct, f;
  if (t.wk == 1) { const int hh = B >= 54 ? 1 : 0, b2 = B - hh * 54, c8 = b2 < 48 ? b2 / 7 : 7; ct = hh * 8 + c8; f = c8 <= 6 ? b2 - c8 * 7 : b2 - 48; }
  else { ct = B / 5; f = B - ct * 5; }
  const int kstr = t.wk == 1 ? 7168 : 5120;
  char* dst = t.Wt + (size_t)(ct * t.KS + t.ks0) * kstr + f * 1024;
  const int k0 = w >> t.sb2, kst = 8 >> t.sb2;
  float acc = 0.f;
  const bf16x2 ones = {(__bf16)1.0f, (__bf16)1.0f};
  u32x4 v[5];
#pragma unroll
  for (int j = 0; j < 5; ++j) {
    int ks = k0 + j * kst; ks = ks < t.nks ? ks : k0;
    const u32 base = lds + (ks * 32 + 8 * g + q) * RB + e * 32 + p * 8;
    s16x4 a = __builtin_amdgcn_ds_read_tr16_b64_v4i16((LDS_AS s16x4*)(size_t)base);
    s16x4 b = __builtin_amdgcn_ds_read_tr16_b64_v4i16((LDS_AS s16x4*)(size_t)(base + 4 * RB));
    u32x2 ua = __builtin_bit_cast(u32x2, a), ub = __builtin_bit_cast(u32x2, b);
    v[j] = u32x4{ua.x, ua.y, ub.x, ub.y};
  }
#pragma unroll
  for (int j = 0; j < 5; ++j) {
    int ks = k0 + j * kst; ks = ks < t.nks ? ks : k0;
    if (t.wk != 2) gstore16(dst, (u32)(ks * kstr + lane * 16), v[j]);
#pragma unroll
    for (int k = 0; k < 4; ++k) acc = __builtin_amdgcn_fdot2_f32_bf16(as_bf2(v[j][k]), ones, acc, false);
  }
  if (t.wk) {
    acc = qsum(acc);
    if (lane < 16) lds_st(red + (w * 16 + lane) * 4, acc);
    WG_BARRIER();
    if (threadIdx.x < 32) {
      const u32 a = red + threadIdx.x * 4;
      gstore4(c.ws + (t.wk == 1 ? O_CS3 : O_CS1) + t.strip * 128, threadIdx.x * 4,
              __float_as_uint((lds_ld<float>(a) + lds_ld<float>(a + 128)) + (lds_ld<float>(a + 256) + lds_ld<float>(a + 384))));
    }
  }
}

constexpr int KG_SZ = 28 * KB, KG_KT = 112 * KB, TP = 528;
struct KP { u32 ldt, lar, aim, cr, ci; };
__device__ __forceinline__ u32 gload2(const void* sbase, u32 voff) {
  u32 r;
  asm volatile("global_load_ushort %0, %1, %2" : "=v"(r) : "v"(voff), "s"(sbase) : "memory");
  return r;
}
__device__ __forceinline__ u32 gload4s(const void* sbase, u32 voff) {
  u32 r;
  asm volatile("global_load_dword %0, %1, %2 sc1" : "=v"(r) : "v"(voff), "s"(sbase) : "memory");
  return r;
}
__device__ __forceinline__ u32 gload4(const void* sbase, u32 voff) {
  u32 r;
  asm volatile("global_load_dword %0, %1, %2" : "=v"(r) : "v"(voff), "s"(sbase) : "memory");
  return r;
}
__device__ __forceinline__ void kg_setA(float rho, float th, f32x2 C, u32 (&a)[21]) {
  f32x2 A[8];
  A[0] = C;
  const f32x2 E1 = cexpp(rho, th, 1.f);
  const f32x2 G1 = cexpp(rho, th, 256.f);
  const f32x2 G4 = cexpp(rho, th, 1024.f);
  const f32x2 E2 = cmul(E1, E1), E4 = cmul(E2, E2);
  A[1] = cmul(A[0], E1); A[2] = cmul(A[0], E2); A[3] = cmul(A[1], E2);
  A[4] = cmul(A[0], E4); A[5] = cmul(A[1], E4); A[6] = cmul(A[2], E4); A[7] = cmul(A[3], E4);
  const f32x2 G2 = cmul(G1, G1), G3 = cmul(G2, G1), A16 = cmul(G4, A[0]);
#pragma unroll
  for (int k = 0; k < 8; ++k) a[k] = pkh(A[k].x, A[k].y);
  const u32 g1 = pkh(G1.x, G1.y), g2 = pkh(G2.x, G2.y), g3 = pkh(G3.x, G3.y), g4 = pkh(G4.x, G4.y);
  a[16] = pkh(A16.x, A16.y);
  a[8] = cmulh(g1, a[0]); a[9] = cmulh(g1, a[2]); a[10] = cmulh(g1, a[4]); a[11] = cmulh(g1, a[6]);
  a[12] = cmulh(g2, a[0]); a[13] = cmulh(g2, a[4]); a[14] = cmulh(g3, a[0]); a[15] = cmulh(g3, a[4]);
  a[17] = cmulh(g1, a[16]); a[18] = cmulh(g2, a[16]); a[19] = cmulh(g3, a[16]); a[20] = cmulh(g4, a[16]);
}
__device__ __forceinline__ void kg_setB(float rho, float th, u32 (&b)[32]) {
  f32x2 lo[8], hi[4];
  lo[1] = cexpp(rho, -th, 8.f);
  hi[1] = cexpp(rho, -th, 64.f);
  lo[2] = cmul(lo[1], lo[1]); lo[3] = cmul(lo[2], lo[1]); lo[4] = cmul(lo[2], lo[2]);
  lo[5] = cmul(lo[4], lo[1]); lo[6] = cmul(lo[4], lo[2]); lo[7] = cmul(lo[4], lo[3]);
  hi[2] = cmul(hi[1], hi[1]); hi[3] = cmul(hi[2], hi[1]);
  b[0] = 0x00003c00u;
#pragma unroll
  for (int k = 1; k < 8; ++k) b[k] = pkh(lo[k].x, lo[k].y);
#pragma unroll
  for (int j = 1; j < 4; ++j) {
    b[8 * j] = pkh(hi[j].x, hi[j].y);
#pragma unroll
    for (int k = 1; k < 8; ++k) b[8 * j + k] = cmulh(b[8 * j], b[k]);
  }
}
__device__ __forceinline__ float kg_tables(const KP& p, u32 tab, int set, int lane) {
  const float LOG2E = 1.4426950408889634f;
  const float dt = rbf(__builtin_amdgcn_exp2f(bflo(p.ldt) * LOG2E));
  const u32 ea = pkbf(__builtin_amdgcn_exp2f(bflo(p.lar) * LOG2E), __builtin_amdgcn_exp2f(bfhi(p.lar) * LOG2E));
  const float rho0 = (-bflo(ea) * dt) * LOG2E, rho1 = (-bfhi(ea) * dt) * LOG2E;
  const float th0 = (bflo(p.aim) * dt) * 0.15915494309189535f, th1 = (bfhi(p.aim) * dt) * 0.15915494309189535f;
  const u32 ta = tab + lane * 8;
  if (set == 0) {
    const u32 mx = wavemax(umax(umax(p.cr & 0x7fffu, (p.cr >> 16) & 0x7fffu), umax(p.ci & 0x7fffu, (p.ci >> 16) & 0x7fffu)));
    u32 eb = (mx >> 7) + 1u;
    eb = eb < 2u ? 2u : (eb > 252u ? 252u : eb);
    const float sc = __uint_as_float(eb << 23), isc = __uint_as_float((254u - eb) << 23);
    u32 a0[21], a1[21];
    kg_setA(rho0, th0, f32x2{bflo(p.cr) * isc, bflo(p.ci) * isc}, a0);
    kg_setA(rho1, th1, f32x2{bfhi(p.cr) * isc, bfhi(p.ci) * isc}, a1);
#pragma unroll
    for (int k = 0; k < 21; ++k) asm volatile("ds_write_b64 %0, %1 offset:%2" :: "v"(ta), "v"(u32x2{a0[k], a1[k]}), "i"(k * TP) : "memory");
    return 2.0f * dt * sc;
  } else {
    u32 b0[32], b1[32];
    kg_setB(rho0, th0, b0);
    kg_setB(rho1, th1, b1);
#pragma unroll
    for (int k = 0; k < 32; ++k) asm volatile("ds_write_b64 %0, %1 offset:%2" :: "v"(ta), "v"(u32x2{b0[k], b1[k]}), "i"((21 + k) * TP) : "memory");
    return 0.f;
  }
}
struct KI { u32 pa, pb, o[4], ob; };
__device__ __forceinline__ KI kg_index(u32 tab, u32 ktb, int at, int lane) {
  const int r = lane & 15, g = lane >> 4;
  int ra = at * 16 + r; ra = ra > 20 ? 20 : ra;
  KI k;
  k.pa = tab + ra * TP + g * 16; k.pb = tab + (21 + r) * TP + g * 16;
  const int sbs = at == 0 ? (g < 2 ? 8 : (g == 2 ? 4 : 2)) : 1;
  const int base = at == 0 ? (g < 2 ? 4 * g : (g == 2 ? 256 : 384)) : (g == 0 ? 512 : 640 + (g > 1 ? 64 : 0));
  const int sk = at == 0 ? 1 : (g == 0 ? 32 : 64), x2 = (at == 0 && g == 3) ? 62 : 0;
  const u32 o = ktb + (base + r * sbs) * 2;
  k.o[0] = o; k.o[1] = o + sk * 2; k.o[2] = o + (2 * sk + x2) * 2; k.o[3] = o + (3 * sk + x2) * 2;
  k.ob = sbs * 32;
  pin(k.pa); pin(k.pb); pin(k.o[0]); pin(k.o[1]); pin(k.o[2]); pin(k.o[3]); pin(k.ob);
  return k;
}
__device__ __forceinline__ void kg_mfma(const KI& k) {
  f32x4 acc0 = {0.f, 0.f, 0.f, 0.f}, acc1 = acc0;
#pragma unroll
  for (int s = 0; s < 8; ++s) {
    const f16x8 a = lds_ld<f16x8>(k.pa + s * 64);
    acc0 = __builtin_amdgcn_mfma_f32_16x16x32_f16(a, lds_ld<f16x8>(k.pb + s * 64), acc0, 0, 0, 0);
    acc1 = __builtin_amdgcn_mfma_f32_16x16x32_f16(a, lds_ld<f16x8>(k.pb + 16 * TP + s * 64), acc1, 0, 0, 0);
  }
  asm volatile("s_nop 7\n\ts_nop 7" : "+a"(acc0), "+a"(acc1));
#pragma unroll
  for (int e = 0; e < 4; ++e) { lds_st(k.o[e], (f16)acc0[e]); lds_st(k.o[e] + k.ob, (f16)acc1[e]); }
}
__device__ __forceinline__ void kg_lagidx(int t, u32 kt, u32 (&ix)[11]) {
#pragma unroll
  for (int k = 0; k < 11; ++k) {
    const int lag = t * 8 + k;
    const int tt = k < 8 ? t : t + 1;
    const int s = tt < 32 ? 0 : (tt < 64 ? 1 : (tt < 128 ? 2 : 3));
    const int add = ((1 << s) >> 1) - (s > 0 ? 1 : 0);
    ix[k] = kt + (128 * s + ((lag + add + ((lag >> s) & (s > 0 ? 1 : 0))) >> s)) * 2;
    pin(ix[k]);
  }
}
__device__ __forceinline__ void kg_expand(const u32 (&ix)[11], int ko, u32 kc, int t) {
  u32 v[11];
#pragma unroll
  for (int k = 0; k < 11; ++k) v[k] = lds_ld<u16>(ix[k] + ko);
  u32 e[5], o[5];
#pragma unroll
  for (int k = 0; k < 5; ++k) { e[k] = v[2 * k] | (v[2 * k + 1] << 16); o[k] = v[2 * k + 1] | (v[2 * k + 2] << 16); }
  const u32 a = kc + 32 + t * 16;
  lds_st(a, u32x4{e[0], e[1], e[2], e[3]});
  lds_st(a + CV_KC, u32x4{o[0], o[1], o[2], o[3]});
  lds_st(a + 2 * CV_KC, u32x4{e[1], e[2], e[3], e[4]});
  lds_st(a + 3 * CV_KC, u32x4{o[1], o[2], o[3], o[4]});
  if (t == 0) {
    const u32x4 z = {0u, 0u, 0u, 0u};
#pragma unroll
    for (int cc = 0; cc < 4; ++cc) lds_st(kc + cc * CV_KC, z);
    lds_st(kc + 16, z);
    lds_st(kc + CV_KC + 16, u32x4{0u, 0u, 0u, v[0] << 16});
    lds_st(kc + 2 * CV_KC + 16, u32x4{0u, 0u, 0u, e[0]});
    lds_st(kc + 3 * CV_KC + 16, u32x4{0u, 0u, v[0] << 16, o[0]});
  }
}

__device__ __forceinline__ void conv_phase(Ctx& c, u32x2 dsv, int cg, f32x4 ksc) {
  char* ws = c.ws;
  const int lane = c.lane, w = c.w;
  const int ch = w & 3, hh = w >> 2;
  const u32 sb = c.sb;
  const u32 mya = sb + ch * CV_CH;
  const int r = lane & 15, g = lane >> 4;
  u32x4 uv0 = gload16(ws + O_UT + (size_t)cg * 16384 + w * 2048, c.vo, 0), uv1 = gload16(ws + O_UT + (size_t)cg * 16384 + w * 2048, c.vo, 1024);
  u32x4 stv[2];
#pragma unroll
  for (int i = 0; i < 2; ++i) stv[i] = gload16a(ws + O_ST0 + w * 2048, lane * 16, i * 1024);
  wait_vm<2>();
  pin(uv0); pin(uv1);
  {
    const u32 ua = sb + (lane & 3) * CV_CH + 4 * CV_KC + (w * 32 + (lane >> 2)) * 16;
    lds_st(ua, uv0); lds_st(ua + 256, uv1);
  }
  WG_BARRIER();
  STAMP(1, 2);

  const u32 pa = mya + ((r + 1) & 3) * CV_KC + (((r + 1) & ~3) + 8 * g) * 2 + hh * 256;
  const u32 pb = mya + 4 * CV_KC + (2032 - 16 * r + 8 * g) * 2 - 7 * 512 + hh * 256;
  f32x4 acc[8];
#pragma unroll
  for (int i = 0; i < 8; ++i) { acc[i] = f32x4{0.f, 0.f, 0.f, 0.f}; pina(acc[i]); }
  u32x2 Al[2][8], Ah[2][8];
  u32x4 B[2][8];
#pragma unroll
  for (int a = 0; a < 8; ++a) {
    Al[0][a] = dsr64(pa, a * 512); Ah[0][a] = dsr64(pa, a * 512 + 8);
    B[0][a] = dsr128(pb, (7 - a) * 512);
    if (a == 3) asm volatile("s_waitcnt lgkmcnt(6)" ::: "memory");
  }
#pragma unroll
  for (int rho = 0; rho < 4; ++rho) {
    const int cur = rho & 1, nxt = cur ^ 1;
    asm volatile("s_waitcnt lgkmcnt(0)" ::: "memory");
#pragma unroll
    for (int a = 0; a < 8; ++a) { pin(Al[cur][a]); pin(Ah[cur][a]); pina(B[cur][a]); }
    SCHED_FENCE();
    int cnt = 0;
#pragma unroll
    for (int s = 7; s >= 0; --s)
#pragma unroll
      for (int a = 0; a <= s; ++a) {
        const u32x4 av = {Al[cur][a].x, Al[cur][a].y, Ah[cur][a].x, Ah[cur][a].y};
        asm volatile("v_mfma_f32_16x16x32_f16 %0, %1, %2, %0" : "+a"(acc[s]) : "v"(av), "a"(B[cur][s - a]));
        if (rho < 3 && cnt < 24) {
          const int i = cnt / 3, kind = cnt % 3;
          if (kind == 0) Al[nxt][i] = dsr64(pa, i * 512 + (rho + 1) * 64);
          else if (kind == 1) Ah[nxt][i] = dsr64(pa, i * 512 + (rho + 1) * 64 + 8);
          else B[nxt][i] = dsr128(pb, (7 - i) * 512 + (rho + 1) * 64);
        }
        if (rho < 3 && cnt >= 25 && cnt < 34 && (cnt - 25) % 3 == 0) pf_piece<1>(c, rho, (cnt - 25) / 3);
        ++cnt;
        asm volatile("" : "+a"(acc[0]), "+a"(acc[1]), "+a"(acc[2]), "+a"(acc[3]), "+a"(acc[4]), "+a"(acc[5]), "+a"(acc[6]), "+a"(acc[7]));
        SCHED_FENCE();
      }
  }
  asm volatile("s_nop 7\n\ts_nop 7" : "+a"(acc[0]), "+a"(acc[1]), "+a"(acc[2]), "+a"(acc[3]), "+a"(acc[4]), "+a"(acc[5]), "+a"(acc[6]), "+a"(acc[7]));
  STAMP(1, 3);
  asm volatile("s_barrier" ::: "memory");
  {
    const u32 ex = mya + hh * 8192 + g * 256 + ((r ^ (4 * g)) * 16);
#pragma unroll
    for (int i = 0; i < 8; ++i) asm volatile("ds_write_b128 %0, %1 offset:%2" :: "v"(ex), "a"(acc[i]), "i"(i * 1024) : "memory");
  }
  WG_BARRIER();
  STAMP(1, 6);
  wait_vm<9>();
  {
    const int d0 = cg * 4;
    char* hb = ws + O_H + (d0 >> 5) * 1024 + ((d0 >> 2) & 1) * 512 + ((d0 & 31) >> 3) * 128;
    const float dk[4] = {bflo(dsv.x), bfhi(dsv.x), bflo(dsv.y), bfhi(dsv.y)};
#pragma unroll
    for (int i = 0; i < 2; ++i) {
      const int l = w * 256 + i * 128 + lane * 2;
      pina(stv[i]);
      const float m0 = __uint_as_float(stv[i][0]), sd0 = __uint_as_float(stv[i][1]);
      const float m1 = __uint_as_float(stv[i][2]), sd1 = __uint_as_float(stv[i][3]);
      float v0[4], v1[4];
      const int eg = (l >> 2) & 3;
      const u32 eo = (u32)((l >> 8) * 1024 + eg * 256 + ((((l >> 4) & 15) ^ (4 * eg)) * 16) + (l & 3) * 4);
#pragma unroll
      for (int k = 0; k < 4; ++k) {
        const f16x2 u = lds_ld<f16x2>(sb + k * CV_CH + 4 * CV_KC + (2046 - l) * 2);
        const f32x2 ya = lds_ld<f32x2>(sb + k * CV_CH + eo), yb = lds_ld<f32x2>(sb + k * CV_CH + 8192 + eo);
        v0[k] = (ya.x + yb.x) * ksc[k] + (m0 + (sd0 + dk[k]) * (float)u[1]);
        v1[k] = (ya.y + yb.y) * ksc[k] + (m1 + (sd1 + dk[k]) * (float)u[0]);
      }
      gstore16(hb, (u32)((l >> 4) * 20480 + (l & 15) * 8),
               u32x4{pkbf(v0[0], v0[1]), pkbf(v0[2], v0[3]), pkbf(v1[0], v1[1]), pkbf(v1[2], v1[3])});
    }
  }
}

template <int MODE> struct GP {
  static constexpr int NB = MODE == 3 ? 7 : 5;
  static constexpr int KS = MODE == 4 ? 54 : 20;
  static constexpr int SPC = MODE == 3 ? 4 : 2;
  static constexpr int NS = MODE == 4 ? 27 : (MODE == 3 ? 20 : 10);
  static constexpr int LPX = MODE == 1 ? 2 : 1;
  static constexpr int A0 = MODE == 1 ? 3 : 4;
  static constexpr int AH = MODE <= 2 ? 6 : 10;
  static constexpr int hi(int s) {
    int h = A0 + 2 * s; h = h < s + AH ? h : s + AH; return h < NS ? h : NS;
  }
  static constexpr int pfm(int s) {
    return MODE == 1 ? ((s == 6 || s == 7) ? 2 : 0) : MODE == 2 ? ((s == 6 || s == 7) ? 3 : 0)
         : MODE == 3 ? ((s == 6 || s == 8 || s == 10 || s == 12 || s == 14) ? 4 : 0)
         : ((s >= 4 && s <= 14 && !(s & 1)) ? 4 : 0);
  }
  static constexpr int pfc(int s) { return MODE == 1 ? s - 3 : MODE == 2 ? s - 5 : MODE == 3 ? (s - 6) / 2 : 8 + (s - 4) / 2; }
  static constexpr int npf(int s) { return pfm(s) == 0 ? 0 : (pfm(s) == 3 ? 4 : 3); }
  static constexpr int nx(int s) { return (hi(s + 1) - hi(s)) * LPX; }
  static constexpr int before(int s) { int n = A0 * LPX; for (int i = 0; i < s; ++i) n += nx(i) + npf(i); return n; }
  static constexpr int xend(int x) {
    if (x < A0) return (x + 1) * LPX;
    int s = 0; while (hi(s + 1) <= x) ++s;
    return before(s) + (x - hi(s) + 1) * LPX;
  }
  static constexpr bool bar(int s) {
    return MODE == 1 ? (s == 4 || s == 6) : MODE == 2 ? s == 6 : MODE == 3 ? (s == 8 || s == 12)
         : (s == 4 || s == 8 || s == 12 || s == 15 || s == 19 || s == 23);
  }
  static constexpr int waitn(int s) {
    int n = before(s) - xend(s);
    if (MODE == 4 && (s == 15 || s == 19 || s == 23)) {
      const int lim = before(s) - before(s - 8);
      n = n < lim ? n : lim;
    }
    return n;
  }
  static constexpr int SCR = (MODE <= 2 ? 140 : 0) * KB;
};

struct Tile { int tb, ct, kh, Bp, Bs, oA, oS, oB; bool has_s; };
template <int MODE> __device__ __forceinline__ Tile tile_of(const Ctx& cx) {
  Tile t;
  t.kh = MODE == 3 ? 0 : cx.w >> 2;
  if (MODE == 3) {
    t.ct = cx.bi & 15;
    t.tb = (cx.xcd * 2 + (cx.bi >> 4)) * 8 + cx.w;
    const int c8 = t.ct & 7;
    const int B0 = (t.ct >> 3) * 54 + (c8 <= 6 ? c8 * 7 : 48);
    const int odd = B0 & 1;
    t.Bp = B0 + odd; t.Bs = odd ? B0 : B0 + 6; t.has_s = c8 < 6;
    t.oA = odd; t.oS = odd ? 0 : 6; t.oB = 0;
  } else {
    t.ct = cx.bi & 7;
    t.tb = (cx.xcd * 4 + (cx.bi >> 3)) * 4 + (cx.w & 3);
    const int odd = t.ct & 1;
    t.oA = odd + 2 * t.kh; t.oS = odd ? 0 : 4; t.oB = odd + 2 * (t.kh ^ 1);
    t.Bp = t.ct * 5 + t.oA; t.Bs = t.ct * 5 + t.oS; t.has_s = t.kh == 0;
  }
  return t;
}
template <int MODE>
__device__ __forceinline__ void gemm_phase(Ctx& cx) {
  using P = GP<MODE>;
  constexpr int NB = P::NB, KS = P::KS, NS = P::NS;
  char* ws = cx.ws;
  const int lane = cx.lane, w = cx.w;
  const u32 sb = cx.sb, vo = cx.vo;
  const int c = lane & 15, q = lane >> 4;
  const Tile T = tile_of<MODE>(cx);
  const int tb = T.tb, ct = T.ct, kh = T.kh, Bp = T.Bp, Bs = T.Bs;
  const bool has_s = T.has_s;
  const char* Xg = ws + (MODE == 1 ? O_H : MODE == 2 ? O_G1 : MODE == 3 ? O_X1 : O_G3) + (size_t)tb * (KS * 1024) + kh * 2048;
  constexpr int NE = MODE == 3 ? 7 : 3;

  u32x4 xf[NS];
  u32x2 xl[NS], xh[NS];
  const u32 vol = vo - kh * 1024;
  u32 vsh[4];
  if (MODE == 2) {
#pragma unroll
    for (int g = 0; g < 4; ++g) {
      const int l2 = 16 + c - (g == 0 ? 1 : g == 1 ? -1 : g == 2 ? 2 : -2);
      vsh[g] = (u32)((l2 >> 4) * 20480 + q * 256 + (l2 & 15) * 16);
    }
  }
  auto issueX = [&](int s, int i) __attribute__((always_inline)) {
    const int off = ((s / P::SPC) * 4 + s % P::SPC) * 1024;
    if (MODE == 2) {
      const int g0 = (off >> 10) / 5, g1 = ((off >> 10) + 2) / 5;
      xf[s] = gload16a(Xg - 20480, (g0 == g1 || !kh) ? vsh[g0] : vsh[g1], off);
      return;
    }
    if (MODE == 1) { if (i == 0) xl[s] = gload8(Xg, lane * 8, off); else xh[s] = gload8(Xg, lane * 8, off + 512); }
    else xf[s] = gload16a(Xg, (MODE == 4 && s == NS - 1) ? vol : vo, off);
  };
#pragma unroll
  for (int s = 0; s < P::A0; ++s)
#pragma unroll
    for (int i = 0; i < P::LPX; ++i) issueX(s, i);

  f32x4 acc[NB];
#pragma unroll
  for (int f = 0; f < NB; ++f) { acc[f] = f32x4{0.f, 0.f, 0.f, 0.f}; pina(acc[f]); }
  float s1 = 0.f, s2 = 0.f;
  const bf16x2 ones = {(__bf16)1.0f, (__bf16)1.0f};
  constexpr int NT = MODE == 1 ? 2 : 1, BS = MODE == 1 ? 32 : 1024;
  u32 rb[NT][3][3];
#pragma unroll
  for (int t = 0; t < NT; ++t)
#pragma unroll
    for (int k = 0; k < 3; ++k) {
      const u32 b0 = sb + k * 65536 + kh * (2 * NB * 1024) + (MODE == 1 ? (8 * q + 4 * (t ^ (q & 1)) + (c >> 2)) * 160 + (c & 3) * 8 : vo);
      rb[t][0][k] = b0 + T.oA * BS; rb[t][1][k] = b0 + T.oS * BS; rb[t][2][k] = b0 + T.oB * BS;
      pin(rb[t][0][k]); pin(rb[t][1][k]); if (MODE != 3) pin(rb[t][2][k]);
    }
  auto rdW = [&](int s, int f) __attribute__((always_inline)) -> u32x4 {
    const int off = wpos(MODE, s / P::SPC) + (s % P::SPC) * NB * 1024 + (f < NE - 1 ? f : (f == NE - 1 ? 0 : f - NE)) * BS;
    const int kind = f < NE - 1 ? 0 : (f == NE - 1 ? 1 : 2);
    if (MODE != 1) {
      u32 base = rb[0][kind][off >> 16];
      if (MODE == 4 && s == NS - 1) base -= kh * (NB * 1024);
      return lds_ld<u32x4>(base + (off & 65535));
    }
    const s16x4 a = __builtin_amdgcn_ds_read_tr16_b64_v4i16((LDS_AS s16x4*)(size_t)(rb[0][kind][off >> 16] + (off & 65535)));
    const s16x4 b = __builtin_amdgcn_ds_read_tr16_b64_v4i16((LDS_AS s16x4*)(size_t)(rb[NT - 1][kind][off >> 16] + (off & 65535)));
    const u32x2 ua = __builtin_bit_cast(u32x2, a), ub = __builtin_bit_cast(u32x2, b);
    return u32x4{ua.x, ua.y, ub.x, ub.y};
  };
  u32x4 wf[2][NB];
#pragma unroll
  for (int f = 0; f < NB; ++f) wf[0][f] = rdW(0, f);
  u32 csw[2];
  u32x2 psv[4];

  sfor<NS>([&](auto S_) __attribute__((always_inline)) {
    constexpr int s = decltype(S_)::value;
    if constexpr (P::bar(s)) wait_vm_bar<P::waitn(s)>();
    else wait_vm<P::waitn(s)>();
    if (MODE == 1) { pin(xl[s]); pin(xh[s]); xf[s] = u32x4{xl[s].x, xl[s].y, xh[s].x, xh[s].y}; }
    else pina(xf[s]);
    SCHED_FENCE();
#pragma unroll
    for (int f = 0; f < NB; ++f) {
      if (s + 1 < NS) wf[(s + 1) & 1][f] = rdW(s + 1, f);
      if (MODE == 1) asm volatile("v_mfma_f32_16x16x32_bf16 %0, %1, %2, %0" : "+a"(acc[f]) : "v"(wf[s & 1][f]), "v"(xf[s]));
      else asm volatile("v_mfma_f32_16x16x32_bf16 %0, %1, %2, %0" : "+a"(acc[f]) : "v"(wf[s & 1][f]), "a"(xf[s]));
      if (MODE == 1 && f < 4) {
        bf16x2 v = as_bf2(xf[s][f]);
        s1 = __builtin_amdgcn_fdot2_f32_bf16(v, ones, s1, false);
        s2 = __builtin_amdgcn_fdot2_f32_bf16(v, v, s2, false);
      }
      constexpr int nx = P::nx(s);
      if (f < nx) issueX(P::hi(s) + f / P::LPX, f % P::LPX);
      else if (f - nx < P::npf(s)) pf_piece<(P::pfm(s) ? P::pfm(s) : 1)>(cx, P::pfc(s), f - nx);
      if ((MODE == 1 || MODE == 3) && s == 2 && f == 4) {
        const char* cs = ws + (MODE == 1 ? O_CS1 : O_CS3);
#pragma unroll
        for (int k = 0; k < (MODE == 3 ? 2 : 1); ++k) {
          const int sl = k * 4 + (c >> 2);
          csw[k] = gload4s(cs, (u32)((sl >= NE - 1 ? Bs : Bp + sl) * 64 + q * 16 + (c & 3) * 4));
        }
      }
      if (MODE == 3 && s == 3 && f >= 3) psv[f - 3] = gload8(ws + O_PS + tb * 2048, lane * 8, (f - 3) * 512);
      if (MODE == 1 && s == NS - 1 && f < 3) {
        const int B = f < 2 ? Bp + f : Bs;
        cx.hv[f] = gload8a(ws + O_H + (size_t)tb * 20480 + (B >> 1) * 1024 + (B & 1) * 256, (u32)((q & 1) * 512 + (q >> 1) * 128 + c * 8), 0);
      }
      SCHED_FENCE();
    }
  });
  asm volatile("s_nop 7\n\ts_nop 7" ::: "memory");
#pragma unroll
  for (int f = 0; f < NB; ++f) pina(acc[f]);
  STAMP(1 + MODE, 14);
  asm volatile("s_barrier" ::: "memory");
  if (MODE != 3) {
    auto wr = [&](u32 a, f32x4& v) __attribute__((always_inline)) { asm volatile("ds_write_b128 %0, %1" :: "v"(a), "a"(v) : "memory"); };
    const u32 base = sb + P::SCR + (w & 3) * 5120 + vo;
    const u32 mine = base + kh * 2048, theirs = base + (kh ^ 1) * 2048;
    wr(mine, acc[3]); wr(mine + 1024, acc[4]);
    if (kh) wr(mine + 2048, acc[2]);
    if (MODE == 1) {
      s1 = qsum(s1); s2 = qsum(s2);
      lds_st(sb + 28 * KB + w * 512 + lane * 8, f32x2{s1, s2});
    }
    WG_BARRIER();
    acc[0] += lds_ld<f32x4>(theirs); acc[1] += lds_ld<f32x4>(theirs + 1024); acc[2] += lds_ld<f32x4>(theirs + 2048);
    if (MODE == 1) { f32x2 o = lds_ld<f32x2>(sb + 28 * KB + (w ^ 4) * 512 + lane * 8); s1 += o.x; s2 += o.y; }
  }
  STAMP(1 + MODE, 13);
  const u32 sofs = (u32)(Bs * 512 + (q >> 1) * 256);
  const int gq = (q & 1) * 2 + (q >> 1);
  if (MODE == 1 || MODE == 3) {
    pin(csw[0]); if (MODE == 3) pin(csw[1]);
    float a = s1, bq = s2;
    if (MODE == 3) {
#pragma unroll
      for (int t = 0; t < 4; ++t) { pin(psv[t]); a += __uint_as_float(psv[t].x); bq += __uint_as_float(psv[t].y); }
      a = qsum(a); bq = qsum(bq);
    }
    const float m = a * (1.0f / D);
    const float var = fmaxf(bq * (1.0f / D) - m * m, 0.f);
    const float r = __builtin_amdgcn_rsqf(var + 1e-5f);
    float rr = r;
    if (MODE == 1) { rr = r * __builtin_amdgcn_rsqf(var * r * r + 1e-5f); cx.m = m; cx.r = r; }
    const float mr = -m * rr;
    const u32 tofs = (u32)(tb * (MODE == 1 ? 20480 : 54 * 1024) + c * 16);
    char* dst = ws + (MODE == 1 ? O_G1 : O_G3);
    auto act = [&](auto F_) __attribute__((always_inline)) -> u32x2 {
      constexpr int f = decltype(F_)::value;
      const f32x4 cs = {__uint_as_float(dpp<0x150 + (f & 3) * 4>(csw[f >> 2])), __uint_as_float(dpp<0x151 + (f & 3) * 4>(csw[f >> 2])),
                        __uint_as_float(dpp<0x152 + (f & 3) * 4>(csw[f >> 2])), __uint_as_float(dpp<0x153 + (f & 3) * 4>(csw[f >> 2]))};
      const f32x4 xx = acc[f] * rr + cs * mr;
      const f32x2 o0 = gelu2(f32x2{xx[0], xx[1]}), o1 = gelu2(f32x2{xx[2], xx[3]});
      return u32x2{pkbf(o0.x, o0.y), pkbf(o1.x, o1.y)};
    };
    sfor<(NE - 1) / 2>([&](auto J_) __attribute__((always_inline)) {
      constexpr int j = decltype(J_)::value;
      if (MODE == 3) { pf_piece<4>(cx, 5 + j, 0); pf_piece<4>(cx, 5 + j, 1); } else { pf_piece<3>(cx, 0, 0); pf_piece<3>(cx, 0, 1); }
      const u32x2 p0 = act(std::integral_constant<int, 2 * j>{});
      if (MODE == 3) pf_piece<4>(cx, 5 + j, 2); else { pf_piece<3>(cx, 0, 2); pf_piece<3>(cx, 0, 3); }
      const u32x2 p1 = act(std::integral_constant<int, 2 * j + 1>{});
      gstore16(dst, tofs + (u32)((Bp + 2 * j) * 512 + gq * 256), widen2(p0, p1));
    });
    if (has_s) { const u32x4 v = widen(act(std::integral_constant<int, NE - 1>{})); if (!(q & 1)) gstore16(dst, tofs + sofs, v); }
  } else if (MODE == 2) {
    const float m = cx.m, r = cx.r;
    pina(cx.hv[0]); pina(cx.hv[1]); pina(cx.hv[2]);
    u32 p[6];
#pragma unroll
    for (int k = 0; k < 6; ++k) {
      const u32 hv = cx.hv[k >> 1][k & 1];
      p[k] = pkbf((bflo(hv) - m) * r + acc[k >> 1][(k & 1) * 2], (bfhi(hv) - m) * r + acc[k >> 1][(k & 1) * 2 + 1]);
    }
#pragma unroll
    for (int k = 0; k < 3; ++k) cx.xv[k] = u32x2{p[2 * k], p[2 * k + 1]};
    gstore16(ws + O_X1 + (size_t)tb * 20480 + Bp * 512, (u32)(gq * 256 + c * 16), widen2(cx.xv[0], cx.xv[1]));
    if (has_s) { const u32x4 v = widen(cx.xv[2]); if (!(q & 1)) gstore16(ws + O_X1 + (size_t)tb * 20480, sofs + c * 16, v); }
    if (!has_s) { p[4] = 0u; p[5] = 0u; }
#pragma unroll
    for (int k = 0; k < 6; ++k) {
      s1 = __builtin_amdgcn_fdot2_f32_bf16(as_bf2(p[k]), ones, s1, false);
      s2 = __builtin_amdgcn_fdot2_f32_bf16(as_bf2(p[k]), as_bf2(p[k]), s2, false);
    }
    s1 = qsum(s1); s2 = qsum(s2);
    if (q == 0) gstore8(ws + O_PS + tb * 2048 + (ct * 2 + kh) * 128, c * 8, u32x2{__float_as_uint(s1), __float_as_uint(s2)});
  } else {
    u32 p[6];
#pragma unroll
    for (int k = 0; k < 6; ++k) {
      const u32 hv = cx.hv[k >> 1][k & 1], xv = cx.xv[k >> 1][k & 1];
      p[k] = pkbf(bflo(hv) + bflo(xv) + acc[k >> 1][(k & 1) * 2], bfhi(hv) + bfhi(xv) + acc[k >> 1][(k & 1) * 2 + 1]);
    }
    u16* orow = cx.out + (size_t)(tb * 16 + c) * D;
    *(u32x4*)(orow + Bp * 16 + gq * 8) = widen2(u32x2{p[0], p[1]}, u32x2{p[2], p[3]});
    if (has_s) *(u32x2*)(orow + Bs * 16 + q * 4) = u32x2{p[4], p[5]};
  }
  STAMP(1 + MODE, 15);
}

__global__ __launch_bounds__(512, 2) __attribute__((amdgpu_waves_per_eu(2, 2)))
void k_main(In in, char* __restrict__ ws, u16* __restrict__ out) {
  __shared__ __attribute__((aligned(16))) char smem[K2_LDS];
  STAMP(0, 0);
  Ctx c;
  c.ws = ws; c.out = out;
  c.lane = threadIdx.x & 63; c.w = __builtin_amdgcn_readfirstlane(threadIdx.x >> 6);
  c.sb = lds_addr(smem); c.vo = c.lane * 16;
  c.xcd = blockIdx.x & 7; c.bi = blockIdx.x >> 3;
  const int rt = c.xcd * 4 + (c.bi >> 3), ct = c.bi & 7, rt3 = c.xcd * 2 + (c.bi >> 4);
  c.Wg[1] = (const char*)(in.w1 + ct * 80 + c.w * 16 * D);
  c.Wg[2] = ws + O_W2 + (size_t)ct * (100 * KB) + c.w * 2560;
  c.Wg[3] = ws + O_W3 + (size_t)(c.bi & 15) * (140 * KB) + c.w * 3584;
  c.Wg[4] = ws + O_W4 + (size_t)ct * (270 * KB) + c.w * 2560;
#pragma unroll
  for (int i = 0; i < 3; ++i) {
    const int u = i * 64 + c.lane, r = (u / 10) & 15, cc = u % 10;
    c.wo[i] = (u32)((r ^ ((r >> 3) << 2)) * (D * 2) + cc * 16);
  }
  const bool isconv = c.bi >= 8 && c.bi < 28;
  const int cg = c.xcd * 20 + c.bi - 8;
  auto mktag = [&](u32x2& epv) __attribute__((always_inline)) {
    pin(epv);
    const unsigned long long ep = ((unsigned long long)epv.y << 32) | epv.x;
    unsigned long long t = (ep ^ (ep >> 31)) * 0x9E3779B97F4A7C15ull + 0x632BE59BD9B4E019ull;
    t ^= t >> 29;
    c.tlo = __builtin_amdgcn_readfirstlane((u32)t); c.thi = __builtin_amdgcn_readfirstlane((u32)(t >> 32));
  };
  const int tun = (c.bi < 8 ? c.bi : c.bi - 20) * 8 + c.xcd;
  if (isconv) {
    const int kch = c.w >> 1, set = c.w & 1, d = cg * 4 + kch;
    const u32 n4 = (u32)(d * NM * 2 + c.lane * 4);
    KP kp;
    kp.lar = gload4(in.lar, n4); kp.aim = gload4(in.aim, n4); kp.ldt = gload2(in.ldt, (u32)d * 2u);
    kp.cr = gload4(in.cre, n4); kp.ci = gload4(in.cim, n4);
    u32x2 dsv = gload8(in.dsk, (u32)cg * 8u, 0);
    u32x2 epv = gload8(ws + O_EP, 0u, 0);
    const u32 tab = c.sb + kch * KG_SZ;
    const KI ki = kg_index(tab, c.sb + KG_KT + kch * 2048, set, c.lane);
    u32 ix[11];
    const int gq = c.w >> 2;
    kg_lagidx(threadIdx.x & 255, c.sb + KG_KT + gq * 2048, ix);
    wait_vm<0>();
    pin(kp.lar); pin(kp.aim); pin(kp.ldt); pin(kp.cr); pin(kp.ci); pin(dsv);
    mktag(epv);
    {
      const int k = cg * 8 + c.w;
      const u16* p = k < 540 ? in.w4 : (k < 1080 ? in.w3 : in.w1);
      const int u = k < 540 ? k : (k < 1080 ? k - 540 : k - 1080);
      asm volatile("s_mov_b32 m0, %2\n\ts_nop 0\n\tglobal_load_lds_dword %0, %1" :: "v"((u32)u * 4096u + (u32)c.lane * 64u), "s"(p), "s"(c.sb + 150 * KB) : "memory");
    }
    STAMP(0, 1);
    const float ks = kg_tables(kp, tab, set, c.lane);
    if (set == 0 && c.lane == 0) lds_st(c.sb + KG_KT + 8192 + kch * 4, ks);
    WG_BARRIER();
    STAMP(0, 2);
    kg_mfma(ki);
    WG_BARRIER();
    kg_expand(ix, 0, c.sb + gq * CV_CH, threadIdx.x & 255);
    kg_expand(ix, 4096, c.sb + (2 + gq) * CV_CH, threadIdx.x & 255);
    if (c.w < 4 && c.lane < 32) lds_st(c.sb + c.w * CV_CH + 4 * CV_KC + 4096 + c.lane * 16, u32x4{0u, 0u, 0u, 0u});
    const f32x4 ksc = lds_ld<f32x4>(c.sb + KG_KT + 8192);
    STAMP(0, 3);
    await2(c, FL_LN, 64);
    asm volatile("s_barrier" ::: "memory");
    STAMP(1, 0);
    conv_phase(c, dsv, cg, ksc);
  } else {
    if (tun < 64) {
      u32x4 xv[5];
      const char* xr = (const char*)(in.x + (size_t)(tun * 32 + c.w * 4) * D);
      const u32 xo = (u32)(c.lane >> 4) * (D * 2) + (u32)(c.lane & 15) * 16u;
#pragma unroll
      for (int i = 0; i < 5; ++i) xv[i] = gload16(xr, xo, i * 256);
      u32x2 epv = gload8(ws + O_EP, 0u, 0);
      if (tun == 0 && threadIdx.x < 24) gstore4(ws + O_CNT, threadIdx.x * 256u, 0u);
      wait_vm<0>();
#pragma unroll
      for (int i = 0; i < 5; ++i) pin(xv[i]);
      mktag(epv);
      ln_compute(c, xv);
      WG_BARRIER();
      ln_store(c, tun);
      STAMP(0, 1);
      const TU t0 = tu_of(in, ws, 64 + tun), t1 = tu_of(in, ws, tun < 26 ? 128 + tun : 64 + tun);
      publish(c, FL_LN, tun);
      STAMP(0, 2);
      tu_load(c, t0, c.sb);
      if (tun < 26) tu_load(c, t1, c.sb + 40 * KB);
      asm volatile("s_waitcnt vmcnt(0)\n\ts_barrier" ::: "memory");
      tu_emit(c, t0, c.sb, c.sb + 98 * KB);
      if (tun < 26) tu_emit(c, t1, c.sb + 40 * KB, c.sb + 99 * KB);
    } else {
      u32x2 epv = gload8(ws + O_EP, 0u, 0);
      const TU t0 = tu_of(in, ws, tun - 64), t1 = tu_of(in, ws, tun - 32);
      tu_load(c, t0, c.sb); tu_load(c, t1, c.sb + 40 * KB);
      asm volatile("s_waitcnt vmcnt(5)\n\ts_barrier" ::: "memory");
      mktag(epv);
      if (tun >= 94) {
        char* z = ws + (tun == 94 ? O_G1 - 20480 : O_G1 + (size_t)L * D * 2);
        for (int i = threadIdx.x; i < 1280; i += 512) gstore16(z, i * 16u, u32x4{0u, 0u, 0u, 0u});
      }
      tu_emit(c, t0, c.sb, c.sb + 98 * KB);
      asm volatile("s_waitcnt vmcnt(0)\n\ts_barrier" ::: "memory");
      tu_emit(c, t1, c.sb + 40 * KB, c.sb + 99 * KB);
    }
    STAMP(0, 3);
    await1(c, FL_LN, 0);
    arrive(c, 16 + c.xcd);
#pragma unroll
    for (int k = 0; k < 3; ++k) pf_chunk<1>(c, k);
    asm volatile("s_waitcnt vmcnt(0)" ::: "memory");
  }
  STAMP(1, 15);
  if (isconv) arrive(c, blockIdx.x & 15);
  if (c.w) { pf7_chunk<1>(c, 3); pf7_chunk<1>(c, 4); }
  await_cnt(c, 16 + (c.lane & 7), 12u);
  asm volatile("s_barrier" ::: "memory");
  if (c.w) { pf7_chunk<2>(c, 0); pf7_chunk<2>(c, 1); pf7_chunk<2>(c, 2); }
  await_cnt(c, c.lane & 15, 10u);
  if (blockIdx.x == 0 && threadIdx.x == 0) gstore8(ws + O_EP, 0u, u32x2{c.tlo, c.thi});
  asm volatile("s_barrier" ::: "memory");
  STAMP(2, 0);
  gemm_phase<1>(c);
  publish(c, FL_1, rt * 8 + ct);
  await1(c, FL_1, ((rt * 8 - 8 + (c.lane < 24 ? c.lane : 0)) & 255));
  asm volatile("s_barrier" ::: "memory");
  STAMP(3, 0);
  gemm_phase<2>(c);
  publish(c, FL_2, rt * 8 + ct);
  if (c.w) { pf7_chunk<3>(c, 3); pf7_chunk<3>(c, 4); }
  await1(c, FL_2, rt3 * 16 + (c.lane & 15));
  asm volatile("s_barrier" ::: "memory");
  STAMP(4, 0);
  gemm_phase<3>(c);
  publish(c, FL_3, rt3 * 16 + (c.bi & 15));
  await1(c, FL_3, (rt >> 1) * 16 + (c.lane & 15));
  asm volatile("s_barrier" ::: "memory");
  STAMP(5, 0);
  gemm_phase<4>(c);
}

}

extern "C" void kernel_launch(void* const* d_in, const int* in_sizes, int n_in, void* d_out, int out_size, void* d_ws,
                              size_t ws_size, hipStream_t stream) {
  In in;
  in.x = (const u16*)d_in[0]; in.lar = (const u16*)d_in[1]; in.aim = (const u16*)d_in[2]; in.cre = (const u16*)d_in[3];
  in.cim = (const u16*)d_in[4]; in.ldt = (const u16*)d_in[5]; in.dsk = (const u16*)d_in[6];
  in.w1 = (const u16*)d_in[8]; in.w2 = (const u16*)d_in[9]; in.w3 = (const u16*)d_in[10]; in.w4 = (const u16*)d_in[11];
  k_main<<<256, 512, 0, stream>>>(in, (char*)d_ws, (u16*)d_out);
}
```

```cpp
#include <hip/hip_runtime.h>
#include <hip/hip_bf16.h>
#include <stdint.h>
#include <utility>

typedef unsigned short u16;
typedef unsigned u32;
typedef _Float16 f16;
typedef __attribute__((ext_vector_type(2))) _Float16 f16x2;
typedef __attribute__((ext_vector_type(8))) _Float16 f16x8;
typedef __attribute__((ext_vector_type(2))) __bf16 bf16x2;
typedef __attribute__((ext_vector_type(8))) __bf16 bf16x8;
typedef __attribute__((ext_vector_type(2))) float f32x2;
typedef __attribute__((ext_vector_type(4))) float f32x4;
typedef __attribute__((ext_vector_type(2))) unsigned u32x2;
typedef __attribute__((ext_vector_type(4))) unsigned u32x4;
typedef __attribute__((ext_vector_type(4))) short s16x4;
#define LDS_AS __attribute__((address_space(3)))

namespace {

constexpr int L = 2048, D = 640, NM = 128, F = 1728;
constexpr size_t O_UT  = 0;
constexpr size_t O_H   = O_UT  + (size_t)D * L * 2;
constexpr size_t O_G1  = O_H   + (size_t)L * D * 2 + 20480;
constexpr size_t O_X1  = O_G1  + (size_t)L * D * 2 + 20480;
constexpr size_t O_G3  = O_X1  + (size_t)L * D * 2;
constexpr size_t O_W2  = O_G3  + (size_t)L * F * 2;
constexpr size_t O_W3  = O_W2  + (size_t)D * D * 2;
constexpr size_t O_W4  = O_W3  + (size_t)16 * 20 * 7 * 1024;
constexpr size_t O_CS1 = O_W4  + (size_t)D * F * 2 + 16384;
constexpr size_t O_CS3 = O_CS1 + D * 4;
constexpr size_t O_ST0 = O_CS3 + 1792 * 4;
constexpr size_t O_PS  = O_ST0 + L * 8;
constexpr size_t O_FL  = O_PS + 128 * 2048;
constexpr size_t O_EP  = O_FL + 6 * 2048;
constexpr size_t O_CNT = O_EP + 256;
constexpr int FL_LN = 0, FL_W = 1, FL_C = 2, FL_1 = 3, FL_2 = 4, FL_3 = 5;

__device__ __forceinline__ float bf2f(u16 v) { return __uint_as_float(((u32)v) << 16); }
__device__ __forceinline__ float bflo(u32 v) { return __uint_as_float(v << 16); }
__device__ __forceinline__ float bfhi(u32 v) { return __uint_as_float(v & 0xffff0000u); }
__device__ __forceinline__ u32 pkbf(float a, float b) {
  f32x2 v = {a, b};
  return __builtin_bit_cast(u32, __builtin_convertvector(v, bf16x2));
}
__device__ __forceinline__ float rbf(float f) { return bflo(pkbf(f, 0.f)); }
__device__ __forceinline__ u32 pkh(float a, float b) {
  f32x2 v = {a, b};
  return __builtin_bit_cast(u32, __builtin_convertvector(v, f16x2));
}
__device__ __forceinline__ u32 umin(u32 a, u32 b) { return a < b ? a : b; }
__device__ __forceinline__ u32 umax(u32 a, u32 b) { return a > b ? a : b; }
__device__ __forceinline__ bf16x2 as_bf2(u32 v) { return __builtin_bit_cast(bf16x2, v); }
__device__ __forceinline__ f16x2 as_h2(u32 v) { return __builtin_bit_cast(f16x2, v); }
__device__ __forceinline__ u32 lds_addr(const void* p) { return (u32)(size_t)(LDS_AS const void*)p; }
template <class T> __device__ __forceinline__ T lds_ld(u32 a) { return *(LDS_AS const T*)(size_t)a; }
template <class T> __device__ __forceinline__ void lds_st(u32 a, T v) { *(LDS_AS T*)(size_t)a = v; }

template <bool SC1>
__device__ __forceinline__ void glds16(const void* sbase, u32 voff, int goff, u32 lbase, int loff) {
  u32 t;
  if (SC1) asm volatile("s_add_i32 m0, %4, %5\n\tv_add_u32 %0, %3, %1\n\tglobal_load_lds_dwordx4 %0, %2 sc1"
               : "=&v"(t) : "v"(voff), "s"(sbase), "i"(goff), "s"(lbase), "i"(loff) : "memory", "scc");
  else asm volatile("s_add_i32 m0, %4, %5\n\tv_add_u32 %0, %3, %1\n\tglobal_load_lds_dwordx4 %0, %2"
               : "=&v"(t) : "v"(voff), "s"(sbase), "i"(goff), "s"(lbase), "i"(loff) : "memory", "scc");
}
__device__ __forceinline__ void glds16r(const void* sbase, u32 voff, u32 goff, u32 laddr) {
  u32 t;
  asm volatile("s_nop 4\n\ts_mov_b32 m0, %4\n\tv_add_u32 %0, %3, %1\n\tglobal_load_lds_dwordx4 %0, %2"
               : "=&v"(t) : "v"(voff), "s"(sbase), "s"(goff), "s"(laddr) : "memory");
}
template <bool SC1>
__device__ __forceinline__ void glds16_half(const void* sbase, u32 voff, int goff, u32 lbase, int loff) {
  u32 t;
  if (SC1) asm volatile("s_add_i32 m0, %4, %5\n\tv_add_u32 %0, %3, %1\n\ts_mov_b32 exec_hi, 0\n\tglobal_load_lds_dwordx4 %0, %2 sc1\n\ts_mov_b32 exec_hi, -1"
               : "=&v"(t) : "v"(voff), "s"(sbase), "i"(goff), "s"(lbase), "i"(loff) : "memory", "scc");
  else asm volatile("s_add_i32 m0, %4, %5\n\tv_add_u32 %0, %3, %1\n\ts_mov_b32 exec_hi, 0\n\tglobal_load_lds_dwordx4 %0, %2\n\ts_mov_b32 exec_hi, -1"
               : "=&v"(t) : "v"(voff), "s"(sbase), "i"(goff), "s"(lbase), "i"(loff) : "memory", "scc");
}
__device__ __forceinline__ u32x4 gload16(const void* sbase, u32 voff, int goff) {
  u32x4 r; u32 t;
  asm volatile("v_add_u32 %1, %4, %2\n\tglobal_load_dwordx4 %0, %1, %3 sc1" : "=v"(r), "=&v"(t) : "v"(voff), "s"(sbase), "i"(goff) : "memory");
  return r;
}
__device__ __forceinline__ u32x2 gload8(const void* sbase, u32 voff, int goff) {
  u32x2 r; u32 t;
  asm volatile("v_add_u32 %1, %4, %2\n\tglobal_load_dwordx2 %0, %1, %3 sc1" : "=v"(r), "=&v"(t) : "v"(voff), "s"(sbase), "i"(goff) : "memory");
  return r;
}
__device__ __forceinline__ u32x4 gload16a(const void* sbase, u32 voff, int goff) {
  u32x4 r; u32 t;
  asm volatile("v_add_u32 %1, %4, %2\n\tglobal_load_dwordx4 %0, %1, %3 sc1" : "=a"(r), "=&v"(t) : "v"(voff), "s"(sbase), "i"(goff) : "memory");
  return r;
}
__device__ __forceinline__ u32x2 gload8a(const void* sbase, u32 voff, int goff) {
  u32x2 r; u32 t;
  asm volatile("v_add_u32 %1, %4, %2\n\tglobal_load_dwordx2 %0, %1, %3 sc1" : "=a"(r), "=&v"(t) : "v"(voff), "s"(sbase), "i"(goff) : "memory");
  return r;
}
template <bool HALF>
__device__ __forceinline__ void glds16s(const void* sbase, u32 voff, u32 soff, u32 lbase, int loff) {
  u32 t;
  if (HALF) asm volatile("s_add_i32 m0, %4, %5\n\tv_add_u32 %0, %3, %1\n\ts_mov_b32 exec_hi, 0\n\tglobal_load_lds_dwordx4 %0, %2 sc1\n\ts_mov_b32 exec_hi, -1"
               : "=&v"(t) : "v"(voff), "s"(sbase), "s"(soff), "s"(lbase), "i"(loff) : "memory", "scc");
  else asm volatile("s_add_i32 m0, %4, %5\n\tv_add_u32 %0, %3, %1\n\tglobal_load_lds_dwordx4 %0, %2 sc1"
               : "=&v"(t) : "v"(voff), "s"(sbase), "s"(soff), "s"(lbase), "i"(loff) : "memory", "scc");
}
__device__ __forceinline__ u32x4 gload16as(const void* sbase, u32 voff, u32 soff, int ioff) {
  u32x4 r; u32 t;
  asm volatile("v_add_u32 %1, %4, %2\n\tglobal_load_dwordx4 %0, %1, %3 offset:%5 sc1" : "=a"(r), "=&v"(t) : "v"(voff), "s"(sbase), "s"(soff), "i"(ioff) : "memory");
  return r;
}
__device__ __forceinline__ u32x2 gload8s(const void* sbase, u32 voff, u32 soff, int ioff) {
  u32x2 r; u32 t;
  asm volatile("v_add_u32 %1, %4, %2\n\tglobal_load_dwordx2 %0, %1, %3 offset:%5 sc1" : "=v"(r), "=&v"(t) : "v"(voff), "s"(sbase), "s"(soff), "i"(ioff) : "memory");
  return r;
}
__device__ __forceinline__ void gstore16(void* sbase, u32 voff, u32x4 v) {
  asm volatile("global_store_dwordx4 %0, %1, %2 sc1\n\ts_nop 1" :: "v"(voff), "v"(v), "s"(sbase) : "memory");
}
__device__ __forceinline__ void gstore8(void* sbase, u32 voff, u32x2 v) {
  asm volatile("global_store_dwordx2 %0, %1, %2 sc1\n\ts_nop 0" :: "v"(voff), "v"(v), "s"(sbase) : "memory");
}
__device__ __forceinline__ void gstore4(void* sbase, u32 voff, u32 v) {
  asm volatile("global_store_dword %0, %1, %2 sc1\n\ts_nop 0" :: "v"(voff), "v"(v), "s"(sbase) : "memory");
}
__device__ __forceinline__ void reload16(u32x4& r, const void* sbase, u32 voff, int goff) {
  u32 t;
  asm volatile("v_add_u32 %1, %4, %2\n\tglobal_load_dwordx4 %0, %1, %3 sc1\n\ts_waitcnt vmcnt(0)" : "+v"(r), "=&v"(t) : "v"(voff), "s"(sbase), "i"(goff) : "memory");
}
__device__ __forceinline__ void reload8(u32x2& r, const void* sbase, u32 voff, int goff) {
  u32 t;
  asm volatile("v_add_u32 %1, %4, %2\n\tglobal_load_dwordx2 %0, %1, %3 sc1\n\ts_waitcnt vmcnt(0)" : "+v"(r), "=&v"(t) : "v"(voff), "s"(sbase), "i"(goff) : "memory");
}
__device__ __forceinline__ u32 mx4(u32x4 v) { return umax(umax(v[0], v[1]), umax(v[2], v[3])); }
__device__ __forceinline__ u32 mx2(u32x2 v) { return umax(v[0], v[1]); }
__device__ __forceinline__ bool unset(u32 m) { return __any(m == 0xffffffffu); }
__device__ __forceinline__ u32 san(u32 p) { return umin(p, 0xfffffffeu); }
constexpr int SPIN = 1 << 16;
__device__ __forceinline__ float qsum(float a) {
  auto r = __builtin_amdgcn_permlane16_swap(__float_as_uint(a), __float_as_uint(a), false, false);
  a = __uint_as_float(r[0]) + __uint_as_float(r[1]);
  auto r2 = __builtin_amdgcn_permlane32_swap(__float_as_uint(a), __float_as_uint(a), false, false);
  return __uint_as_float(r2[0]) + __uint_as_float(r2[1]);
}
template <class T> __device__ __forceinline__ void pin(T& x) { asm volatile("" : "+v"(x)); }
template <class T> __device__ __forceinline__ void pina(T& x) { asm volatile("" : "+a"(x)); }
#define STAMP(kid, idx)
template <int N> __device__ __forceinline__ void wait_vm() { asm volatile("s_waitcnt vmcnt(%0)" ::"i"(N) : "memory"); }
#define SCHED_FENCE() __builtin_amdgcn_sched_barrier(0)
template <class Fn, int... I> __device__ __forceinline__ void sfor_impl(std::integer_sequence<int, I...>, Fn&& f) {
  (f(std::integral_constant<int, I>{}), ...);
}
template <int N, class Fn> __device__ __forceinline__ void sfor(Fn&& f) { sfor_impl(std::make_integer_sequence<int, N>{}, f); }
template <int N> __device__ __forceinline__ void wait_vm_bar() { asm volatile("s_waitcnt vmcnt(%0)\n\ts_barrier" ::"i"(N) : "memory"); }

__device__ __forceinline__ f32x2 gelu2(f32x2 x) {
  const float c0 = -2.0f * 0.7978845608028654f * 1.4426950408889634f;
  const float c1 = c0 * 0.044715f;
  const f32x2 z = x * __builtin_elementwise_fma(x * x, f32x2{c1, c1}, f32x2{c0, c0});
  const f32x2 d = f32x2{__builtin_amdgcn_exp2f(z.x), __builtin_amdgcn_exp2f(z.y)} + 1.0f;
  return x * f32x2{__builtin_amdgcn_rcpf(d.x), __builtin_amdgcn_rcpf(d.y)};
}
__device__ __forceinline__ u32x4 widen2(u32x2 p0, u32x2 p1);
__device__ __forceinline__ u32x4 widen(u32x2 p) {
  auto a = __builtin_amdgcn_permlane16_swap(p.x, p.x, false, false);
  auto b = __builtin_amdgcn_permlane16_swap(p.y, p.y, false, false);
  return u32x4{(u32)a[0], (u32)b[0], (u32)a[1], (u32)b[1]};
}

__device__ __forceinline__ u32x4 widen2(u32x2 p0, u32x2 p1) {
  auto a = __builtin_amdgcn_permlane16_swap(p0.x, p1.x, false, false);
  auto b = __builtin_amdgcn_permlane16_swap(p0.y, p1.y, false, false);
  return u32x4{(u32)a[0], (u32)b[0], (u32)a[1], (u32)b[1]};
}
__device__ __forceinline__ f32x2 cmul(f32x2 a, f32x2 b) {
  f32x2 r;
  asm("v_pk_mul_f32 %0, %1, %2 op_sel_hi:[1,0]\n\t"
      "v_pk_fma_f32 %0, %1, %2, %0 op_sel:[1,1,0] op_sel_hi:[0,1,1] neg_lo:[1,0,0]" : "=&v"(r) : "v"(a), "v"(b));
  return r;
}
__device__ __forceinline__ u32 cmulh(u32 a, u32 b) {
  u32 r;
  asm("v_pk_mul_f16 %0, %1, %2 op_sel_hi:[1,0]\n\t"
      "v_pk_fma_f16 %0, %1, %2, %0 op_sel:[1,1,0] op_sel_hi:[0,1,1] neg_lo:[1,0,0]" : "=&v"(r) : "v"(a), "v"(b));
  return r;
}
template <int CTRL> __device__ __forceinline__ u32 dpp(u32 v) { return (u32)__builtin_amdgcn_update_dpp(0, (int)v, CTRL, 0xf, 0xf, true); }
__device__ __forceinline__ float rowsum16(float v) {
  v += __uint_as_float(dpp<0xB1>(__float_as_uint(v)));
  v += __uint_as_float(dpp<0x4E>(__float_as_uint(v)));
  v += __uint_as_float(dpp<0x141>(__float_as_uint(v)));
  v += __uint_as_float(dpp<0x140>(__float_as_uint(v)));
  return v;
}
__device__ __forceinline__ u32 wavemax(u32 v) {
  v = umax(v, dpp<0xB1>(v)); v = umax(v, dpp<0x4E>(v)); v = umax(v, dpp<0x141>(v)); v = umax(v, dpp<0x140>(v));
  auto r = __builtin_amdgcn_permlane16_swap(v, v, false, false);
  v = umax((u32)r[0], (u32)r[1]);
  auto r2 = __builtin_amdgcn_permlane32_swap(v, v, false, false);
  return umax((u32)r2[0], (u32)r2[1]);
}
__device__ __forceinline__ f32x2 cexpp(float rho, float th, float p) {
  float mag = __builtin_amdgcn_exp2f(rho * p);
  float ph = __builtin_amdgcn_fractf(th * p);
  return f32x2{mag * __builtin_amdgcn_cosf(ph), mag * __builtin_amdgcn_sinf(ph)};
}

constexpr int CV_KC = 4160;
constexpr int CV_U = 4608;
constexpr int CV_CH = 4 * CV_KC + CV_U + 32;
constexpr int K2_LDS = 160 * 1024;
constexpr int KB = 1024;

__device__ __forceinline__ u32x2 dsr64(u32 a, int off) {
  u32x2 r;
  asm volatile("ds_read_b64 %0, %1 offset:%2" : "=v"(r) : "v"(a), "i"(off));
  return r;
}
__device__ __forceinline__ u32x4 dsr128(u32 a, int off) {
  u32x4 r;
  asm volatile("ds_read_b128 %0, %1 offset:%2" : "=a"(r) : "v"(a), "i"(off));
  return r;
}

constexpr int wpos(int m, int ch) {
  return m == 1 ? (ch < 3 ? (100 + 20 * ch) * KB : 20 * (ch - 3) * KB)
       : m == 2 ? (40 + 20 * ch) * KB
       : m == 3 ? 28 * ch * KB
       : ((ch + 7) & 7) * 20 * KB;
}

struct Ctx {
  char* ws; u16* out;
  u32 sb, vo;
  int lane, w, xcd, bi;
  u32 tlo, thi;
  const char* Wg[5];
  u32 wo[3];

  float m, r;
  u32x2 hv[3], xv[3];
};

template <int M> __device__ __forceinline__ void pf_piece(const Ctx& c, int ch, int i) {
  constexpr int CHB = (M == 3 ? 28 : 20) * KB, NW = M == 3 ? 4 : 3;
  const u32 wl = c.sb + c.w * (CHB / 8);
  const u32 v = M == 1 ? c.wo[i] : c.vo;
  const int go = M == 1 ? ch * 128 * D * 2 : ch * CHB + i * 1024;
  if (i == NW - 1) glds16_half<(M != 1)>(c.Wg[M], v, go, wl, wpos(M, ch) + i * 1024);
  else glds16<(M != 1)>(c.Wg[M], v, go, wl, wpos(M, ch) + i * 1024);
}
template <int M> __device__ __forceinline__ void pf_chunk(const Ctx& c, int ch) {
#pragma unroll
  for (int i = 0; i < (M == 3 ? 4 : 3); ++i) pf_piece<M>(c, ch, i);
}

template <int M> __device__ __forceinline__ void pf7_chunk(const Ctx& c, int ch) {
  constexpr int CHB = (M == 3 ? 28 : 20) * KB, NU = M == 3 ? 4 : 3;
  const char* base = c.Wg[M] - (M == 1 ? c.w * 16 * D * 2 : c.w * (CHB / 8));
#pragma unroll
  for (int i = 0; i < NU; ++i) {
    int u = (c.w - 1) * NU + i; u = u < CHB / 1024 ? u : CHB / 1024 - 1;
    u32 v = c.vo + u * 1024;
    if (M == 1) { const int x = u * 64 + c.lane, r = x / 10, cc = x - r * 10; v = (u32)((r ^ ((r >> 1) & 4)) * (D * 2) + cc * 16); }
    glds16<(M != 1)>(base, v, M == 1 ? ch * 128 * D * 2 : ch * CHB, c.sb + u * 1024, wpos(M, ch));
  }
}
__device__ __forceinline__ void post(const Ctx& c, int arr, int idx) {
  if (threadIdx.x == 0) gstore8(c.ws + O_FL + arr * 2048, (u32)idx * 8u, u32x2{c.tlo, c.thi});
}
__device__ __forceinline__ void publish(const Ctx& c, int arr, int idx) {
  asm volatile("s_waitcnt vmcnt(0) lgkmcnt(0)\n\ts_barrier" ::: "memory");
  post(c, arr, idx);
}
__device__ __forceinline__ void await1(const Ctx& c, int arr, int idx_of_lane) {
  if (c.w == 0) {
    for (int sp = 0; sp < SPIN; ++sp) {
      u32x2 v;
      asm volatile("global_load_dwordx2 %0, %1, %2 sc1\n\ts_waitcnt vmcnt(0)" : "=v"(v) : "v"((u32)(arr * 2048 + idx_of_lane * 8)), "s"(c.ws + O_FL) : "memory");
      if (__all(v.x == c.tlo && v.y == c.thi)) break;
      __builtin_amdgcn_s_sleep(1);
    }
  }
}
__device__ __forceinline__ void await2(const Ctx& c, int arr, int n) {
  if (c.w == 0) {
    const u32 off = (u32)(2 * c.lane < n ? c.lane : 0) * 16u;
    for (int sp = 0; sp < SPIN; ++sp) {
      u32x4 a;
      asm volatile("global_load_dwordx4 %0, %1, %2 sc1\n\ts_waitcnt vmcnt(0)" : "=v"(a) : "v"(off), "s"(c.ws + O_FL + arr * 2048) : "memory");
      if (__all(((a[0] ^ c.tlo) | (a[1] ^ c.thi) | (a[2] ^ c.tlo) | (a[3] ^ c.thi)) == 0u)) break;
      __builtin_amdgcn_s_sleep(4);
    }
  }
}
__device__ __forceinline__ void arrive(const Ctx& c, int cnt) {
  asm volatile("s_waitcnt vmcnt(0) lgkmcnt(0)\n\ts_barrier" ::: "memory");
  if (threadIdx.x == 0) __hip_atomic_fetch_add((u32*)(c.ws + O_CNT) + cnt * 64, 1u, __ATOMIC_RELAXED, __HIP_MEMORY_SCOPE_AGENT);
}
__device__ __forceinline__ void await_cnt(const Ctx& c, int cnt_of_lane, u32 want) {
  if (c.w == 0) {
    for (int sp = 0; sp < SPIN; ++sp) {
      u32 v;
      asm volatile("global_load_dword %0, %1, %2 sc1\n\ts_waitcnt vmcnt(0)" : "=v"(v) : "v"((u32)cnt_of_lane * 256u), "s"(c.ws + O_CNT) : "memory");
      if (__all(v >= want)) break;
      __builtin_amdgcn_s_sleep(2);
    }
  }
}
#define WG_BARRIER() asm volatile("s_waitcnt lgkmcnt(0)\n\ts_barrier" ::: "memory")

struct In { const u16 *x, *lar, *aim, *cre, *cim, *ldt, *dsk, *w1, *w2, *w3, *w4; };

constexpr int LN_T = 100 * KB, LN_P = 1280 + 16, LN_ST = LN_T + 32 * LN_P;
__device__ __forceinline__ void ln_compute(const Ctx& c, u32x4 (&v)[5]) {
  const int row = c.w * 4 + (c.lane >> 4), i = c.lane & 15;
  const bf16x2 ones = {(__bf16)1.0f, (__bf16)1.0f};
  float s1 = 0.f, s2 = 0.f;
#pragma unroll
  for (int s = 0; s < 5; ++s)
#pragma unroll
    for (int k = 0; k < 4; ++k) {
      s1 = __builtin_amdgcn_fdot2_f32_bf16(as_bf2(v[s][k]), ones, s1, false);
      s2 = __builtin_amdgcn_fdot2_f32_bf16(as_bf2(v[s][k]), as_bf2(v[s][k]), s2, false);
    }
  const float m = rowsum16(s1) * (1.0f / D);
  const float ve = fmaxf(rowsum16(s2) * (1.0f / D) - m * m, 0.f) + 1e-5f;
  const float r = __builtin_amdgcn_rsqf(ve), mr = -m * r;
  if (i == 0) lds_st(c.sb + LN_ST + row * 8, f32x2{m, ve * r});
#pragma unroll
  for (int s = 0; s < 5; ++s) {
    u32x4 o;
#pragma unroll
    for (int k = 0; k < 4; ++k) o[k] = pkh(bflo(v[s][k]) * r + mr, bfhi(v[s][k]) * r + mr);
    lds_st(c.sb + LN_T + row * LN_P + (i + 16 * s) * 16, o);
  }
}
__device__ __forceinline__ void ln_store(const Ctx& c, int j) {
  const int lane = c.lane, gi = lane >> 4, qq = (lane & 15) >> 2, p = lane & 3;
  char* dst = c.ws + O_UT + (size_t)(252 - 4 * j) * 64;
#pragma unroll
  for (int it = 0; it < 5; ++it) {
    const int cgp = (it * 8 + c.w) * 4 + gi;
    const u32 base = c.sb + LN_T + (31 - 8 * p - qq) * LN_P + cgp * 8;
    s16x4 a = __builtin_amdgcn_ds_read_tr16_b64_v4i16((LDS_AS s16x4*)(size_t)base);
    s16x4 b = __builtin_amdgcn_ds_read_tr16_b64_v4i16((LDS_AS s16x4*)(size_t)(base - 4 * LN_P));
    u32x2 ua = __builtin_bit_cast(u32x2, a), ub = __builtin_bit_cast(u32x2, b);
    gstore16(dst, (u32)(cgp * 16384 + (lane & 15) * 16), u32x4{ua.x, ua.y, ub.x, ub.y});
  }
  if (threadIdx.x < 16) gstore16(c.ws + O_ST0 + j * 256, threadIdx.x * 16, lds_ld<u32x4>(c.sb + LN_ST + threadIdx.x * 16));
}

struct TU { const u16* W; char* Wt; int N, KS, sb2, strip, ks0, nks, wk; };
__device__ __forceinline__ TU tu_of(const In& in, char* ws, int u) {
  TU t;
  if (u < 20)      t = TU{in.w2, ws + O_W2, D, 20, 2, u >> 1, (u & 1) * 10, 10, 0};
  else if (u < 74) t = TU{in.w3, ws + O_W3, F, 20, 1, u - 20, 0, 20, 1};
  else if (u < 134) t = TU{in.w4, ws + O_W4, D, 54, 2, (u - 74) / 6, ((u - 74) % 6) * 9, 9, 0};
  else             t = TU{in.w1, nullptr, D, 20, 1, u - 134, 0, 20, 2};
  return t;
}
__device__ __forceinline__ void tu_load(const Ctx& c, const TU& t, u32 lds) {
  const int lpr2 = t.sb2 + 1;
  const char* src = (const char*)(t.W + (size_t)t.ks0 * 32 * t.N + (t.strip << (t.sb2 + 4)));
  const u32 voff = (u32)(c.lane >> lpr2) * (u32)t.N * 2u + (u32)(c.lane & ((1 << lpr2) - 1)) * 16u;
  const int ni = t.nks << t.sb2;
  const u32 step = (u32)(64 >> lpr2) * (u32)t.N * 2u;
#pragma unroll
  for (int j = 0; j < 5; ++j) {
    int i = c.w + j * 8; i = i < ni ? i : c.w;
    glds16r(src, voff, (u32)i * step, lds + i * 1024);
  }
}
__device__ __forceinline__ void tu_emit(const Ctx& c, const TU& t, u32 lds, u32 red) {
  const int lane = c.lane, w = c.w;
  const int RB = 32 << t.sb2;
  const int e = w & ((1 << t.sb2) - 1);
  const int g = lane >> 4, q = (lane & 15) >> 2, p = lane & 3;
  const int B = (t.strip << t.sb2) + e;
  int ct, f;
  if (t.wk == 1) { const int hh = B >= 54 ? 1 : 0, b2 = B - hh * 54, c8 = b2 < 48 ? b2 / 7 : 7; ct = hh * 8 + c8; f = c8 <= 6 ? b2 - c8 * 7 : b2 - 48; }
  else { ct = B / 5; f = B - ct * 5; }
  const int kstr = t.wk == 1 ? 7168 : 5120;
  char* dst = t.Wt + (size_t)(ct * t.KS + t.ks0) * kstr + f * 1024;
  const int k0 = w >> t.sb2, kst = 8 >> t.sb2;
  float acc = 0.f;
  const bf16x2 ones = {(__bf16)1.0f, (__bf16)1.0f};
  u32x4 v[5];
#pragma unroll
  for (int j = 0; j < 5; ++j) {
    int ks = k0 + j * kst; ks = ks < t.nks ? ks : k0;
    const u32 base = lds + (ks * 32 + 8 * g + q) * RB + e * 32 + p * 8;
    s16x4 a = __builtin_amdgcn_ds_read_tr16_b64_v4i16((LDS_AS s16x4*)(size_t)base);
    s16x4 b = __builtin_amdgcn_ds_read_tr16_b64_v4i16((LDS_AS s16x4*)(size_t)(base + 4 * RB));
    u32x2 ua = __builtin_bit_cast(u32x2, a), ub = __builtin_bit_cast(u32x2, b);
    v[j] = u32x4{ua.x, ua.y, ub.x, ub.y};
  }
#pragma unroll
  for (int j = 0; j < 5; ++j) {
    int ks = k0 + j * kst; ks = ks < t.nks ? ks : k0;
    if (t.wk != 2) gstore16(dst, (u32)(ks * kstr + lane * 16), v[j]);
#pragma unroll
    for (int k = 0; k < 4; ++k) acc = __builtin_amdgcn_fdot2_f32_bf16(as_bf2(v[j][k]), ones, acc, false);
  }
  if (t.wk) {
    acc = qsum(acc);
    if (lane < 16) lds_st(red + (w * 16 + lane) * 4, acc);
    WG_BARRIER();
    if (threadIdx.x < 32) {
      const u32 a = red + threadIdx.x * 4;
      gstore4(c.ws + (t.wk == 1 ? O_CS3 : O_CS1) + t.strip * 128, threadIdx.x * 4,
              __float_as_uint((lds_ld<float>(a) + lds_ld<float>(a + 128)) + (lds_ld<float>(a + 256) + lds_ld<float>(a + 384))));
    }
  }
}

constexpr int KG_SZ = 28 * KB, KG_KT = 112 * KB, TP = 528;
struct KP { u32 ldt, lar, aim, cr, ci; };
__device__ __forceinline__ u32 gload2(const void* sbase, u32 voff) {
  u32 r;
  asm volatile("global_load_ushort %0, %1, %2" : "=v"(r) : "v"(voff), "s"(sbase) : "memory");
  return r;
}
__device__ __forceinline__ u32 gload4s(const void* sbase, u32 voff) {
  u32 r;
  asm volatile("global_load_dword %0, %1, %2 sc1" : "=v"(r) : "v"(voff), "s"(sbase) : "memory");
  return r;
}
__device__ __forceinline__ u32 gload4(const void* sbase, u32 voff) {
  u32 r;
  asm volatile("global_load_dword %0, %1, %2" : "=v"(r) : "v"(voff), "s"(sbase) : "memory");
  return r;
}
__device__ __forceinline__ void kg_setA(float rho, float th, f32x2 C, u32 (&a)[21]) {
  f32x2 A[8];
  A[0] = C;
  const f32x2 E1 = cexpp(rho, th, 1.f);
  const f32x2 G1 = cexpp(rho, th, 256.f);
  const f32x2 G4 = cexpp(rho, th, 1024.f);
  const f32x2 E2 = cmul(E1, E1), E4 = cmul(E2, E2);
  A[1] = cmul(A[0], E1); A[2] = cmul(A[0], E2); A[3] = cmul(A[1], E2);
  A[4] = cmul(A[0], E4); A[5] = cmul(A[1], E4); A[6] = cmul(A[2], E4); A[7] = cmul(A[3], E4);
  const f32x2 G2 = cmul(G1, G1), G3 = cmul(G2, G1), A16 = cmul(G4, A[0]);
#pragma unroll
  for (int k = 0; k < 8; ++k) a[k] = pkh(A[k].x, A[k].y);
  const u32 g1 = pkh(G1.x, G1.y), g2 = pkh(G2.x, G2.y), g3 = pkh(G3.x, G3.y), g4 = pkh(G4.x, G4.y);
  a[16] = pkh(A16.x, A16.y);
  a[8] = cmulh(g1, a[0]); a[9] = cmulh(g1, a[2]); a[10] = cmulh(g1, a[4]); a[11] = cmulh(g1, a[6]);
  a[12] = cmulh(g2, a[0]); a[13] = cmulh(g2, a[4]); a[14] = cmulh(g3, a[0]); a[15] = cmulh(g3, a[4]);
  a[17] = cmulh(g1, a[16]); a[18] = cmulh(g2, a[16]); a[19] = cmulh(g3, a[16]); a[20] = cmulh(g4, a[16]);
}
__device__ __forceinline__ void kg_setB(float rho, float th, u32 (&b)[32]) {
  f32x2 lo[8], hi[4];
  lo[1] = cexpp(rho, -th, 8.f);
  hi[1] = cexpp(rho, -th, 64.f);
  lo[2] = cmul(lo[1], lo[1]); lo[3] = cmul(lo[2], lo[1]); lo[4] = cmul(lo[2], lo[2]);
  lo[5] = cmul(lo[4], lo[1]); lo[6] = cmul(lo[4], lo[2]); lo[7] = cmul(lo[4], lo[3]);
  hi[2] = cmul(hi[1], hi[1]); hi[3] = cmul(hi[2], hi[1]);
  b[0] = 0x00003c00u;
#pragma unroll
  for (int k = 1; k < 8; ++k) b[k] = pkh(lo[k].x, lo[k].y);
#pragma unroll
  for (int j = 1; j < 4; ++j) {
    b[8 * j] = pkh(hi[j].x, hi[j].y);
#pragma unroll
    for (int k = 1; k < 8; ++k) b[8 * j + k] = cmulh(b[8 * j], b[k]);
  }
}
__device__ __forceinline__ float kg_tables(const KP& p, u32 tab, int set, int lane) {
  const float LOG2E = 1.4426950408889634f;
  const int sh = set ? 0 : 16;
  const float dt = rbf(__builtin_amdgcn_exp2f(bflo(p.ldt) * LOG2E));
  const float rho = (-rbf(__builtin_amdgcn_exp2f(bfhi(p.lar << sh) * LOG2E)) * dt) * LOG2E;
  const float th = (bfhi(p.aim << sh) * dt) * 0.15915494309189535f;
  const u32 mx = wavemax(umax(umax(p.cr & 0x7fffu, (p.cr >> 16) & 0x7fffu), umax(p.ci & 0x7fffu, (p.ci >> 16) & 0x7fffu)));
  u32 eb = (mx >> 7) + 1u;
  eb = eb < 2u ? 2u : (eb > 252u ? 252u : eb);
  const float sc = __uint_as_float(eb << 23), isc = __uint_as_float((254u - eb) << 23);
  const u32 ta = tab + set * 256 + lane * 4;
  u32 a[21], b[32];
  kg_setA(rho, th, f32x2{bfhi(p.cr << sh) * isc, bfhi(p.ci << sh) * isc}, a);
  kg_setB(rho, th, b);
#pragma unroll
  for (int k = 0; k < 21; ++k) asm volatile("ds_write_b32 %0, %1 offset:%2" :: "v"(ta), "v"(a[k]), "i"(k * TP) : "memory");
#pragma unroll
  for (int k = 0; k < 32; ++k) asm volatile("ds_write_b32 %0, %1 offset:%2" :: "v"(ta), "v"(b[k]), "i"((21 + k) * TP) : "memory");
  return 2.0f * dt * sc;
}
struct KI { u32 pa, pb, o[4], ob; };
__device__ __forceinline__ KI kg_index(u32 tab, u32 ktb, int at, int lane) {
  const int r = lane & 15, g = lane >> 4;
  int ra = at * 16 + r; ra = ra > 20 ? 20 : ra;
  KI k;
  k.pa = tab + ra * TP + g * 16; k.pb = tab + (21 + r) * TP + g * 16;
  const int sbs = at == 0 ? (g < 2 ? 8 : (g == 2 ? 4 : 2)) : 1;
  const int base = at == 0 ? (g < 2 ? 4 * g : (g == 2 ? 256 : 384)) : (g == 0 ? 512 : 640 + (g > 1 ? 64 : 0));
  const int sk = at == 0 ? 1 : (g == 0 ? 32 : 64), x2 = (at == 0 && g == 3) ? 62 : 0;
  const u32 o = ktb + (base + r * sbs) * 2;
  k.o[0] = o; k.o[1] = o + sk * 2; k.o[2] = o + (2 * sk + x2) * 2; k.o[3] = o + (3 * sk + x2) * 2;
  k.ob = sbs * 32;
  pin(k.pa); pin(k.pb); pin(k.o[0]); pin(k.o[1]); pin(k.o[2]); pin(k.o[3]); pin(k.ob);
  return k;
}
__device__ __forceinline__ void kg_mfma(const KI& k) {
  f32x4 acc0 = {0.f, 0.f, 0.f, 0.f}, acc1 = acc0;
#pragma unroll
  for (int s = 0; s < 8; ++s) {
    const f16x8 a = lds_ld<f16x8>(k.pa + s * 64);
    acc0 = __builtin_amdgcn_mfma_f32_16x16x32_f16(a, lds_ld<f16x8>(k.pb + s * 64), acc0, 0, 0, 0);
    acc1 = __builtin_amdgcn_mfma_f32_16x16x32_f16(a, lds_ld<f16x8>(k.pb + 16 * TP + s * 64), acc1, 0, 0, 0);
  }
  asm volatile("s_nop 7\n\ts_nop 7" : "+a"(acc0), "+a"(acc1));
#pragma unroll
  for (int e = 0; e < 4; ++e) { lds_st(k.o[e], (f16)acc0[e]); lds_st(k.o[e] + k.ob, (f16)acc1[e]); }
}
__device__ __forceinline__ void kg_lagidx(int t, u32 kt, u32 (&ix)[11]) {
#pragma unroll
  for (int k = 0; k < 11; ++k) {
    const int lag = t * 8 + k;
    const int tt = k < 8 ? t : t + 1;
    const int s = tt < 32 ? 0 : (tt < 64 ? 1 : (tt < 128 ? 2 : 3));
    const int add = ((1 << s) >> 1) - (s > 0 ? 1 : 0);
    ix[k] = kt + (128 * s + ((lag + add + ((lag >> s) & (s > 0 ? 1 : 0))) >> s)) * 2;
    pin(ix[k]);
  }
}
__device__ __forceinline__ void kg_expand(const u32 (&ix)[11], int ko, u32 kc, int t) {
  u32 v[11];
#pragma unroll
  for (int k = 0; k < 11; ++k) v[k] = lds_ld<u16>(ix[k] + ko);
  u32 e[5], o[5];
#pragma unroll
  for (int k = 0; k < 5; ++k) { e[k] = v[2 * k] | (v[2 * k + 1] << 16); o[k] = v[2 * k + 1] | (v[2 * k + 2] << 16); }
  const u32 a = kc + 32 + t * 16;
  lds_st(a, u32x4{e[0], e[1], e[2], e[3]});
  lds_st(a + CV_KC, u32x4{o[0], o[1], o[2], o[3]});
  lds_st(a + 2 * CV_KC, u32x4{e[1], e[2], e[3], e[4]});
  lds_st(a + 3 * CV_KC, u32x4{o[1], o[2], o[3], o[4]});
  if (t == 0) {
    const u32x4 z = {0u, 0u, 0u, 0u};
#pragma unroll
    for (int cc = 0; cc < 4; ++cc) lds_st(kc + cc * CV_KC, z);
    lds_st(kc + 16, z);
    lds_st(kc + CV_KC + 16, u32x4{0u, 0u, 0u, v[0] << 16});
    lds_st(kc + 2 * CV_KC + 16, u32x4{0u, 0u, 0u, e[0]});
    lds_st(kc + 3 * CV_KC + 16, u32x4{0u, 0u, v[0] << 16, o[0]});
  }
}

__device__ __forceinline__ void conv_phase(Ctx& c, u32x2 dsv, int cg, f32x4 ksc) {
  char* ws = c.ws;
  const int lane = c.lane, w = c.w;
  const int ch = w & 3, hh = w >> 2;
  const u32 sb = c.sb;
  const u32 mya = sb + ch * CV_CH;
  const int r = lane & 15, g = lane >> 4;
  u32x4 uv0 = gload16(ws + O_UT + (size_t)cg * 16384 + w * 2048, c.vo, 0), uv1 = gload16(ws + O_UT + (size_t)cg * 16384 + w * 2048, c.vo, 1024);
  u32x4 stv[2];
#pragma unroll
  for (int i = 0; i < 2; ++i) stv[i] = gload16a(ws + O_ST0 + w * 2048, lane * 16, i * 1024);
  wait_vm<2>();
  pin(uv0); pin(uv1);
  {
    const u32 ua = sb + (lane & 3) * CV_CH + 4 * CV_KC + (w * 32 + (lane >> 2)) * 16;
    lds_st(ua, uv0); lds_st(ua + 256, uv1);
  }
  WG_BARRIER();
  STAMP(1, 2);

  const u32 pa = mya + ((r + 1) & 3) * CV_KC + (((r + 1) & ~3) + 8 * g) * 2 + hh * 256;
  const u32 pb = mya + 4 * CV_KC + (2032 - 16 * r + 8 * g) * 2 - 7 * 512 + hh * 256;
  f32x4 acc[8];
#pragma unroll
  for (int i = 0; i < 8; ++i) { acc[i] = f32x4{0.f, 0.f, 0.f, 0.f}; pina(acc[i]); }
  u32x2 Al[2][8], Ah[2][8];
  u32x4 B[2][8];
#pragma unroll
  for (int a = 0; a < 8; ++a) {
    Al[0][a] = dsr64(pa, a * 512); Ah[0][a] = dsr64(pa, a * 512 + 8);
    B[0][a] = dsr128(pb, (7 - a) * 512);
    if (a == 3) asm volatile("s_waitcnt lgkmcnt(6)" ::: "memory");
  }
#pragma unroll
  for (int rho = 0; rho < 4; ++rho) {
    const int cur = rho & 1, nxt = cur ^ 1;
    asm volatile("s_waitcnt lgkmcnt(0)" ::: "memory");
#pragma unroll
    for (int a = 0; a < 8; ++a) { pin(Al[cur][a]); pin(Ah[cur][a]); pina(B[cur][a]); }
    SCHED_FENCE();
    int cnt = 0;
#pragma unroll
    for (int s = 7; s >= 0; --s)
#pragma unroll
      for (int a = 0; a <= s; ++a) {
        const u32x4 av = {Al[cur][a].x, Al[cur][a].y, Ah[cur][a].x, Ah[cur][a].y};
        asm volatile("v_mfma_f32_16x16x32_f16 %0, %1, %2, %0" : "+a"(acc[s]) : "v"(av), "a"(B[cur][s - a]));
        if (rho < 3 && cnt < 24) {
          const int i = cnt / 3, kind = cnt % 3;
          if (kind == 0) Al[nxt][i] = dsr64(pa, i * 512 + (rho + 1) * 64);
          else if (kind == 1) Ah[nxt][i] = dsr64(pa, i * 512 + (rho + 1) * 64 + 8);
          else B[nxt][i] = dsr128(pb, (7 - i) * 512 + (rho + 1) * 64);
        }
        if (rho < 3 && cnt >= 25 && cnt < 34 && (cnt - 25) % 3 == 0) pf_piece<1>(c, rho, (cnt - 25) / 3);
        ++cnt;
        asm volatile("" : "+a"(acc[0]), "+a"(acc[1]), "+a"(acc[2]), "+a"(acc[3]), "+a"(acc[4]), "+a"(acc[5]), "+a"(acc[6]), "+a"(acc[7]));
        SCHED_FENCE();
      }
  }
  asm volatile("s_nop 7\n\ts_nop 7" : "+a"(acc[0]), "+a"(acc[1]), "+a"(acc[2]), "+a"(acc[3]), "+a"(acc[4]), "+a"(acc[5]), "+a"(acc[6]), "+a"(acc[7]));
  STAMP(1, 3);
  asm volatile("s_barrier" ::: "memory");
  {
    const u32 ex = mya + hh * 8192 + g * 256 + ((r ^ (4 * g)) * 16);
#pragma unroll
    for (int i = 0; i < 8; ++i) asm volatile("ds_write_b128 %0, %1 offset:%2" :: "v"(ex), "a"(acc[i]), "i"(i * 1024) : "memory");
  }
  WG_BARRIER();
  STAMP(1, 6);
  wait_vm<9>();
  {
    const int d0 = cg * 4;
    char* hb = ws + O_H + (d0 >> 5) * 1024 + ((d0 >> 2) & 1) * 512 + ((d0 & 31) >> 3) * 128;
    const float dk[4] = {bflo(dsv.x), bfhi(dsv.x), bflo(dsv.y), bfhi(dsv.y)};
#pragma unroll
    for (int i = 0; i < 2; ++i) {
      const int l = w * 256 + i * 128 + lane * 2;
      pina(stv[i]);
      const float m0 = __uint_as_float(stv[i][0]), sd0 = __uint_as_float(stv[i][1]);
      const float m1 = __uint_as_float(stv[i][2]), sd1 = __uint_as_float(stv[i][3]);
      float v0[4], v1[4];
      const int eg = (l >> 2) & 3;
      const u32 eo = (u32)((l >> 8) * 1024 + eg * 256 + ((((l >> 4) & 15) ^ (4 * eg)) * 16) + (l & 3) * 4);
#pragma unroll
      for (int k = 0; k < 4; ++k) {
        const f16x2 u = lds_ld<f16x2>(sb + k * CV_CH + 4 * CV_KC + (2046 - l) * 2);
        const f32x2 ya = lds_ld<f32x2>(sb + k * CV_CH + eo), yb = lds_ld<f32x2>(sb + k * CV_CH + 8192 + eo);
        v0[k] = (ya.x + yb.x) * ksc[k] + (m0 + (sd0 + dk[k]) * (float)u[1]);
        v1[k] = (ya.y + yb.y) * ksc[k] + (m1 + (sd1 + dk[k]) * (float)u[0]);
      }
      gstore16(hb, (u32)((l >> 4) * 20480 + (l & 15) * 8),
               u32x4{pkbf(v0[0], v0[1]), pkbf(v0[2], v0[3]), pkbf(v1[0], v1[1]), pkbf(v1[2], v1[3])});
    }
  }
}

template <int MODE> struct GP {
  static constexpr int NB = MODE == 3 ? 7 : 5;
  static constexpr int KS = MODE == 4 ? 54 : 20;
  static constexpr int SPC = MODE == 3 ? 4 : 2;
  static constexpr int NS = MODE == 4 ? 27 : (MODE == 3 ? 20 : 10);
  static constexpr int LPX = MODE == 1 ? 2 : 1;
  static constexpr int A0 = MODE == 1 ? 3 : 4;
  static constexpr int AH = MODE <= 2 ? 6 : 10;
  static constexpr int hi(int s) {
    int h = A0 + 2 * s; h = h < s + AH ? h : s + AH; return h < NS ? h : NS;
  }
  static constexpr int pfm(int s) {
    return MODE == 1 ? ((s == 6 || s == 7) ? 2 : 0) : MODE == 2 ? ((s == 6 || s == 7) ? 3 : 0)
         : MODE == 3 ? ((s == 6 || s == 8 || s == 10 || s == 12 || s == 14) ? 4 : 0)
         : ((s >= 4 && s <= 14 && !(s & 1)) ? 4 : 0);
  }
  static constexpr int pfc(int s) { return MODE == 1 ? s - 3 : MODE == 2 ? s - 5 : MODE == 3 ? (s - 6) / 2 : 8 + (s - 4) / 2; }
  static constexpr int npf(int s) { return pfm(s) == 0 ? 0 : (pfm(s) == 3 ? 4 : 3); }
  static constexpr int nx(int s) { return (hi(s + 1) - hi(s)) * LPX; }
  static constexpr int before(int s) { int n = A0 * LPX; for (int i = 0; i < s; ++i) n += nx(i) + npf(i); return n; }
  static constexpr int xend(int x) {
    if (x < A0) return (x + 1) * LPX;
    int s = 0; while (hi(s + 1) <= x) ++s;
    return before(s) + (x - hi(s) + 1) * LPX;
  }
  static constexpr bool bar(int s) {
    return MODE == 1 ? (s == 4 || s == 6) : MODE == 2 ? s == 6 : MODE == 3 ? (s == 8 || s == 12)
         : (s == 4 || s == 8 || s == 12 || s == 15 || s == 19 || s == 23);
  }
  static constexpr int waitn(int s) {
    int n = before(s) - xend(s);
    if (MODE == 4 && (s == 15 || s == 19 || s == 23)) {
      const int lim = before(s) - before(s - 8);
      n = n < lim ? n : lim;
    }
    return n;
  }
  static constexpr int SCR = (MODE <= 2 ? 140 : 0) * KB;
};

struct Tile { int tb, ct, kh, Bp, Bs, oA, oS, oB; bool has_s; };
template <int MODE> __device__ __forceinline__ Tile tile_of(const Ctx& cx) {
  Tile t;
  t.kh = MODE == 3 ? 0 : cx.w >> 2;
  if (MODE == 3) {
    t.ct = cx.bi & 15;
    t.tb = (cx.xcd * 2 + (cx.bi >> 4)) * 8 + cx.w;
    const int c8 = t.ct & 7;
    const int B0 = (t.ct >> 3) * 54 + (c8 <= 6 ? c8 * 7 : 48);
    const int odd = B0 & 1;
    t.Bp = B0 + odd; t.Bs = odd ? B0 : B0 + 6; t.has_s = c8 < 6;
    t.oA = odd; t.oS = odd ? 0 : 6; t.oB = 0;
  } else {
    t.ct = cx.bi & 7;
    t.tb = (cx.xcd * 4 + (cx.bi >> 3)) * 4 + (cx.w & 3);
    const int odd = t.ct & 1;
    t.oA = odd + 2 * t.kh; t.oS = odd ? 0 : 4; t.oB = odd + 2 * (t.kh ^ 1);
    t.Bp = t.ct * 5 + t.oA; t.Bs = t.ct * 5 + t.oS; t.has_s = t.kh == 0;
  }
  return t;
}
template <int MODE>
__device__ __forceinline__ void gemm_phase(Ctx& cx) {
  using P = GP<MODE>;
  constexpr int NB = P::NB, KS = P::KS, NS = P::NS;
  char* ws = cx.ws;
  const int lane = cx.lane, w = cx.w;
  const u32 sb = cx.sb, vo = cx.vo;
  const int c = lane & 15, q = lane >> 4;
  const Tile T = tile_of<MODE>(cx);
  const int tb = T.tb, ct = T.ct, kh = T.kh, Bp = T.Bp, Bs = T.Bs;
  const bool has_s = T.has_s;
  const char* Xg = ws + (MODE == 1 ? O_H : MODE == 2 ? O_G1 : MODE == 3 ? O_X1 : O_G3) + (size_t)tb * (KS * 1024) + kh * 2048;
  constexpr int NE = MODE == 3 ? 7 : 3;

  u32x4 xf[NS];
  u32x2 xl[NS], xh[NS];
  const u32 vol = vo - kh * 1024;
  u32 vsh[4];
  if (MODE == 2) {
#pragma unroll
    for (int g = 0; g < 4; ++g) {
      const int l2 = 16 + c - (g == 0 ? 1 : g == 1 ? -1 : g == 2 ? 2 : -2);
      vsh[g] = (u32)((l2 >> 4) * 20480 + q * 256 + (l2 & 15) * 16);
    }
  }
  auto issueX = [&](int s, int i) __attribute__((always_inline)) {
    const int off = ((s / P::SPC) * 4 + s % P::SPC) * 1024;
    if (MODE == 2) {
      const int g0 = (off >> 10) / 5, g1 = ((off >> 10) + 2) / 5;
      xf[s] = gload16a(Xg - 20480, (g0 == g1 || !kh) ? vsh[g0] : vsh[g1], off);
      return;
    }
    if (MODE == 1) { if (i == 0) xl[s] = gload8(Xg, lane * 8, off); else xh[s] = gload8(Xg, lane * 8, off + 512); }
    else xf[s] = gload16a(Xg, (MODE == 4 && s == NS - 1) ? vol : vo, off);
  };
#pragma unroll
  for (int s = 0; s < P::A0; ++s)
#pragma unroll
    for (int i = 0; i < P::LPX; ++i) issueX(s, i);

  f32x4 acc[NB];
#pragma unroll
  for (int f = 0; f < NB; ++f) { acc[f] = f32x4{0.f, 0.f, 0.f, 0.f}; pina(acc[f]); }
  float s1 = 0.f, s2 = 0.f;
  const bf16x2 ones = {(__bf16)1.0f, (__bf16)1.0f};
  constexpr int NT = MODE == 1 ? 2 : 1, BS = MODE == 1 ? 32 : 1024;
  u32 rb[NT][3][3];
#pragma unroll
  for (int t = 0; t < NT; ++t)
#pragma unroll
    for (int k = 0; k < 3; ++k) {
      const u32 b0 = sb + k * 65536 + kh * (2 * NB * 1024) + (MODE == 1 ? (8 * q + 4 * (t ^ (q & 1)) + (c >> 2)) * 160 + (c & 3) * 8 : vo);
      rb[t][0][k] = b0 + T.oA * BS; rb[t][1][k] = b0 + T.oS * BS; rb[t][2][k] = b0 + T.oB * BS;
      pin(rb[t][0][k]); pin(rb[t][1][k]); if (MODE != 3) pin(rb[t][2][k]);
    }
  auto rdW = [&](int s, int f) __attribute__((always_inline)) -> u32x4 {
    const int off = wpos(MODE, s / P::SPC) + (s % P::SPC) * NB * 1024 + (f < NE - 1 ? f : (f == NE - 1 ? 0 : f - NE)) * BS;
    const int kind = f < NE - 1 ? 0 : (f == NE - 1 ? 1 : 2);
    if (MODE != 1) {
      u32 base = rb[0][kind][off >> 16];
      if (MODE == 4 && s == NS - 1) base -= kh * (NB * 1024);
      return lds_ld<u32x4>(base + (off & 65535));
    }
    const s16x4 a = __builtin_amdgcn_ds_read_tr16_b64_v4i16((LDS_AS s16x4*)(size_t)(rb[0][kind][off >> 16] + (off & 65535)));
    const s16x4 b = __builtin_amdgcn_ds_read_tr16_b64_v4i16((LDS_AS s16x4*)(size_t)(rb[NT - 1][kind][off >> 16] + (off & 65535)));
    const u32x2 ua = __builtin_bit_cast(u32x2, a), ub = __builtin_bit_cast(u32x2, b);
    return u32x4{ua.x, ua.y, ub.x, ub.y};
  };
  u32x4 wf[2][NB];
#pragma unroll
  for (int f = 0; f < NB; ++f) wf[0][f] = rdW(0, f);
  u32 csw[2];
  u32x2 psv[4];

  sfor<NS>([&](auto S_) __attribute__((always_inline)) {
    constexpr int s = decltype(S_)::value;
    if constexpr (P::bar(s)) wait_vm_bar<P::waitn(s)>();
    else wait_vm<P::waitn(s)>();
    if (MODE == 1) { pin(xl[s]); pin(xh[s]); xf[s] = u32x4{xl[s].x, xl[s].y, xh[s].x, xh[s].y}; }
    else pina(xf[s]);
    SCHED_FENCE();
#pragma unroll
    for (int f = 0; f < NB; ++f) {
      if (s + 1 < NS) wf[(s + 1) & 1][f] = rdW(s + 1, f);
      if (MODE == 1) asm volatile("v_mfma_f32_16x16x32_bf16 %0, %1, %2, %0" : "+a"(acc[f]) : "v"(wf[s & 1][f]), "v"(xf[s]));
      else asm volatile("v_mfma_f32_16x16x32_bf16 %0, %1, %2, %0" : "+a"(acc[f]) : "v"(wf[s & 1][f]), "a"(xf[s]));
      if (MODE == 1 && f < 4) {
        bf16x2 v = as_bf2(xf[s][f]);
        s1 = __builtin_amdgcn_fdot2_f32_bf16(v, ones, s1, false);
        s2 = __builtin_amdgcn_fdot2_f32_bf16(v, v, s2, false);
      }
      constexpr int nx = P::nx(s);
      if (f < nx) issueX(P::hi(s) + f / P::LPX, f % P::LPX);
      else if (f - nx < P::npf(s)) pf_piece<(P::pfm(s) ? P::pfm(s) : 1)>(cx, P::pfc(s), f - nx);
      if ((MODE == 1 || MODE == 3) && s == 2 && f == 4) {
        const char* cs = ws + (MODE == 1 ? O_CS1 : O_CS3);
#pragma unroll
        for (int k = 0; k < (MODE == 3 ? 2 : 1); ++k) {
          const int sl = k * 4 + (c >> 2);
          csw[k] = gload4s(cs, (u32)((sl >= NE - 1 ? Bs : Bp + sl) * 64 + q * 16 + (c & 3) * 4));
        }
      }
      if (MODE == 3 && s == 3 && f >= 3) psv[f - 3] = gload8(ws + O_PS + tb * 2048, lane * 8, (f - 3) * 512);
      if (MODE == 1 && s == NS - 1 && f < 3) {
        const int B = f < 2 ? Bp + f : Bs;
        cx.hv[f] = gload8a(ws + O_H + (size_t)tb * 20480 + (B >> 1) * 1024 + (B & 1) * 256, (u32)((q & 1) * 512 + (q >> 1) * 128 + c * 8), 0);
      }
      SCHED_FENCE();
    }
  });
  asm volatile("s_nop 7\n\ts_nop 7" ::: "memory");
#pragma unroll
  for (int f = 0; f < NB; ++f) pina(acc[f]);
  STAMP(1 + MODE, 14);
  asm volatile("s_barrier" ::: "memory");
  if (MODE != 3) {
    auto wr = [&](u32 a, f32x4& v) __attribute__((always_inline)) { asm volatile("ds_write_b128 %0, %1" :: "v"(a), "a"(v) : "memory"); };
    const u32 base = sb + P::SCR + (w & 3) * 5120 + vo;
    const u32 mine = base + kh * 2048, theirs = base + (kh ^ 1) * 2048;
    wr(mine, acc[3]); wr(mine + 1024, acc[4]);
    if (kh) wr(mine + 2048, acc[2]);
    if (MODE == 1) {
      s1 = qsum(s1); s2 = qsum(s2);
      lds_st(sb + 28 * KB + w * 512 + lane * 8, f32x2{s1, s2});
    }
    WG_BARRIER();
    acc[0] += lds_ld<f32x4>(theirs); acc[1] += lds_ld<f32x4>(theirs + 1024); acc[2] += lds_ld<f32x4>(theirs + 2048);
    if (MODE == 1) { f32x2 o = lds_ld<f32x2>(sb + 28 * KB + (w ^ 4) * 512 + lane * 8); s1 += o.x; s2 += o.y; }
  }
  STAMP(1 + MODE, 13);
  const u32 sofs = (u32)(Bs * 512 + (q >> 1) * 256);
  const int gq = (q & 1) * 2 + (q >> 1);
  if (MODE == 1 || MODE == 3) {
    pin(csw[0]); if (MODE == 3) pin(csw[1]);
    float a = s1, bq = s2;
    if (MODE == 3) {
#pragma unroll
      for (int t = 0; t < 4; ++t) { pin(psv[t]); a += __uint_as_float(psv[t].x); bq += __uint_as_float(psv[t].y); }
      a = qsum(a); bq = qsum(bq);
    }
    const float m = a * (1.0f / D);
    const float var = fmaxf(bq * (1.0f / D) - m * m, 0.f);
    const float r = __builtin_amdgcn_rsqf(var + 1e-5f);
    float rr = r;
    if (MODE == 1) { rr = r * __builtin_amdgcn_rsqf(var * r * r + 1e-5f); cx.m = m; cx.r = r; }
    const float mr = -m * rr;
    const u32 tofs = (u32)(tb * (MODE == 1 ? 20480 : 54 * 1024) + c * 16);
    char* dst = ws + (MODE == 1 ? O_G1 : O_G3);
    auto act = [&](auto F_) __attribute__((always_inline)) -> u32x2 {
      constexpr int f = decltype(F_)::value;
      const f32x4 cs = {__uint_as_float(dpp<0x150 + (f & 3) * 4>(csw[f >> 2])), __uint_as_float(dpp<0x151 + (f & 3) * 4>(csw[f >> 2])),
                        __uint_as_float(dpp<0x152 + (f & 3) * 4>(csw[f >> 2])), __uint_as_float(dpp<0x153 + (f & 3) * 4>(csw[f >> 2]))};
      const f32x4 xx = acc[f] * rr + cs * mr;
      const f32x2 o0 = gelu2(f32x2{xx[0], xx[1]}), o1 = gelu2(f32x2{xx[2], xx[3]});
      return u32x2{pkbf(o0.x, o0.y), pkbf(o1.x, o1.y)};
    };
    sfor<(NE - 1) / 2>([&](auto J_) __attribute__((always_inline)) {
      constexpr int j = decltype(J_)::value;
      if (MODE == 3) { pf_piece<4>(cx, 5 + j, 0); pf_piece<4>(cx, 5 + j, 1); } else { pf_piece<3>(cx, 0, 0); pf_piece<3>(cx, 0, 1); }
      const u32x2 p0 = act(std::integral_constant<int, 2 * j>{});
      if (MODE == 3) pf_piece<4>(cx, 5 + j, 2); else { pf_piece<3>(cx, 0, 2); pf_piece<3>(cx, 0, 3); }
      const u32x2 p1 = act(std::integral_constant<int, 2 * j + 1>{});
      gstore16(dst, tofs + (u32)((Bp + 2 * j) * 512 + gq * 256), widen2(p0, p1));
    });
    if (has_s) { const u32x4 v = widen(act(std::integral_constant<int, NE - 1>{})); if (!(q & 1)) gstore16(dst, tofs + sofs, v); }
  } else if (MODE == 2) {
    const float m = cx.m, r = cx.r;
    pina(cx.hv[0]); pina(cx.hv[1]); pina(cx.hv[2]);
    u32 p[6];
#pragma unroll
    for (int k = 0; k < 6; ++k) {
      const u32 hv = cx.hv[k >> 1][k & 1];
      p[k] = pkbf((bflo(hv) - m) * r + acc[k >> 1][(k & 1) * 2], (bfhi(hv) - m) * r + acc[k >> 1][(k & 1) * 2 + 1]);
    }
#pragma unroll
    for (int k = 0; k < 3; ++k) cx.xv[k] = u32x2{p[2 * k], p[2 * k + 1]};
    gstore16(ws + O_X1 + (size_t)tb * 20480 + Bp * 512, (u32)(gq * 256 + c * 16), widen2(cx.xv[0], cx.xv[1]));
    if (has_s) { const u32x4 v = widen(cx.xv[2]); if (!(q & 1)) gstore16(ws + O_X1 + (size_t)tb * 20480, sofs + c * 16, v); }
    if (!has_s) { p[4] = 0u; p[5] = 0u; }
#pragma unroll
    for (int k = 0; k < 6; ++k) {
      s1 = __builtin_amdgcn_fdot2_f32_bf16(as_bf2(p[k]), ones, s1, false);
      s2 = __builtin_amdgcn_fdot2_f32_bf16(as_bf2(p[k]), as_bf2(p[k]), s2, false);
    }
    s1 = qsum(s1); s2 = qsum(s2);
    if (q == 0) gstore8(ws + O_PS + tb * 2048 + (ct * 2 + kh) * 128, c * 8, u32x2{__float_as_uint(s1), __float_as_uint(s2)});
  } else {
    u32 p[6];
#pragma unroll
    for (int k = 0; k < 6; ++k) {
      const u32 hv = cx.hv[k >> 1][k & 1], xv = cx.xv[k >> 1][k & 1];
      p[k] = pkbf(bflo(hv) + bflo(xv) + acc[k >> 1][(k & 1) * 2], bfhi(hv) + bfhi(xv) + acc[k >> 1][(k & 1) * 2 + 1]);
    }
    u16* orow = cx.out + (size_t)(tb * 16 + c) * D;
    *(u32x4*)(orow + Bp * 16 + gq * 8) = widen2(u32x2{p[0], p[1]}, u32x2{p[2], p[3]});
    if (has_s) *(u32x2*)(orow + Bs * 16 + q * 4) = u32x2{p[4], p[5]};
  }
  STAMP(1 + MODE, 15);
}

__global__ __launch_bounds__(512, 2) __attribute__((amdgpu_waves_per_eu(2, 2)))
void k_main(In in, char* __restrict__ ws, u16* __restrict__ out) {
  __shared__ __attribute__((aligned(16))) char smem[K2_LDS];
  STAMP(0, 0);
  Ctx c;
  c.ws = ws; c.out = out;
  c.lane = threadIdx.x & 63; c.w = __builtin_amdgcn_readfirstlane(threadIdx.x >> 6);
  c.sb = lds_addr(smem); c.vo = c.lane * 16;
  c.xcd = blockIdx.x & 7; c.bi = blockIdx.x >> 3;
  const int rt = c.xcd * 4 + (c.bi >> 3), ct = c.bi & 7, rt3 = c.xcd * 2 + (c.bi >> 4);
  c.Wg[1] = (const char*)(in.w1 + ct * 80 + c.w * 16 * D);
  c.Wg[2] = ws + O_W2 + (size_t)ct * (100 * KB) + c.w * 2560;
  c.Wg[3] = ws + O_W3 + (size_t)(c.bi & 15) * (140 * KB) + c.w * 3584;
  c.Wg[4] = ws + O_W4 + (size_t)ct * (270 * KB) + c.w * 2560;
#pragma unroll
  for (int i = 0; i < 3; ++i) {
    const int u = i * 64 + c.lane, r = (u / 10) & 15, cc = u % 10;
    c.wo[i] = (u32)((r ^ ((r >> 3) << 2)) * (D * 2) + cc * 16);
  }
  const bool isconv = c.bi >= 8 && c.bi < 28;
  const int cg = c.xcd * 20 + c.bi - 8;
  auto mktag = [&](u32x2& epv) __attribute__((always_inline)) {
    pin(epv);
    const unsigned long long ep = ((unsigned long long)epv.y << 32) | epv.x;
    unsigned long long t = (ep ^ (ep >> 31)) * 0x9E3779B97F4A7C15ull + 0x632BE59BD9B4E019ull;
    t ^= t >> 29;
    c.tlo = __builtin_amdgcn_readfirstlane((u32)t); c.thi = __builtin_amdgcn_readfirstlane((u32)(t >> 32));
  };
  const int tun = (c.bi < 8 ? c.bi : c.bi - 20) * 8 + c.xcd;
  if (isconv) {
    const int kch = c.w >> 1, set = c.w & 1, d = cg * 4 + kch;
    const u32 n4 = (u32)(d * NM * 2 + c.lane * 4);
    KP kp;
    kp.lar = gload4(in.lar, n4); kp.aim = gload4(in.aim, n4); kp.ldt = gload2(in.ldt, (u32)d * 2u);
    kp.cr = gload4(in.cre, n4); kp.ci = gload4(in.cim, n4);
    u32x2 dsv = gload8(in.dsk, (u32)cg * 8u, 0);
    u32x2 epv = gload8(ws + O_EP, 0u, 0);
    const u32 tab = c.sb + kch * KG_SZ;
    const KI ki = kg_index(tab, c.sb + KG_KT + kch * 2048, set, c.lane);
    u32 ix[11];
    const int gq = c.w >> 2;
    kg_lagidx(threadIdx.x & 255, c.sb + KG_KT + gq * 2048, ix);
    wait_vm<0>();
    pin(kp.lar); pin(kp.aim); pin(kp.ldt); pin(kp.cr); pin(kp.ci); pin(dsv);
    mktag(epv);
    {
      const int k = cg * 8 + c.w;
      const u16* p = k < 540 ? in.w4 : (k < 1080 ? in.w3 : in.w1);
      const int u = k < 540 ? k : (k < 1080 ? k - 540 : k - 1080);
      asm volatile("s_mov_b32 m0, %2\n\ts_nop 0\n\tglobal_load_lds_dword %0, %1" :: "v"((u32)u * 4096u + (u32)c.lane * 64u), "s"(p), "s"(c.sb + 150 * KB) : "memory");
    }
    STAMP(0, 1);
    const float ks = kg_tables(kp, tab, set, c.lane);
    if (set == 0 && c.lane == 0) lds_st(c.sb + KG_KT + 8192 + kch * 4, ks);
    WG_BARRIER();
    STAMP(0, 2);
    kg_mfma(ki);
    WG_BARRIER();
    kg_expand(ix, 0, c.sb + gq * CV_CH, threadIdx.x & 255);
    kg_expand(ix, 4096, c.sb + (2 + gq) * CV_CH, threadIdx.x & 255);
    if (c.w < 4 && c.lane < 32) lds_st(c.sb + c.w * CV_CH + 4 * CV_KC + 4096 + c.lane * 16, u32x4{0u, 0u, 0u, 0u});
    const f32x4 ksc = lds_ld<f32x4>(c.sb + KG_KT + 8192);
    STAMP(0, 3);
    await2(c, FL_LN, 64);
    asm volatile("s_barrier" ::: "memory");
    STAMP(1, 0);
    conv_phase(c, dsv, cg, ksc);
  } else {
    if (tun < 64) {
      u32x4 xv[5];
      const char* xr = (const char*)(in.x + (size_t)(tun * 32 + c.w * 4) * D);
      const u32 xo = (u32)(c.lane >> 4) * (D * 2) + (u32)(c.lane & 15) * 16u;
#pragma unroll
      for (int i = 0; i < 5; ++i) xv[i] = gload16(xr, xo, i * 256);
      u32x2 epv = gload8(ws + O_EP, 0u, 0);
      if (tun == 0 && threadIdx.x < 24) gstore4(ws + O_CNT, threadIdx.x * 256u, 0u);
      wait_vm<0>();
#pragma unroll
      for (int i = 0; i < 5; ++i) pin(xv[i]);
      mktag(epv);
      ln_compute(c, xv);
      WG_BARRIER();
      ln_store(c, tun);
      STAMP(0, 1);
      const TU t0 = tu_of(in, ws, 64 + tun), t1 = tu_of(in, ws, tun < 26 ? 128 + tun : 64 + tun);
      publish(c, FL_LN, tun);
      STAMP(0, 2);
      tu_load(c, t0, c.sb);
      if (tun < 26) tu_load(c, t1, c.sb + 40 * KB);
      asm volatile("s_waitcnt vmcnt(0)\n\ts_barrier" ::: "memory");
      tu_emit(c, t0, c.sb, c.sb + 98 * KB);
      if (tun < 26) tu_emit(c, t1, c.sb + 40 * KB, c.sb + 99 * KB);
    } else {
      u32x2 epv = gload8(ws + O_EP, 0u, 0);
      if (tun == 93 && c.w < 3)
        asm volatile("s_mov_b32 m0, %2\n\ts_nop 0\n\tglobal_load_lds_dword %0, %1 sc1" :: "v"((u32)(c.w * 64 + c.lane) * 128u), "s"(ws + O_FL), "s"(c.sb + 150 * KB) : "memory");
      const TU t0 = tu_of(in, ws, tun - 64), t1 = tu_of(in, ws, tun - 32);
      tu_load(c, t0, c.sb); tu_load(c, t1, c.sb + 40 * KB);
      asm volatile("s_waitcnt vmcnt(5)\n\ts_barrier" ::: "memory");
      mktag(epv);
      if (tun >= 94) {
        char* z = ws + (tun == 94 ? O_G1 - 20480 : O_G1 + (size_t)L * D * 2);
        for (int i = threadIdx.x; i < 1280; i += 512) gstore16(z, i * 16u, u32x4{0u, 0u, 0u, 0u});
      }
      tu_emit(c, t0, c.sb, c.sb + 98 * KB);
      asm volatile("s_waitcnt vmcnt(0)\n\ts_barrier" ::: "memory");
      tu_emit(c, t1, c.sb + 40 * KB, c.sb + 99 * KB);
    }
    STAMP(0, 3);
    await1(c, FL_LN, 0);
    arrive(c, 16 + c.xcd);
#pragma unroll
    for (int k = 0; k < 3; ++k) pf_chunk<1>(c, k);
    asm volatile("s_waitcnt vmcnt(0)" ::: "memory");
  }
  STAMP(1, 15);
  if (isconv) arrive(c, blockIdx.x & 15);
  if (c.w) { pf7_chunk<1>(c, 3); pf7_chunk<1>(c, 4); }
  await_cnt(c, 16 + (c.lane & 7), 12u);
  asm volatile("s_barrier" ::: "memory");
  if (c.w) { pf7_chunk<2>(c, 0); pf7_chunk<2>(c, 1); pf7_chunk<2>(c, 2); }
  await_cnt(c, c.lane & 15, 10u);
  if (blockIdx.x == 0 && threadIdx.x == 0) gstore8(ws + O_EP, 0u, u32x2{c.tlo, c.thi});
  asm volatile("s_barrier" ::: "memory");
  STAMP(2, 0);
  gemm_phase<1>(c);
  publish(c, FL_1, rt * 8 + ct);
  await1(c, FL_1, ((rt * 8 - 8 + (c.lane < 24 ? c.lane : 0)) & 255));
  asm volatile("s_barrier" ::: "memory");
  STAMP(3, 0);
  gemm_phase<2>(c);
  publish(c, FL_2, rt * 8 + ct);
  if (c.w) { pf7_chunk<3>(c, 3); pf7_chunk<3>(c, 4); }
  await1(c, FL_2, rt3 * 16 + (c.lane & 15));
  asm volatile("s_barrier" ::: "memory");
  STAMP(4, 0);
  gemm_phase<3>(c);
  publish(c, FL_3, rt3 * 16 + (c.bi & 15));
  await1(c, FL_3, (rt >> 1) * 16 + (c.lane & 15));
  asm volatile("s_barrier" ::: "memory");
  STAMP(5, 0);
  gemm_phase<4>(c);
}

}

extern "C" void kernel_launch(void* const* d_in, const int* in_sizes, int n_in, void* d_out, int out_size, void* d_ws,
                              size_t ws_size, hipStream_t stream) {
  In in;
  in.x = (const u16*)d_in[0]; in.lar = (const u16*)d_in[1]; in.aim = (const u16*)d_in[2]; in.cre = (const u16*)d_in[3];
  in.cim = (const u16*)d_in[4]; in.ldt = (const u16*)d_in[5]; in.dsk = (const u16*)d_in[6];
  in.w1 = (const u16*)d_in[8]; in.w2 = (const u16*)d_in[9]; in.w3 = (const u16*)d_in[10]; in.w4 = (const u16*)d_in[11];
  k_main<<<256, 512, 0, stream>>>(in, (char*)d_ws, (u16*)d_out);
}
```

```cpp
#include <hip/hip_runtime.h>
#include <hip/hip_bf16.h>
#include <stdint.h>
#include <utility>

typedef unsigned short u16;
typedef unsigned u32;
typedef _Float16 f16;
typedef __attribute__((ext_vector_type(2))) _Float16 f16x2;
typedef __attribute__((ext_vector_type(8))) _Float16 f16x8;
typedef __attribute__((ext_vector_type(2))) __bf16 bf16x2;
typedef __attribute__((ext_vector_type(8))) __bf16 bf16x8;
typedef __attribute__((ext_vector_type(2))) float f32x2;
typedef __attribute__((ext_vector_type(4))) float f32x4;
typedef __attribute__((ext_vector_type(2))) unsigned u32x2;
typedef __attribute__((ext_vector_type(4))) unsigned u32x4;
typedef __attribute__((ext_vector_type(4))) short s16x4;
#define LDS_AS __attribute__((address_space(3)))

namespace {

constexpr int L = 2048, D = 640, NM = 128, F = 1728;
constexpr size_t O_UT  = 0;
constexpr size_t O_H   = O_UT  + (size_t)D * L * 2;
constexpr size_t O_G1  = O_H   + (size_t)L * D * 2 + 20480;
constexpr size_t O_X1  = O_G1  + (size_t)L * D * 2 + 20480;
constexpr size_t O_G3  = O_X1  + (size_t)L * D * 2;
constexpr size_t O_W2  = O_G3  + (size_t)L * F * 2;
constexpr size_t O_W3  = O_W2  + (size_t)D * D * 2;
constexpr size_t O_W4  = O_W3  + (size_t)16 * 20 * 7 * 1024;
constexpr size_t O_CS1 = O_W4  + (size_t)D * F * 2 + 16384;
constexpr size_t O_CS3 = O_CS1 + D * 4;
constexpr size_t O_ST0 = O_CS3 + 1792 * 4;
constexpr size_t O_PS  = O_ST0 + L * 8;
constexpr size_t O_EP  = O_PS + 128 * 2048;
constexpr size_t O_MB  = O_EP + 2048;
constexpr int MB_LN = 0, MB_1 = 512, MB_2 = 768, MB_3 = 896, MB_C = 1024, MB_T = 1152, MB_AC = 1280, MB_AT = 1536;

__device__ __forceinline__ float bf2f(u16 v) { return __uint_as_float(((u32)v) << 16); }
__device__ __forceinline__ float bflo(u32 v) { return __uint_as_float(v << 16); }
__device__ __forceinline__ float bfhi(u32 v) { return __uint_as_float(v & 0xffff0000u); }
__device__ __forceinline__ u32 pkbf(float a, float b) {
  f32x2 v = {a, b};
  return __builtin_bit_cast(u32, __builtin_convertvector(v, bf16x2));
}
__device__ __forceinline__ float rbf(float f) { return bflo(pkbf(f, 0.f)); }
__device__ __forceinline__ u32 pkh(float a, float b) {
  f32x2 v = {a, b};
  return __builtin_bit_cast(u32, __builtin_convertvector(v, f16x2));
}
__device__ __forceinline__ u32 umin(u32 a, u32 b) { return a < b ? a : b; }
__device__ __forceinline__ u32 umax(u32 a, u32 b) { return a > b ? a : b; }
__device__ __forceinline__ bf16x2 as_bf2(u32 v) { return __builtin_bit_cast(bf16x2, v); }
__device__ __forceinline__ f16x2 as_h2(u32 v) { return __builtin_bit_cast(f16x2, v); }
__device__ __forceinline__ u32 lds_addr(const void* p) { return (u32)(size_t)(LDS_AS const void*)p; }
template <class T> __device__ __forceinline__ T lds_ld(u32 a) { return *(LDS_AS const T*)(size_t)a; }
template <class T> __device__ __forceinline__ void lds_st(u32 a, T v) { *(LDS_AS T*)(size_t)a = v; }

template <bool SC1>
__device__ __forceinline__ void glds16(const void* sbase, u32 voff, int goff, u32 lbase, int loff) {
  u32 t;
  if (SC1) asm volatile("s_add_i32 m0, %4, %5\n\tv_add_u32 %0, %3, %1\n\tglobal_load_lds_dwordx4 %0, %2 sc1"
               : "=&v"(t) : "v"(voff), "s"(sbase), "i"(goff), "s"(lbase), "i"(loff) : "memory", "scc");
  else asm volatile("s_add_i32 m0, %4, %5\n\tv_add_u32 %0, %3, %1\n\tglobal_load_lds_dwordx4 %0, %2"
               : "=&v"(t) : "v"(voff), "s"(sbase), "i"(goff), "s"(lbase), "i"(loff) : "memory", "scc");
}
__device__ __forceinline__ void glds16r(const void* sbase, u32 voff, u32 goff, u32 laddr) {
  u32 t;
  asm volatile("s_nop 4\n\ts_mov_b32 m0, %4\n\tv_add_u32 %0, %3, %1\n\tglobal_load_lds_dwordx4 %0, %2"
               : "=&v"(t) : "v"(voff), "s"(sbase), "s"(goff), "s"(laddr) : "memory");
}
template <bool SC1>
__device__ __forceinline__ void glds16_half(const void* sbase, u32 voff, int goff, u32 lbase, int loff) {
  u32 t;
  if (SC1) asm volatile("s_add_i32 m0, %4, %5\n\tv_add_u32 %0, %3, %1\n\ts_mov_b32 exec_hi, 0\n\tglobal_load_lds_dwordx4 %0, %2 sc1\n\ts_mov_b32 exec_hi, -1"
               : "=&v"(t) : "v"(voff), "s"(sbase), "i"(goff), "s"(lbase), "i"(loff) : "memory", "scc");
  else asm volatile("s_add_i32 m0, %4, %5\n\tv_add_u32 %0, %3, %1\n\ts_mov_b32 exec_hi, 0\n\tglobal_load_lds_dwordx4 %0, %2\n\ts_mov_b32 exec_hi, -1"
               : "=&v"(t) : "v"(voff), "s"(sbase), "i"(goff), "s"(lbase), "i"(loff) : "memory", "scc");
}
__device__ __forceinline__ u32x4 gload16(const void* sbase, u32 voff, int goff) {
  u32x4 r; u32 t;
  asm volatile("v_add_u32 %1, %4, %2\n\tglobal_load_dwordx4 %0, %1, %3 sc1" : "=v"(r), "=&v"(t) : "v"(voff), "s"(sbase), "i"(goff) : "memory");
  return r;
}
__device__ __forceinline__ u32x2 gload8(const void* sbase, u32 voff, int goff) {
  u32x2 r; u32 t;
  asm volatile("v_add_u32 %1, %4, %2\n\tglobal_load_dwordx2 %0, %1, %3 sc1" : "=v"(r), "=&v"(t) : "v"(voff), "s"(sbase), "i"(goff) : "memory");
  return r;
}
__device__ __forceinline__ u32x4 gload16a(const void* sbase, u32 voff, int goff) {
  u32x4 r; u32 t;
  asm volatile("v_add_u32 %1, %4, %2\n\tglobal_load_dwordx4 %0, %1, %3 sc1" : "=a"(r), "=&v"(t) : "v"(voff), "s"(sbase), "i"(goff) : "memory");
  return r;
}
__device__ __forceinline__ u32x2 gload8a(const void* sbase, u32 voff, int goff) {
  u32x2 r; u32 t;
  asm volatile("v_add_u32 %1, %4, %2\n\tglobal_load_dwordx2 %0, %1, %3 sc1" : "=a"(r), "=&v"(t) : "v"(voff), "s"(sbase), "i"(goff) : "memory");
  return r;
}
template <bool HALF>
__device__ __forceinline__ void glds16s(const void* sbase, u32 voff, u32 soff, u32 lbase, int loff) {
  u32 t;
  if (HALF) asm volatile("s_add_i32 m0, %4, %5\n\tv_add_u32 %0, %3, %1\n\ts_mov_b32 exec_hi, 0\n\tglobal_load_lds_dwordx4 %0, %2 sc1\n\ts_mov_b32 exec_hi, -1"
               : "=&v"(t) : "v"(voff), "s"(sbase), "s"(soff), "s"(lbase), "i"(loff) : "memory", "scc");
  else asm volatile("s_add_i32 m0, %4, %5\n\tv_add_u32 %0, %3, %1\n\tglobal_load_lds_dwordx4 %0, %2 sc1"
               : "=&v"(t) : "v"(voff), "s"(sbase), "s"(soff), "s"(lbase), "i"(loff) : "memory", "scc");
}
__device__ __forceinline__ u32x4 gload16as(const void* sbase, u32 voff, u32 soff, int ioff) {
  u32x4 r; u32 t;
  asm volatile("v_add_u32 %1, %4, %2\n\tglobal_load_dwordx4 %0, %1, %3 offset:%5 sc1" : "=a"(r), "=&v"(t) : "v"(voff), "s"(sbase), "s"(soff), "i"(ioff) : "memory");
  return r;
}
__device__ __forceinline__ u32x2 gload8s(const void* sbase, u32 voff, u32 soff, int ioff) {
  u32x2 r; u32 t;
  asm volatile("v_add_u32 %1, %4, %2\n\tglobal_load_dwordx2 %0, %1, %3 offset:%5 sc1" : "=v"(r), "=&v"(t) : "v"(voff), "s"(sbase), "s"(soff), "i"(ioff) : "memory");
  return r;
}
__device__ __forceinline__ void gstore16(void* sbase, u32 voff, u32x4 v) {
  asm volatile("global_store_dwordx4 %0, %1, %2 sc1\n\ts_nop 1" :: "v"(voff), "v"(v), "s"(sbase) : "memory");
}
__device__ __forceinline__ void gstore8(void* sbase, u32 voff, u32x2 v) {
  asm volatile("global_store_dwordx2 %0, %1, %2 sc1\n\ts_nop 0" :: "v"(voff), "v"(v), "s"(sbase) : "memory");
}
__device__ __forceinline__ void gstore4(void* sbase, u32 voff, u32 v) {
  asm volatile("global_store_dword %0, %1, %2 sc1\n\ts_nop 0" :: "v"(voff), "v"(v), "s"(sbase) : "memory");
}
__device__ __forceinline__ void reload16(u32x4& r, const void* sbase, u32 voff, int goff) {
  u32 t;
  asm volatile("v_add_u32 %1, %4, %2\n\tglobal_load_dwordx4 %0, %1, %3 sc1\n\ts_waitcnt vmcnt(0)" : "+v"(r), "=&v"(t) : "v"(voff), "s"(sbase), "i"(goff) : "memory");
}
__device__ __forceinline__ void reload8(u32x2& r, const void* sbase, u32 voff, int goff) {
  u32 t;
  asm volatile("v_add_u32 %1, %4, %2\n\tglobal_load_dwordx2 %0, %1, %3 sc1\n\ts_waitcnt vmcnt(0)" : "+v"(r), "=&v"(t) : "v"(voff), "s"(sbase), "i"(goff) : "memory");
}
__device__ __forceinline__ u32 mx4(u32x4 v) { return umax(umax(v[0], v[1]), umax(v[2], v[3])); }
__device__ __forceinline__ u32 mx2(u32x2 v) { return umax(v[0], v[1]); }
__device__ __forceinline__ bool unset(u32 m) { return __any(m == 0xffffffffu); }
__device__ __forceinline__ u32 san(u32 p) { return umin(p, 0xfffffffeu); }
constexpr int SPIN = 1 << 16;
__device__ __forceinline__ float qsum(float a) {
  auto r = __builtin_amdgcn_permlane16_swap(__float_as_uint(a), __float_as_uint(a), false, false);
  a = __uint_as_float(r[0]) + __uint_as_float(r[1]);
  auto r2 = __builtin_amdgcn_permlane32_swap(__float_as_uint(a), __float_as_uint(a), false, false);
  return __uint_as_float(r2[0]) + __uint_as_float(r2[1]);
}
template <class T> __device__ __forceinline__ void pin(T& x) { asm volatile("" : "+v"(x)); }
template <class T> __device__ __forceinline__ void pina(T& x) { asm volatile("" : "+a"(x)); }
#define STAMP(kid, idx)
template <int N> __device__ __forceinline__ void wait_vm() { asm volatile("s_waitcnt vmcnt(%0)" ::"i"(N) : "memory"); }
#define SCHED_FENCE() __builtin_amdgcn_sched_barrier(0)
template <class Fn, int... I> __device__ __forceinline__ void sfor_impl(std::integer_sequence<int, I...>, Fn&& f) {
  (f(std::integral_constant<int, I>{}), ...);
}
template <int N, class Fn> __device__ __forceinline__ void sfor(Fn&& f) { sfor_impl(std::make_integer_sequence<int, N>{}, f); }
template <int N> __device__ __forceinline__ void wait_vm_bar() { asm volatile("s_waitcnt vmcnt(%0)\n\ts_barrier" ::"i"(N) : "memory"); }

__device__ __forceinline__ f32x2 gelu2(f32x2 x) {
  const float c0 = -2.0f * 0.7978845608028654f * 1.4426950408889634f;
  const float c1 = c0 * 0.044715f;
  const f32x2 z = x * __builtin_elementwise_fma(x * x, f32x2{c1, c1}, f32x2{c0, c0});
  const f32x2 d = f32x2{__builtin_amdgcn_exp2f(z.x), __builtin_amdgcn_exp2f(z.y)} + 1.0f;
  return x * f32x2{__builtin_amdgcn_rcpf(d.x), __builtin_amdgcn_rcpf(d.y)};
}
__device__ __forceinline__ u32x4 widen2(u32x2 p0, u32x2 p1);
__device__ __forceinline__ u32x4 widen(u32x2 p) {
  auto a = __builtin_amdgcn_permlane16_swap(p.x, p.x, false, false);
  auto b = __builtin_amdgcn_permlane16_swap(p.y, p.y, false, false);
  return u32x4{(u32)a[0], (u32)b[0], (u32)a[1], (u32)b[1]};
}

__device__ __forceinline__ u32x4 widen2(u32x2 p0, u32x2 p1) {
  auto a = __builtin_amdgcn_permlane16_swap(p0.x, p1.x, false, false);
  auto b = __builtin_amdgcn_permlane16_swap(p0.y, p1.y, false, false);
  return u32x4{(u32)a[0], (u32)b[0], (u32)a[1], (u32)b[1]};
}
__device__ __forceinline__ f32x2 cmul(f32x2 a, f32x2 b) {
  f32x2 r;
  asm("v_pk_mul_f32 %0, %1, %2 op_sel_hi:[1,0]\n\t"
      "v_pk_fma_f32 %0, %1, %2, %0 op_sel:[1,1,0] op_sel_hi:[0,1,1] neg_lo:[1,0,0]" : "=&v"(r) : "v"(a), "v"(b));
  return r;
}
__device__ __forceinline__ u32 cmulh(u32 a, u32 b) {
  u32 r;
  asm("v_pk_mul_f16 %0, %1, %2 op_sel_hi:[1,0]\n\t"
      "v_pk_fma_f16 %0, %1, %2, %0 op_sel:[1,1,0] op_sel_hi:[0,1,1] neg_lo:[1,0,0]" : "=&v"(r) : "v"(a), "v"(b));
  return r;
}
template <int CTRL> __device__ __forceinline__ u32 dpp(u32 v) { return (u32)__builtin_amdgcn_update_dpp(0, (int)v, CTRL, 0xf, 0xf, true); }
__device__ __forceinline__ float rowsum16(float v) {
  v += __uint_as_float(dpp<0xB1>(__float_as_uint(v)));
  v += __uint_as_float(dpp<0x4E>(__float_as_uint(v)));
  v += __uint_as_float(dpp<0x141>(__float_as_uint(v)));
  v += __uint_as_float(dpp<0x140>(__float_as_uint(v)));
  return v;
}
__device__ __forceinline__ u32 wavemax(u32 v) {
  v = umax(v, dpp<0xB1>(v)); v = umax(v, dpp<0x4E>(v)); v = umax(v, dpp<0x141>(v)); v = umax(v, dpp<0x140>(v));
  auto r = __builtin_amdgcn_permlane16_swap(v, v, false, false);
  v = umax((u32)r[0], (u32)r[1]);
  auto r2 = __builtin_amdgcn_permlane32_swap(v, v, false, false);
  return umax((u32)r2[0], (u32)r2[1]);
}
__device__ __forceinline__ f32x2 cexpp(float rho, float th, float p) {
  float mag = __builtin_amdgcn_exp2f(rho * p);
  float ph = __builtin_amdgcn_fractf(th * p);
  return f32x2{mag * __builtin_amdgcn_cosf(ph), mag * __builtin_amdgcn_sinf(ph)};
}

constexpr int CV_KC = 4160;
constexpr int CV_U = 4608;
constexpr int CV_CH = 4 * CV_KC + CV_U + 32;
constexpr int K2_LDS = 160 * 1024;
constexpr int KB = 1024;

__device__ __forceinline__ u32x2 dsr64(u32 a, int off) {
  u32x2 r;
  asm volatile("ds_read_b64 %0, %1 offset:%2" : "=v"(r) : "v"(a), "i"(off));
  return r;
}
__device__ __forceinline__ u32x4 dsr128(u32 a, int off) {
  u32x4 r;
  asm volatile("ds_read_b128 %0, %1 offset:%2" : "=a"(r) : "v"(a), "i"(off));
  return r;
}

constexpr int wpos(int m, int ch) {
  return m == 1 ? (ch < 3 ? (100 + 20 * ch) * KB : 20 * (ch - 3) * KB)
       : m == 2 ? (40 + 20 * ch) * KB
       : m == 3 ? 28 * ch * KB
       : ((ch + 7) & 7) * 20 * KB;
}

struct Ctx {
  char* ws; u16* out;
  u32 sb, vo;
  int lane, w, xcd, bi;
  u32 tlo, thi;
  const char* Wg[5];
  u32 wo[3];

  float m, r;
  u32x2 hv[3], xv[3];
};

template <int M> __device__ __forceinline__ void pf_piece(const Ctx& c, int ch, int i) {
  constexpr int CHB = (M == 3 ? 28 : 20) * KB, NW = M == 3 ? 4 : 3;
  const u32 wl = c.sb + c.w * (CHB / 8);
  const u32 v = M == 1 ? c.wo[i] : c.vo;
  const int go = M == 1 ? ch * 128 * D * 2 : ch * CHB + i * 1024;
  if (i == NW - 1) glds16_half<(M != 1)>(c.Wg[M], v, go, wl, wpos(M, ch) + i * 1024);
  else glds16<(M != 1)>(c.Wg[M], v, go, wl, wpos(M, ch) + i * 1024);
}
template <int M> __device__ __forceinline__ void pf_chunk(const Ctx& c, int ch) {
#pragma unroll
  for (int i = 0; i < (M == 3 ? 4 : 3); ++i) pf_piece<M>(c, ch, i);
}

template <int M> __device__ __forceinline__ void pf7_chunk(const Ctx& c, int ch) {
  constexpr int CHB = (M == 3 ? 28 : 20) * KB, NU = M == 3 ? 4 : 3;
  const char* base = c.Wg[M] - (M == 1 ? c.w * 16 * D * 2 : c.w * (CHB / 8));
#pragma unroll
  for (int i = 0; i < NU; ++i) {
    int u = (c.w - 1) * NU + i; u = u < CHB / 1024 ? u : CHB / 1024 - 1;
    u32 v = c.vo + u * 1024;
    if (M == 1) { const int x = u * 64 + c.lane, r = x / 10, cc = x - r * 10; v = (u32)((r ^ ((r >> 1) & 4)) * (D * 2) + cc * 16); }
    glds16<(M != 1)>(base, v, M == 1 ? ch * 128 * D * 2 : ch * CHB, c.sb + u * 1024, wpos(M, ch));
  }
}
#define DRAIN_BARRIER() asm volatile("s_waitcnt vmcnt(0) lgkmcnt(0)\n\ts_barrier" ::: "memory")
__device__ __forceinline__ void post(const Ctx& c, int n, int wg, int sec, int slot) {
  if ((int)threadIdx.x < n) gstore8(c.ws + O_MB, (u32)(wg * 2048 + sec + slot * 8), u32x2{c.tlo, c.thi});
}
__device__ __forceinline__ void await(const Ctx& c, int sec, int n) {
  if (c.w == 0) {
    const u32 off = (u32)(sec + (c.lane < n ? c.lane : 0) * 8);
    for (int sp = 0; sp < SPIN; ++sp) {
      u32x2 v;
      asm volatile("global_load_dwordx2 %0, %1, %2 sc1\n\ts_waitcnt vmcnt(0)" : "=v"(v) : "v"(off), "s"(c.ws + O_MB + (size_t)blockIdx.x * 2048) : "memory");
      if (__all(v.x == c.tlo && v.y == c.thi)) break;
    }
  }
}
#define WG_BARRIER() asm volatile("s_waitcnt lgkmcnt(0)\n\ts_barrier" ::: "memory")

struct In { const u16 *x, *lar, *aim, *cre, *cim, *ldt, *dsk, *w1, *w2, *w3, *w4; };

constexpr int LN_T = 100 * KB, LN_P = 1280 + 16, LN_ST = LN_T + 32 * LN_P;
__device__ __forceinline__ void ln_compute(const Ctx& c, u32x4 (&v)[5]) {
  const int row = c.w * 4 + (c.lane >> 4), i = c.lane & 15;
  const bf16x2 ones = {(__bf16)1.0f, (__bf16)1.0f};
  float s1 = 0.f, s2 = 0.f;
#pragma unroll
  for (int s = 0; s < 5; ++s)
#pragma unroll
    for (int k = 0; k < 4; ++k) {
      s1 = __builtin_amdgcn_fdot2_f32_bf16(as_bf2(v[s][k]), ones, s1, false);
      s2 = __builtin_amdgcn_fdot2_f32_bf16(as_bf2(v[s][k]), as_bf2(v[s][k]), s2, false);
    }
  const float m = rowsum16(s1) * (1.0f / D);
  const float ve = fmaxf(rowsum16(s2) * (1.0f / D) - m * m, 0.f) + 1e-5f;
  const float r = __builtin_amdgcn_rsqf(ve), mr = -m * r;
  if (i == 0) lds_st(c.sb + LN_ST + row * 8, f32x2{m, ve * r});
#pragma unroll
  for (int s = 0; s < 5; ++s) {
    u32x4 o;
#pragma unroll
    for (int k = 0; k < 4; ++k) o[k] = pkh(bflo(v[s][k]) * r + mr, bfhi(v[s][k]) * r + mr);
    lds_st(c.sb + LN_T + row * LN_P + (i + 16 * s) * 16, o);
  }
}
__device__ __forceinline__ void ln_store(const Ctx& c, int j) {
  const int lane = c.lane, gi = lane >> 4, qq = (lane & 15) >> 2, p = lane & 3;
  char* dst = c.ws + O_UT + (size_t)(252 - 4 * j) * 64;
#pragma unroll
  for (int it = 0; it < 5; ++it) {
    const int cgp = (it * 8 + c.w) * 4 + gi;
    const u32 base = c.sb + LN_T + (31 - 8 * p - qq) * LN_P + cgp * 8;
    s16x4 a = __builtin_amdgcn_ds_read_tr16_b64_v4i16((LDS_AS s16x4*)(size_t)base);
    s16x4 b = __builtin_amdgcn_ds_read_tr16_b64_v4i16((LDS_AS s16x4*)(size_t)(base - 4 * LN_P));
    u32x2 ua = __builtin_bit_cast(u32x2, a), ub = __builtin_bit_cast(u32x2, b);
    gstore16(dst, (u32)(cgp * 16384 + (lane & 15) * 16), u32x4{ua.x, ua.y, ub.x, ub.y});
  }
  if (threadIdx.x < 16) gstore16(c.ws + O_ST0 + j * 256, threadIdx.x * 16, lds_ld<u32x4>(c.sb + LN_ST + threadIdx.x * 16));
}

struct TU { const u16* W; char* Wt; int N, KS, sb2, strip, ks0, nks, wk; };
__device__ __forceinline__ TU tu_of(const In& in, char* ws, int u) {
  TU t;
  if (u < 20)      t = TU{in.w2, ws + O_W2, D, 20, 2, u >> 1, (u & 1) * 10, 10, 0};
  else if (u < 74) t = TU{in.w3, ws + O_W3, F, 20, 1, u - 20, 0, 20, 1};
  else if (u < 134) t = TU{in.w4, ws + O_W4, D, 54, 2, (u - 74) / 6, ((u - 74) % 6) * 9, 9, 0};
  else             t = TU{in.w1, nullptr, D, 20, 1, u - 134, 0, 20, 2};
  return t;
}
__device__ __forceinline__ void tu_load(const Ctx& c, const TU& t, u32 lds) {
  const int lpr2 = t.sb2 + 1;
  const char* src = (const char*)(t.W + (size_t)t.ks0 * 32 * t.N + (t.strip << (t.sb2 + 4)));
  const u32 voff = (u32)(c.lane >> lpr2) * (u32)t.N * 2u + (u32)(c.lane & ((1 << lpr2) - 1)) * 16u;
  const int ni = t.nks << t.sb2;
  const u32 step = (u32)(64 >> lpr2) * (u32)t.N * 2u;
#pragma unroll
  for (int j = 0; j < 5; ++j) {
    int i = c.w + j * 8; i = i < ni ? i : c.w;
    glds16r(src, voff, (u32)i * step, lds + i * 1024);
  }
}
__device__ __forceinline__ void tu_emit(const Ctx& c, const TU& t, u32 lds, u32 red) {
  const int lane = c.lane, w = c.w;
  const int RB = 32 << t.sb2;
  const int e = w & ((1 << t.sb2) - 1);
  const int g = lane >> 4, q = (lane & 15) >> 2, p = lane & 3;
  const int B = (t.strip << t.sb2) + e;
  int ct, f;
  if (t.wk == 1) { const int hh = B >= 54 ? 1 : 0, b2 = B - hh * 54, c8 = b2 < 48 ? b2 / 7 : 7; ct = hh * 8 + c8; f = c8 <= 6 ? b2 - c8 * 7 : b2 - 48; }
  else { ct = B / 5; f = B - ct * 5; }
  const int kstr = t.wk == 1 ? 7168 : 5120;
  char* dst = t.Wt + (size_t)(ct * t.KS + t.ks0) * kstr + f * 1024;
  const int k0 = w >> t.sb2, kst = 8 >> t.sb2;
  float acc = 0.f;
  const bf16x2 ones = {(__bf16)1.0f, (__bf16)1.0f};
  u32x4 v[5];
#pragma unroll
  for (int j = 0; j < 5; ++j) {
    int ks = k0 + j * kst; ks = ks < t.nks ? ks : k0;
    const u32 base = lds + (ks * 32 + 8 * g + q) * RB + e * 32 + p * 8;
    s16x4 a = __builtin_amdgcn_ds_read_tr16_b64_v4i16((LDS_AS s16x4*)(size_t)base);
    s16x4 b = __builtin_amdgcn_ds_read_tr16_b64_v4i16((LDS_AS s16x4*)(size_t)(base + 4 * RB));
    u32x2 ua = __builtin_bit_cast(u32x2, a), ub = __builtin_bit_cast(u32x2, b);
    v[j] = u32x4{ua.x, ua.y, ub.x, ub.y};
  }
#pragma unroll
  for (int j = 0; j < 5; ++j) {
    int ks = k0 + j * kst; ks = ks < t.nks ? ks : k0;
    if (t.wk != 2) gstore16(dst, (u32)(ks * kstr + lane * 16), v[j]);
#pragma unroll
    for (int k = 0; k < 4; ++k) acc = __builtin_amdgcn_fdot2_f32_bf16(as_bf2(v[j][k]), ones, acc, false);
  }
  if (t.wk) {
    acc = qsum(acc);
    if (lane < 16) lds_st(red + (w * 16 + lane) * 4, acc);
    WG_BARRIER();
    if (threadIdx.x < 32) {
      const u32 a = red + threadIdx.x * 4;
      gstore4(c.ws + (t.wk == 1 ? O_CS3 : O_CS1) + t.strip * 128, threadIdx.x * 4,
              __float_as_uint((lds_ld<float>(a) + lds_ld<float>(a + 128)) + (lds_ld<float>(a + 256) + lds_ld<float>(a + 384))));
    }
  }
}

constexpr int KG_SZ = 28 * KB, KG_KT = 112 * KB, TP = 528;
struct KP { u32 ldt, lar, aim, cr, ci; };
__device__ __forceinline__ u32 gload2(const void* sbase, u32 voff) {
  u32 r;
  asm volatile("global_load_ushort %0, %1, %2" : "=v"(r) : "v"(voff), "s"(sbase) : "memory");
  return r;
}
__device__ __forceinline__ u32 gload4s(const void* sbase, u32 voff) {
  u32 r;
  asm volatile("global_load_dword %0, %1, %2 sc1" : "=v"(r) : "v"(voff), "s"(sbase) : "memory");
  return r;
}
__device__ __forceinline__ u32 gload4(const void* sbase, u32 voff) {
  u32 r;
  asm volatile("global_load_dword %0, %1, %2" : "=v"(r) : "v"(voff), "s"(sbase) : "memory");
  return r;
}
__device__ __forceinline__ void kg_setA(float rho, float th, f32x2 C, u32 (&a)[21]) {
  f32x2 A[8];
  A[0] = C;
  const f32x2 E1 = cexpp(rho, th, 1.f);
  const f32x2 G1 = cexpp(rho, th, 256.f);
  const f32x2 G4 = cexpp(rho, th, 1024.f);
  const f32x2 E2 = cmul(E1, E1), E4 = cmul(E2, E2);
  A[1] = cmul(A[0], E1); A[2] = cmul(A[0], E2); A[3] = cmul(A[1], E2);
  A[4] = cmul(A[0], E4); A[5] = cmul(A[1], E4); A[6] = cmul(A[2], E4); A[7] = cmul(A[3], E4);
  const f32x2 G2 = cmul(G1, G1), G3 = cmul(G2, G1), A16 = cmul(G4, A[0]);
#pragma unroll
  for (int k = 0; k < 8; ++k) a[k] = pkh(A[k].x, A[k].y);
  const u32 g1 = pkh(G1.x, G1.y), g2 = pkh(G2.x, G2.y), g3 = pkh(G3.x, G3.y), g4 = pkh(G4.x, G4.y);
  a[16] = pkh(A16.x, A16.y);
  a[8] = cmulh(g1, a[0]); a[9] = cmulh(g1, a[2]); a[10] = cmulh(g1, a[4]); a[11] = cmulh(g1, a[6]);
  a[12] = cmulh(g2, a[0]); a[13] = cmulh(g2, a[4]); a[14] = cmulh(g3, a[0]); a[15] = cmulh(g3, a[4]);
  a[17] = cmulh(g1, a[16]); a[18] = cmulh(g2, a[16]); a[19] = cmulh(g3, a[16]); a[20] = cmulh(g4, a[16]);
}
__device__ __forceinline__ void kg_setB(float rho, float th, u32 (&b)[32]) {
  f32x2 lo[8], hi[4];
  lo[1] = cexpp(rho, -th, 8.f);
  hi[1] = cexpp(rho, -th, 64.f);
  lo[2] = cmul(lo[1], lo[1]); lo[3] = cmul(lo[2], lo[1]); lo[4] = cmul(lo[2], lo[2]);
  lo[5] = cmul(lo[4], lo[1]); lo[6] = cmul(lo[4], lo[2]); lo[7] = cmul(lo[4], lo[3]);
  hi[2] = cmul(hi[1], hi[1]); hi[3] = cmul(hi[2], hi[1]);
  b[0] = 0x00003c00u;
#pragma unroll
  for (int k = 1; k < 8; ++k) b[k] = pkh(lo[k].x, lo[k].y);
#pragma unroll
  for (int j = 1; j < 4; ++j) {
    b[8 * j] = pkh(hi[j].x, hi[j].y);
#pragma unroll
    for (int k = 1; k < 8; ++k) b[8 * j + k] = cmulh(b[8 * j], b[k]);
  }
}
__device__ __forceinline__ float kg_tables(const KP& p, u32 tab, int set, int lane) {
  const float LOG2E = 1.4426950408889634f;
  const int sh = set ? 0 : 16;
  const float dt = rbf(__builtin_amdgcn_exp2f(bflo(p.ldt) * LOG2E));
  const float rho = (-rbf(__builtin_amdgcn_exp2f(bfhi(p.lar << sh) * LOG2E)) * dt) * LOG2E;
  const float th = (bfhi(p.aim << sh) * dt) * 0.15915494309189535f;
  const u32 mx = wavemax(umax(umax(p.cr & 0x7fffu, (p.cr >> 16) & 0x7fffu), umax(p.ci & 0x7fffu, (p.ci >> 16) & 0x7fffu)));
  u32 eb = (mx >> 7) + 1u;
  eb = eb < 2u ? 2u : (eb > 252u ? 252u : eb);
  const float sc = __uint_as_float(eb << 23), isc = __uint_as_float((254u - eb) << 23);
  const u32 ta = tab + set * 256 + lane * 4;
  u32 a[21], b[32];
  kg_setA(rho, th, f32x2{bfhi(p.cr << sh) * isc, bfhi(p.ci << sh) * isc}, a);
  kg_setB(rho, th, b);
#pragma unroll
  for (int k = 0; k < 21; ++k) asm volatile("ds_write_b32 %0, %1 offset:%2" :: "v"(ta), "v"(a[k]), "i"(k * TP) : "memory");
#pragma unroll
  for (int k = 0; k < 32; ++k) asm volatile("ds_write_b32 %0, %1 offset:%2" :: "v"(ta), "v"(b[k]), "i"((21 + k) * TP) : "memory");
  return 2.0f * dt * sc;
}
struct KI { u32 pa, pb, o[4], ob; };
__device__ __forceinline__ KI kg_index(u32 tab, u32 ktb, int at, int lane) {
  const int r = lane & 15, g = lane >> 4;
  int ra = at * 16 + r; ra = ra > 20 ? 20 : ra;
  KI k;
  k.pa = tab + ra * TP + g * 16; k.pb = tab + (21 + r) * TP + g * 16;
  const int sbs = at == 0 ? (g < 2 ? 8 : (g == 2 ? 4 : 2)) : 1;
  const int base = at == 0 ? (g < 2 ? 4 * g : (g == 2 ? 256 : 384)) : (g == 0 ? 512 : 640 + (g > 1 ? 64 : 0));
  const int sk = at == 0 ? 1 : (g == 0 ? 32 : 64), x2 = (at == 0 && g == 3) ? 62 : 0;
  const u32 o = ktb + (base + r * sbs) * 2;
  k.o[0] = o; k.o[1] = o + sk * 2; k.o[2] = o + (2 * sk + x2) * 2; k.o[3] = o + (3 * sk + x2) * 2;
  k.ob = sbs * 32;
  pin(k.pa); pin(k.pb); pin(k.o[0]); pin(k.o[1]); pin(k.o[2]); pin(k.o[3]); pin(k.ob);
  return k;
}
__device__ __forceinline__ void kg_mfma(const KI& k) {
  f32x4 acc0 = {0.f, 0.f, 0.f, 0.f}, acc1 = acc0;
  f16x8 a[8], b0[8], b1[8];
#pragma unroll
  for (int s = 0; s < 8; ++s) { a[s] = lds_ld<f16x8>(k.pa + s * 64); b0[s] = lds_ld<f16x8>(k.pb + s * 64); b1[s] = lds_ld<f16x8>(k.pb + 16 * TP + s * 64); }
  SCHED_FENCE();
#pragma unroll
  for (int s = 0; s < 8; ++s) {
    acc0 = __builtin_amdgcn_mfma_f32_16x16x32_f16(a[s], b0[s], acc0, 0, 0, 0);
    acc1 = __builtin_amdgcn_mfma_f32_16x16x32_f16(a[s], b1[s], acc1, 0, 0, 0);
  }
  asm volatile("s_nop 7\n\ts_nop 7" : "+a"(acc0), "+a"(acc1));
#pragma unroll
  for (int e = 0; e < 4; ++e) { lds_st(k.o[e], (f16)acc0[e]); lds_st(k.o[e] + k.ob, (f16)acc1[e]); }
}
__device__ __forceinline__ void kg_lagidx(int t, u32 kt, u32 (&ix)[11]) {
#pragma unroll
  for (int k = 0; k < 11; ++k) {
    const int lag = t * 8 + k;
    const int tt = k < 8 ? t : t + 1;
    const int s = tt < 32 ? 0 : (tt < 64 ? 1 : (tt < 128 ? 2 : 3));
    const int add = ((1 << s) >> 1) - (s > 0 ? 1 : 0);
    ix[k] = kt + (128 * s + ((lag + add + ((lag >> s) & (s > 0 ? 1 : 0))) >> s)) * 2;
    pin(ix[k]);
  }
}
__device__ __forceinline__ void kg_expand(const u32 (&v)[11], u32 kc, int t) {
  u32 e[5], o[5];
#pragma unroll
  for (int k = 0; k < 5; ++k) { e[k] = v[2 * k] | (v[2 * k + 1] << 16); o[k] = v[2 * k + 1] | (v[2 * k + 2] << 16); }
  const u32 a = kc + 32 + t * 16;
  lds_st(a, u32x4{e[0], e[1], e[2], e[3]});
  lds_st(a + CV_KC, u32x4{o[0], o[1], o[2], o[3]});
  lds_st(a + 2 * CV_KC, u32x4{e[1], e[2], e[3], e[4]});
  lds_st(a + 3 * CV_KC, u32x4{o[1], o[2], o[3], o[4]});
  if (t == 0) {
    const u32x4 z = {0u, 0u, 0u, 0u};
#pragma unroll
    for (int cc = 0; cc < 4; ++cc) lds_st(kc + cc * CV_KC, z);
    lds_st(kc + 16, z);
    lds_st(kc + CV_KC + 16, u32x4{0u, 0u, 0u, v[0] << 16});
    lds_st(kc + 2 * CV_KC + 16, u32x4{0u, 0u, 0u, e[0]});
    lds_st(kc + 3 * CV_KC + 16, u32x4{0u, 0u, v[0] << 16, o[0]});
  }
}

__device__ __forceinline__ void conv_phase(Ctx& c, u32x2 dsv, int cg, f32x4 ksc, u32x4 uv0, u32x4 uv1, u32x4 (&stv)[2]) {
  char* ws = c.ws;
  const int lane = c.lane, w = c.w;
  const int ch = w & 3, hh = w >> 2;
  const u32 sb = c.sb;
  const u32 mya = sb + ch * CV_CH;
  const int r = lane & 15, g = lane >> 4;
  wait_vm<2>();
  pin(uv0); pin(uv1);
  {
    const u32 ua = sb + (lane & 3) * CV_CH + 4 * CV_KC + (w * 32 + (lane >> 2)) * 16;
    lds_st(ua, uv0); lds_st(ua + 256, uv1);
  }
  WG_BARRIER();
  STAMP(1, 2);

  const u32 pa = mya + ((r + 1) & 3) * CV_KC + (((r + 1) & ~3) + 8 * g) * 2 + hh * 256;
  const u32 pb = mya + 4 * CV_KC + (2032 - 16 * r + 8 * g) * 2 - 7 * 512 + hh * 256;
  f32x4 acc[8];
#pragma unroll
  for (int i = 0; i < 8; ++i) { acc[i] = f32x4{0.f, 0.f, 0.f, 0.f}; pina(acc[i]); }
  u32x2 Al[2][8], Ah[2][8];
  u32x4 B[2][8];
#pragma unroll
  for (int a = 0; a < 8; ++a) {
    Al[0][a] = dsr64(pa, a * 512); Ah[0][a] = dsr64(pa, a * 512 + 8);
    B[0][a] = dsr128(pb, (7 - a) * 512);
    if (a == 3) asm volatile("s_waitcnt lgkmcnt(6)" ::: "memory");
  }
#pragma unroll
  for (int rho = 0; rho < 4; ++rho) {
    const int cur = rho & 1, nxt = cur ^ 1;
    asm volatile("s_waitcnt lgkmcnt(0)" ::: "memory");
#pragma unroll
    for (int a = 0; a < 8; ++a) { pin(Al[cur][a]); pin(Ah[cur][a]); pina(B[cur][a]); }
    SCHED_FENCE();
    int cnt = 0;
#pragma unroll
    for (int s = 7; s >= 0; --s)
#pragma unroll
      for (int a = 0; a <= s; ++a) {
        const u32x4 av = {Al[cur][a].x, Al[cur][a].y, Ah[cur][a].x, Ah[cur][a].y};
        asm volatile("v_mfma_f32_16x16x32_f16 %0, %1, %2, %0" : "+a"(acc[s]) : "v"(av), "a"(B[cur][s - a]));
        if (rho < 3 && cnt < 24) {
          const int i = cnt / 3, kind = cnt % 3;
          if (kind == 0) Al[nxt][i] = dsr64(pa, i * 512 + (rho + 1) * 64);
          else if (kind == 1) Ah[nxt][i] = dsr64(pa, i * 512 + (rho + 1) * 64 + 8);
          else B[nxt][i] = dsr128(pb, (7 - i) * 512 + (rho + 1) * 64);
        }
        if (rho < 3 && cnt >= 25 && cnt < 34 && (cnt - 25) % 3 == 0) pf_piece<1>(c, rho, (cnt - 25) / 3);
        ++cnt;
        asm volatile("" : "+a"(acc[0]), "+a"(acc[1]), "+a"(acc[2]), "+a"(acc[3]), "+a"(acc[4]), "+a"(acc[5]), "+a"(acc[6]), "+a"(acc[7]));
        SCHED_FENCE();
      }
  }
  asm volatile("s_nop 7\n\ts_nop 7" : "+a"(acc[0]), "+a"(acc[1]), "+a"(acc[2]), "+a"(acc[3]), "+a"(acc[4]), "+a"(acc[5]), "+a"(acc[6]), "+a"(acc[7]));
  STAMP(1, 3);
  asm volatile("s_barrier" ::: "memory");
  {
    const u32 ex = mya + hh * 8192 + g * 256 + ((r ^ (4 * g)) * 16);
#pragma unroll
    for (int i = 0; i < 8; ++i) asm volatile("ds_write_b128 %0, %1 offset:%2" :: "v"(ex), "a"(acc[i]), "i"(i * 1024) : "memory");
  }
  WG_BARRIER();
  STAMP(1, 6);
  wait_vm<9>();
  {
    const int d0 = cg * 4;
    char* hb = ws + O_H + (d0 >> 5) * 1024 + ((d0 >> 2) & 1) * 512 + ((d0 & 31) >> 3) * 128;
    const float dk[4] = {bflo(dsv.x), bfhi(dsv.x), bflo(dsv.y), bfhi(dsv.y)};
#pragma unroll
    for (int i = 0; i < 2; ++i) {
      const int l = w * 256 + i * 128 + lane * 2;
      pina(stv[i]);
      const float m0 = __uint_as_float(stv[i][0]), sd0 = __uint_as_float(stv[i][1]);
      const float m1 = __uint_as_float(stv[i][2]), sd1 = __uint_as_float(stv[i][3]);
      float v0[4], v1[4];
      const int eg = (l >> 2) & 3;
      const u32 eo = (u32)((l >> 8) * 1024 + eg * 256 + ((((l >> 4) & 15) ^ (4 * eg)) * 16) + (l & 3) * 4);
#pragma unroll
      for (int k = 0; k < 4; ++k) {
        const f16x2 u = lds_ld<f16x2>(sb + k * CV_CH + 4 * CV_KC + (2046 - l) * 2);
        const f32x2 ya = lds_ld<f32x2>(sb + k * CV_CH + eo), yb = lds_ld<f32x2>(sb + k * CV_CH + 8192 + eo);
        v0[k] = (ya.x + yb.x) * ksc[k] + (m0 + (sd0 + dk[k]) * (float)u[1]);
        v1[k] = (ya.y + yb.y) * ksc[k] + (m1 + (sd1 + dk[k]) * (float)u[0]);
      }
      gstore16(hb, (u32)((l >> 4) * 20480 + (l & 15) * 8),
               u32x4{pkbf(v0[0], v0[1]), pkbf(v0[2], v0[3]), pkbf(v1[0], v1[1]), pkbf(v1[2], v1[3])});
    }
  }
}

template <int MODE> struct GP {
  static constexpr int NB = MODE == 3 ? 7 : 5;
  static constexpr int KS = MODE == 4 ? 54 : 20;
  static constexpr int SPC = MODE == 3 ? 4 : 2;
  static constexpr int NS = MODE == 4 ? 27 : (MODE == 3 ? 20 : 10);
  static constexpr int LPX = MODE == 1 ? 2 : 1;
  static constexpr int A0 = MODE == 1 ? 3 : 4;
  static constexpr int AH = MODE <= 2 ? 6 : 10;
  static constexpr int hi(int s) {
    int h = A0 + 2 * s; h = h < s + AH ? h : s + AH; return h < NS ? h : NS;
  }
  static constexpr int pfm(int s) {
    return MODE == 1 ? ((s == 6 || s == 7) ? 2 : 0) : MODE == 2 ? ((s == 6 || s == 7) ? 3 : 0)
         : MODE == 3 ? ((s == 6 || s == 8 || s == 10 || s == 12 || s == 14) ? 4 : 0)
         : ((s >= 4 && s <= 14 && !(s & 1)) ? 4 : 0);
  }
  static constexpr int pfc(int s) { return MODE == 1 ? s - 3 : MODE == 2 ? s - 5 : MODE == 3 ? (s - 6) / 2 : 8 + (s - 4) / 2; }
  static constexpr int npf(int s) { return pfm(s) == 0 ? 0 : (pfm(s) == 3 ? 4 : 3); }
  static constexpr int nx(int s) { return (hi(s + 1) - hi(s)) * LPX; }
  static constexpr int before(int s) { int n = A0 * LPX; for (int i = 0; i < s; ++i) n += nx(i) + npf(i); return n; }
  static constexpr int xend(int x) {
    if (x < A0) return (x + 1) * LPX;
    int s = 0; while (hi(s + 1) <= x) ++s;
    return before(s) + (x - hi(s) + 1) * LPX;
  }
  static constexpr bool bar(int s) {
    return MODE == 1 ? (s == 4 || s == 6) : MODE == 2 ? s == 6 : MODE == 3 ? (s == 8 || s == 12)
         : (s == 4 || s == 8 || s == 12 || s == 15 || s == 19 || s == 23);
  }
  static constexpr int waitn(int s) {
    int n = before(s) - xend(s);
    if (MODE == 4 && (s == 15 || s == 19 || s == 23)) {
      const int lim = before(s) - before(s - 8);
      n = n < lim ? n : lim;
    }
    return n;
  }
  static constexpr int SCR = (MODE <= 2 ? 140 : 0) * KB;
};

struct Tile { int tb, ct, kh, Bp, Bs, oA, oS, oB; bool has_s; };
template <int MODE> __device__ __forceinline__ Tile tile_of(const Ctx& cx) {
  Tile t;
  t.kh = MODE == 3 ? 0 : cx.w >> 2;
  if (MODE == 3) {
    t.ct = cx.bi & 15;
    t.tb = (cx.xcd * 2 + (cx.bi >> 4)) * 8 + cx.w;
    const int c8 = t.ct & 7;
    const int B0 = (t.ct >> 3) * 54 + (c8 <= 6 ? c8 * 7 : 48);
    const int odd = B0 & 1;
    t.Bp = B0 + odd; t.Bs = odd ? B0 : B0 + 6; t.has_s = c8 < 6;
    t.oA = odd; t.oS = odd ? 0 : 6; t.oB = 0;
  } else {
    t.ct = cx.bi & 7;
    t.tb = (cx.xcd * 4 + (cx.bi >> 3)) * 4 + (cx.w & 3);
    const int odd = t.ct & 1;
    t.oA = odd + 2 * t.kh; t.oS = odd ? 0 : 4; t.oB = odd + 2 * (t.kh ^ 1);
    t.Bp = t.ct * 5 + t.oA; t.Bs = t.ct * 5 + t.oS; t.has_s = t.kh == 0;
  }
  return t;
}
template <int MODE>
__device__ __forceinline__ void gemm_phase(Ctx& cx) {
  using P = GP<MODE>;
  constexpr int NB = P::NB, KS = P::KS, NS = P::NS;
  char* ws = cx.ws;
  const int lane = cx.lane, w = cx.w;
  const u32 sb = cx.sb, vo = cx.vo;
  const int c = lane & 15, q = lane >> 4;
  const Tile T = tile_of<MODE>(cx);
  const int tb = T.tb, ct = T.ct, kh = T.kh, Bp = T.Bp, Bs = T.Bs;
  const bool has_s = T.has_s;
  const char* Xg = ws + (MODE == 1 ? O_H : MODE == 2 ? O_G1 : MODE == 3 ? O_X1 : O_G3) + (size_t)tb * (KS * 1024) + kh * 2048;
  constexpr int NE = MODE == 3 ? 7 : 3;

  u32x4 xf[NS];
  u32x2 xl[NS], xh[NS];
  const u32 vol = vo - kh * 1024;
  u32 vsh[4];
  if (MODE == 2) {
#pragma unroll
    for (int g = 0; g < 4; ++g) {
      const int l2 = 16 + c - (g == 0 ? 1 : g == 1 ? -1 : g == 2 ? 2 : -2);
      vsh[g] = (u32)((l2 >> 4) * 20480 + q * 256 + (l2 & 15) * 16);
    }
  }
  auto issueX = [&](int s, int i) __attribute__((always_inline)) {
    const int off = ((s / P::SPC) * 4 + s % P::SPC) * 1024;
    if (MODE == 2) {
      const int g0 = (off >> 10) / 5, g1 = ((off >> 10) + 2) / 5;
      xf[s] = gload16a(Xg - 20480, (g0 == g1 || !kh) ? vsh[g0] : vsh[g1], off);
      return;
    }
    if (MODE == 1) { if (i == 0) xl[s] = gload8(Xg, lane * 8, off); else xh[s] = gload8(Xg, lane * 8, off + 512); }
    else xf[s] = gload16a(Xg, (MODE == 4 && s == NS - 1) ? vol : vo, off);
  };
#pragma unroll
  for (int s = 0; s < P::A0; ++s)
#pragma unroll
    for (int i = 0; i < P::LPX; ++i) issueX(s, i);

  f32x4 acc[NB];
#pragma unroll
  for (int f = 0; f < NB; ++f) { acc[f] = f32x4{0.f, 0.f, 0.f, 0.f}; pina(acc[f]); }
  float s1 = 0.f, s2 = 0.f;
  const bf16x2 ones = {(__bf16)1.0f, (__bf16)1.0f};
  constexpr int NT = MODE == 1 ? 2 : 1, BS = MODE == 1 ? 32 : 1024;
  u32 rb[NT][3][3];
#pragma unroll
  for (int t = 0; t < NT; ++t)
#pragma unroll
    for (int k = 0; k < 3; ++k) {
      const u32 b0 = sb + k * 65536 + kh * (2 * NB * 1024) + (MODE == 1 ? (8 * q + 4 * (t ^ (q & 1)) + (c >> 2)) * 160 + (c & 3) * 8 : vo);
      rb[t][0][k] = b0 + T.oA * BS; rb[t][1][k] = b0 + T.oS * BS; rb[t][2][k] = b0 + T.oB * BS;
      pin(rb[t][0][k]); pin(rb[t][1][k]); if (MODE != 3) pin(rb[t][2][k]);
    }
  auto rdW = [&](int s, int f) __attribute__((always_inline)) -> u32x4 {
    const int off = wpos(MODE, s / P::SPC) + (s % P::SPC) * NB * 1024 + (f < NE - 1 ? f : (f == NE - 1 ? 0 : f - NE)) * BS;
    const int kind = f < NE - 1 ? 0 : (f == NE - 1 ? 1 : 2);
    if (MODE != 1) {
      u32 base = rb[0][kind][off >> 16];
      if (MODE == 4 && s == NS - 1) base -= kh * (NB * 1024);
      return lds_ld<u32x4>(base + (off & 65535));
    }
    const s16x4 a = __builtin_amdgcn_ds_read_tr16_b64_v4i16((LDS_AS s16x4*)(size_t)(rb[0][kind][off >> 16] + (off & 65535)));
    const s16x4 b = __builtin_amdgcn_ds_read_tr16_b64_v4i16((LDS_AS s16x4*)(size_t)(rb[NT - 1][kind][off >> 16] + (off & 65535)));
    const u32x2 ua = __builtin_bit_cast(u32x2, a), ub = __builtin_bit_cast(u32x2, b);
    return u32x4{ua.x, ua.y, ub.x, ub.y};
  };
  u32x4 wf[2][NB];
#pragma unroll
  for (int f = 0; f < NB; ++f) wf[0][f] = rdW(0, f);
  u32 csw[2];
  u32x2 psv[4];

  sfor<NS>([&](auto S_) __attribute__((always_inline)) {
    constexpr int s = decltype(S_)::value;
    if constexpr (P::bar(s)) wait_vm_bar<P::waitn(s)>();
    else wait_vm<P::waitn(s)>();
    if (MODE == 1) { pin(xl[s]); pin(xh[s]); xf[s] = u32x4{xl[s].x, xl[s].y, xh[s].x, xh[s].y}; }
    else pina(xf[s]);
    SCHED_FENCE();
#pragma unroll
    for (int f = 0; f < NB; ++f) {
      if (s + 1 < NS) wf[(s + 1) & 1][f] = rdW(s + 1, f);
      if (MODE == 1) asm volatile("v_mfma_f32_16x16x32_bf16 %0, %1, %2, %0" : "+a"(acc[f]) : "v"(wf[s & 1][f]), "v"(xf[s]));
      else asm volatile("v_mfma_f32_16x16x32_bf16 %0, %1, %2, %0" : "+a"(acc[f]) : "v"(wf[s & 1][f]), "a"(xf[s]));
      if (MODE == 1 && f < 4) {
        bf16x2 v = as_bf2(xf[s][f]);
        s1 = __builtin_amdgcn_fdot2_f32_bf16(v, ones, s1, false);
        s2 = __builtin_amdgcn_fdot2_f32_bf16(v, v, s2, false);
      }
      constexpr int nx = P::nx(s);
      if (f < nx) issueX(P::hi(s) + f / P::LPX, f % P::LPX);
      else if (f - nx < P::npf(s)) pf_piece<(P::pfm(s) ? P::pfm(s) : 1)>(cx, P::pfc(s), f - nx);
      if ((MODE == 1 || MODE == 3) && s == 2 && f == 4) {
        const char* cs = ws + (MODE == 1 ? O_CS1 : O_CS3);
#pragma unroll
        for (int k = 0; k < (MODE == 3 ? 2 : 1); ++k) {
          const int sl = k * 4 + (c >> 2);
          csw[k] = gload4s(cs, (u32)((sl >= NE - 1 ? Bs : Bp + sl) * 64 + q * 16 + (c & 3) * 4));
        }
      }
      if (MODE == 3 && s == 3 && f >= 3) psv[f - 3] = gload8(ws + O_PS + tb * 2048, lane * 8, (f - 3) * 512);
      if (MODE == 1 && s == NS - 1 && f < 3) {
        const int B = f < 2 ? Bp + f : Bs;
        cx.hv[f] = gload8a(ws + O_H + (size_t)tb * 20480 + (B >> 1) * 1024 + (B & 1) * 256, (u32)((q & 1) * 512 + (q >> 1) * 128 + c * 8), 0);
      }
      SCHED_FENCE();
    }
  });
  asm volatile("s_nop 7\n\ts_nop 7" ::: "memory");
#pragma unroll
  for (int f = 0; f < NB; ++f) pina(acc[f]);
  STAMP(1 + MODE, 14);
  asm volatile("s_barrier" ::: "memory");
  if (MODE != 3) {
    auto wr = [&](u32 a, f32x4& v) __attribute__((always_inline)) { asm volatile("ds_write_b128 %0, %1" :: "v"(a), "a"(v) : "memory"); };
    const u32 base = sb + P::SCR + (w & 3) * 5120 + vo;
    const u32 mine = base + kh * 2048, theirs = base + (kh ^ 1) * 2048;
    wr(mine, acc[3]); wr(mine + 1024, acc[4]);
    if (kh) wr(mine + 2048, acc[2]);
    if (MODE == 1) {
      s1 = qsum(s1); s2 = qsum(s2);
      lds_st(sb + 28 * KB + w * 512 + lane * 8, f32x2{s1, s2});
    }
    WG_BARRIER();
    acc[0] += lds_ld<f32x4>(theirs); acc[1] += lds_ld<f32x4>(theirs + 1024); acc[2] += lds_ld<f32x4>(theirs + 2048);
    if (MODE == 1) { f32x2 o = lds_ld<f32x2>(sb + 28 * KB + (w ^ 4) * 512 + lane * 8); s1 += o.x; s2 += o.y; }
  }
  STAMP(1 + MODE, 13);
  const u32 sofs = (u32)(Bs * 512 + (q >> 1) * 256);
  const int gq = (q & 1) * 2 + (q >> 1);
  if (MODE == 1 || MODE == 3) {
    pin(csw[0]); if (MODE == 3) pin(csw[1]);
    float a = s1, bq = s2;
    if (MODE == 3) {
#pragma unroll
      for (int t = 0; t < 4; ++t) { pin(psv[t]); a += __uint_as_float(psv[t].x); bq += __uint_as_float(psv[t].y); }
      a = qsum(a); bq = qsum(bq);
    }
    const float m = a * (1.0f / D);
    const float var = fmaxf(bq * (1.0f / D) - m * m, 0.f);
    const float r = __builtin_amdgcn_rsqf(var + 1e-5f);
    float rr = r;
    if (MODE == 1) { rr = r * __builtin_amdgcn_rsqf(var * r * r + 1e-5f); cx.m = m; cx.r = r; }
    const float mr = -m * rr;
    const u32 tofs = (u32)(tb * (MODE == 1 ? 20480 : 54 * 1024) + c * 16);
    char* dst = ws + (MODE == 1 ? O_G1 : O_G3);
    auto act = [&](auto F_) __attribute__((always_inline)) -> u32x2 {
      constexpr int f = decltype(F_)::value;
      const f32x4 cs = {__uint_as_float(dpp<0x150 + (f & 3) * 4>(csw[f >> 2])), __uint_as_float(dpp<0x151 + (f & 3) * 4>(csw[f >> 2])),
                        __uint_as_float(dpp<0x152 + (f & 3) * 4>(csw[f >> 2])), __uint_as_float(dpp<0x153 + (f & 3) * 4>(csw[f >> 2]))};
      const f32x4 xx = acc[f] * rr + cs * mr;
      const f32x2 o0 = gelu2(f32x2{xx[0], xx[1]}), o1 = gelu2(f32x2{xx[2], xx[3]});
      return u32x2{pkbf(o0.x, o0.y), pkbf(o1.x, o1.y)};
    };
    sfor<(NE - 1) / 2>([&](auto J_) __attribute__((always_inline)) {
      constexpr int j = decltype(J_)::value;
      if (MODE == 3) { pf_piece<4>(cx, 5 + j, 0); pf_piece<4>(cx, 5 + j, 1); } else { pf_piece<3>(cx, 0, 0); pf_piece<3>(cx, 0, 1); }
      const u32x2 p0 = act(std::integral_constant<int, 2 * j>{});
      if (MODE == 3) pf_piece<4>(cx, 5 + j, 2); else { pf_piece<3>(cx, 0, 2); pf_piece<3>(cx, 0, 3); }
      const u32x2 p1 = act(std::integral_constant<int, 2 * j + 1>{});
      gstore16(dst, tofs + (u32)((Bp + 2 * j) * 512 + gq * 256), widen2(p0, p1));
    });
    if (has_s) { const u32x4 v = widen(act(std::integral_constant<int, NE - 1>{})); if (!(q & 1)) gstore16(dst, tofs + sofs, v); }
  } else if (MODE == 2) {
    const float m = cx.m, r = cx.r;
    pina(cx.hv[0]); pina(cx.hv[1]); pina(cx.hv[2]);
    u32 p[6];
#pragma unroll
    for (int k = 0; k < 6; ++k) {
      const u32 hv = cx.hv[k >> 1][k & 1];
      p[k] = pkbf((bflo(hv) - m) * r + acc[k >> 1][(k & 1) * 2], (bfhi(hv) - m) * r + acc[k >> 1][(k & 1) * 2 + 1]);
    }
#pragma unroll
    for (int k = 0; k < 3; ++k) cx.xv[k] = u32x2{p[2 * k], p[2 * k + 1]};
    gstore16(ws + O_X1 + (size_t)tb * 20480 + Bp * 512, (u32)(gq * 256 + c * 16), widen2(cx.xv[0], cx.xv[1]));
    if (has_s) { const u32x4 v = widen(cx.xv[2]); if (!(q & 1)) gstore16(ws + O_X1 + (size_t)tb * 20480, sofs + c * 16, v); }
    if (!has_s) { p[4] = 0u; p[5] = 0u; }
#pragma unroll
    for (int k = 0; k < 6; ++k) {
      s1 = __builtin_amdgcn_fdot2_f32_bf16(as_bf2(p[k]), ones, s1, false);
      s2 = __builtin_amdgcn_fdot2_f32_bf16(as_bf2(p[k]), as_bf2(p[k]), s2, false);
    }
    s1 = qsum(s1); s2 = qsum(s2);
    if (q == 0) gstore8(ws + O_PS + tb * 2048 + (ct * 2 + kh) * 128, c * 8, u32x2{__float_as_uint(s1), __float_as_uint(s2)});
  } else {
    u32 p[6];
#pragma unroll
    for (int k = 0; k < 6; ++k) {
      const u32 hv = cx.hv[k >> 1][k & 1], xv = cx.xv[k >> 1][k & 1];
      p[k] = pkbf(bflo(hv) + bflo(xv) + acc[k >> 1][(k & 1) * 2], bfhi(hv) + bfhi(xv) + acc[k >> 1][(k & 1) * 2 + 1]);
    }
    u16* orow = cx.out + (size_t)(tb * 16 + c) * D;
    *(u32x4*)(orow + Bp * 16 + gq * 8) = widen2(u32x2{p[0], p[1]}, u32x2{p[2], p[3]});
    if (has_s) *(u32x2*)(orow + Bs * 16 + q * 4) = u32x2{p[4], p[5]};
  }
  STAMP(1 + MODE, 15);
}

__global__ __launch_bounds__(512, 2) __attribute__((amdgpu_waves_per_eu(2, 2)))
void k_main(In in, char* __restrict__ ws, u16* __restrict__ out) {
  __shared__ __attribute__((aligned(16))) char smem[K2_LDS];
  STAMP(0, 0);
  asm volatile("" :: "s"(in.x), "s"(in.lar), "s"(in.aim), "s"(in.cre), "s"(in.cim), "s"(in.ldt), "s"(in.dsk), "s"(in.w1), "s"(in.w2), "s"(in.w3), "s"(in.w4), "s"(ws), "s"(out));
  Ctx c;
  c.ws = ws; c.out = out;
  c.lane = threadIdx.x & 63; c.w = __builtin_amdgcn_readfirstlane(threadIdx.x >> 6);
  c.sb = lds_addr(smem); c.vo = c.lane * 16;
  c.xcd = blockIdx.x & 7; c.bi = blockIdx.x >> 3;
  const int rt = c.xcd * 4 + (c.bi >> 3), ct = c.bi & 7, rt3 = c.xcd * 2 + (c.bi >> 4);
  auto setup = [&]() __attribute__((always_inline)) {
  c.Wg[1] = (const char*)(in.w1 + ct * 80 + c.w * 16 * D);
  c.Wg[2] = ws + O_W2 + (size_t)ct * (100 * KB) + c.w * 2560;
  c.Wg[3] = ws + O_W3 + (size_t)(c.bi & 15) * (140 * KB) + c.w * 3584;
  c.Wg[4] = ws + O_W4 + (size_t)ct * (270 * KB) + c.w * 2560;
#pragma unroll
  for (int i = 0; i < 3; ++i) {
    const int u = i * 64 + c.lane, r = (u / 10) & 15, cc = u % 10;
    c.wo[i] = (u32)((r ^ ((r >> 3) << 2)) * (D * 2) + cc * 16);
  }
#pragma unroll
  for (int i = 0; i < 3; ++i) pin(c.wo[i]);
  };
  const bool isconv = c.bi >= 8 && c.bi < 28;
  const int cg = c.xcd * 20 + c.bi - 8;
  const u32 epo = (c.w == 0 && c.lane >= 1 && c.lane <= 16) ? (u32)(2048 + blockIdx.x * 2048 + (c.lane - 1) * 128) : 0u;
  auto mktag = [&](u32x2& epv) __attribute__((always_inline)) {
    pin(epv);
    const unsigned long long ep = ((unsigned long long)epv.y << 32) | epv.x;
    unsigned long long t = (ep ^ (ep >> 31)) * 0x9E3779B97F4A7C15ull + 0x632BE59BD9B4E019ull;
    t ^= t >> 29;
    c.tlo = __builtin_amdgcn_readfirstlane((u32)t); c.thi = __builtin_amdgcn_readfirstlane((u32)(t >> 32));
  };
  const int tun = (c.bi < 8 ? c.bi : c.bi - 20) * 8 + c.xcd;
  if (c.bi < 8) {
    u32x4 xv[5];
    const char* xr = (const char*)(in.x + (size_t)(tun * 32 + c.w * 4) * D);
    const u32 xo = (u32)(c.lane >> 4) * (D * 2) + (u32)(c.lane & 15) * 16u;
#pragma unroll
    for (int i = 0; i < 5; ++i) xv[i] = gload16(xr, xo, i * 256);
    u32x2 epv = gload8(ws + O_EP, epo, 0);
    wait_vm<0>();
#pragma unroll
    for (int i = 0; i < 5; ++i) pin(xv[i]);
    mktag(epv);
    ln_compute(c, xv);
    WG_BARRIER();
    ln_store(c, tun);
    STAMP(0, 1);
    asm volatile("s_waitcnt vmcnt(0)\n\ts_barrier" ::: "memory");
    post(c, 160, 64 + threadIdx.x, MB_LN, tun);
    STAMP(0, 2);
  }
  if (__builtin_expect(isconv, 1)) {
    const int kch = c.w >> 1, set = c.w & 1, d = cg * 4 + kch;
    const u32 n4 = (u32)(d * NM * 2 + c.lane * 4);
    KP kp;
    kp.lar = gload4(in.lar, n4); kp.aim = gload4(in.aim, n4); kp.ldt = gload2(in.ldt, (u32)d * 2u);
    kp.cr = gload4(in.cre, n4); kp.ci = gload4(in.cim, n4);
    u32x2 dsv = gload8(in.dsk, (u32)cg * 8u, 0);
    u32x2 epv = gload8(ws + O_EP, epo, 0);
    setup();
    const u32 tab = c.sb + kch * KG_SZ;
    const KI ki = kg_index(tab, c.sb + KG_KT + kch * 2048, set, c.lane);
    u32 ix[11];
    const int gq = c.w >> 2;
    kg_lagidx(threadIdx.x & 255, c.sb + KG_KT + gq * 2048, ix);
    wait_vm<0>();
    pin(kp.lar); pin(kp.aim); pin(kp.ldt); pin(kp.cr); pin(kp.ci); pin(dsv);
    mktag(epv);
    {
      const int k = cg * 8 + c.w;
      const u16* p = k < 540 ? in.w4 : (k < 1080 ? in.w3 : in.w1);
      const int u = k < 540 ? k : (k < 1080 ? k - 540 : k - 1080);
      asm volatile("s_mov_b32 m0, %2\n\ts_nop 0\n\tglobal_load_lds_dword %0, %1" :: "v"((u32)u * 4096u + (u32)c.lane * 64u), "s"(p), "s"(c.sb + 150 * KB) : "memory");
    }
    STAMP(0, 1);
    const float ks = kg_tables(kp, tab, set, c.lane);
    if (set == 0 && c.lane == 0) lds_st(c.sb + KG_KT + 8192 + kch * 4, ks);
    WG_BARRIER();
    STAMP(0, 2);
    kg_mfma(ki);
    await(c, MB_LN, 64);
    WG_BARRIER();
    STAMP(0, 3);
    u32x4 uv0 = gload16(ws + O_UT + (size_t)cg * 16384 + c.w * 2048, c.vo, 0), uv1 = gload16(ws + O_UT + (size_t)cg * 16384 + c.w * 2048, c.vo, 1024);
    u32x4 stv[2];
#pragma unroll
    for (int i = 0; i < 2; ++i) stv[i] = gload16a(ws + O_ST0 + c.w * 2048, c.vo, i * 1024);
    u32 v0[11], v1[11];
#pragma unroll
    for (int k = 0; k < 11; ++k) { v0[k] = lds_ld<u16>(ix[k]); v1[k] = lds_ld<u16>(ix[k] + 4096); }
    SCHED_FENCE();
    kg_expand(v0, c.sb + gq * CV_CH, threadIdx.x & 255);
    kg_expand(v1, c.sb + (2 + gq) * CV_CH, threadIdx.x & 255);
    if (c.w < 4 && c.lane < 32) lds_st(c.sb + c.w * CV_CH + 4 * CV_KC + 4096 + c.lane * 16, u32x4{0u, 0u, 0u, 0u});
    const f32x4 ksc = lds_ld<f32x4>(c.sb + KG_KT + 8192);
    STAMP(1, 0);
    conv_phase(c, dsv, cg, ksc, uv0, uv1, stv);
  } else {
    u32x2 epv = gload8(ws + O_EP, epo, 0);
    const int nu = (tun >= 64 || tun < 26) ? 2 : 1, u0 = tun < 64 ? 64 + tun : tun - 64, ud = tun < 64 ? 64 : 32;
    for (int k = 0; k < nu; ++k) tu_load(c, tu_of(in, ws, u0 + k * ud), c.sb + k * 40 * KB);
    setup();
    if (tun >= 94) {
      char* z = ws + (tun == 94 ? O_G1 - 20480 : O_G1 + (size_t)L * D * 2);
      for (int i = threadIdx.x; i < 1280; i += 512) gstore16(z, i * 16u, u32x4{0u, 0u, 0u, 0u});
    }
    asm volatile("s_waitcnt vmcnt(0)\n\ts_barrier" ::: "memory");
    mktag(epv);
    for (int k = 0; k < nu; ++k) tu_emit(c, tu_of(in, ws, u0 + k * ud), c.sb + k * 40 * KB, c.sb + (98 + k) * KB);
    STAMP(0, 3);
    DRAIN_BARRIER();
    post(c, 1, 248 + c.xcd, MB_AT, c.bi < 8 ? c.bi : c.bi - 20);
#pragma unroll
    for (int k = 0; k < 3; ++k) pf_chunk<1>(c, k);
    asm volatile("s_waitcnt vmcnt(0)" ::: "memory");
  }
  STAMP(1, 15);
  if (isconv) { DRAIN_BARRIER(); post(c, 1, 248 + c.xcd, MB_AC, c.bi - 8); }
  if (c.bi == 31) {
    await(c, MB_AT, 12);
#pragma unroll
    for (int k = 0; k < 4; ++k) post(c, 64, k * 64 + threadIdx.x, MB_T, c.xcd);
  }
  if (c.w) { pf7_chunk<1>(c, 3); pf7_chunk<1>(c, 4); }
  await(c, MB_T, 8);
  asm volatile("s_barrier" ::: "memory");
  if (c.w) { pf7_chunk<2>(c, 0); pf7_chunk<2>(c, 1); pf7_chunk<2>(c, 2); }
  if (c.bi == 31) {
    await(c, MB_AC, 20);
#pragma unroll
    for (int k = 0; k < 4; ++k) post(c, 64, k * 64 + threadIdx.x, MB_C, c.xcd);
  }
  await(c, MB_C, 8);
  if (blockIdx.x == 0 && threadIdx.x == 0) gstore8(ws + O_EP, 0u, u32x2{c.tlo, c.thi});
  asm volatile("s_barrier" ::: "memory");
  STAMP(2, 0);
  gemm_phase<1>(c);
  DRAIN_BARRIER();
  {
    const int t = threadIdx.x, r2 = (rt + (t >> 3) - 1) & 31;
    post(c, 24, (((r2 & 3) * 8 + (t & 7)) << 3) + (r2 >> 2), MB_1, (2 - (t >> 3)) * 8 + ct);
  }
  await(c, MB_1, 24);
  asm volatile("s_barrier" ::: "memory");
  STAMP(3, 0);
  gemm_phase<2>(c);
  DRAIN_BARRIER();
  post(c, 16, (((c.bi & 16) + threadIdx.x) << 3) + c.xcd, MB_2, c.bi & 15);
  if (c.w) { pf7_chunk<3>(c, 3); pf7_chunk<3>(c, 4); }
  await(c, MB_2, 16);
  asm volatile("s_barrier" ::: "memory");
  STAMP(4, 0);
  gemm_phase<3>(c);
  DRAIN_BARRIER();
  post(c, 16, (((c.bi & 16) + threadIdx.x) << 3) + c.xcd, MB_3, c.bi & 15);
  await(c, MB_3, 16);
  asm volatile("s_barrier" ::: "memory");
  STAMP(5, 0);
  gemm_phase<4>(c);
}

}

extern "C" void kernel_launch(void* const* d_in, const int* in_sizes, int n_in, void* d_out, int out_size, void* d_ws,
                              size_t ws_size, hipStream_t stream) {
  In in;
  in.x = (const u16*)d_in[0]; in.lar = (const u16*)d_in[1]; in.aim = (const u16*)d_in[2]; in.cre = (const u16*)d_in[3];
  in.cim = (const u16*)d_in[4]; in.ldt = (const u16*)d_in[5]; in.dsk = (const u16*)d_in[6];
  in.w1 = (const u16*)d_in[8]; in.w2 = (const u16*)d_in[9]; in.w3 = (const u16*)d_in[10]; in.w4 = (const u16*)d_in[11];
  k_main<<<256, 512, 0, stream>>>(in, (char*)d_ws, (u16*)d_out);
}
```

```cpp
#include <hip/hip_runtime.h>
#include <hip/hip_bf16.h>
#include <stdint.h>
#include <utility>

typedef unsigned short u16;
typedef unsigned u32;
typedef _Float16 f16;
typedef __attribute__((ext_vector_type(2))) _Float16 f16x2;
typedef __attribute__((ext_vector_type(8))) _Float16 f16x8;
typedef __attribute__((ext_vector_type(2))) __bf16 bf16x2;
typedef __attribute__((ext_vector_type(8))) __bf16 bf16x8;
typedef __attribute__((ext_vector_type(2))) float f32x2;
typedef __attribute__((ext_vector_type(4))) float f32x4;
typedef __attribute__((ext_vector_type(2))) unsigned u32x2;
typedef __attribute__((ext_vector_type(4))) unsigned u32x4;
typedef __attribute__((ext_vector_type(4))) short s16x4;
#define LDS_AS __attribute__((address_space(3)))

namespace {

constexpr int L = 2048, D = 640, NM = 128, F = 1728;
constexpr size_t O_UT  = 0;
constexpr size_t O_H   = O_UT  + (size_t)D * L * 2;
constexpr size_t O_G1  = O_H   + (size_t)L * D * 2 + 20480;
constexpr size_t O_X1  = O_G1  + (size_t)L * D * 2 + 20480;
constexpr size_t O_G3  = O_X1  + (size_t)L * D * 2;
constexpr size_t O_W2  = O_G3  + (size_t)L * F * 2;
constexpr size_t O_W3  = O_W2  + (size_t)D * D * 2;
constexpr size_t O_W4  = O_W3  + (size_t)16 * 20 * 7 * 1024;
constexpr size_t O_CS1 = O_W4  + (size_t)D * F * 2 + 16384;
constexpr size_t O_CS3 = O_CS1 + D * 4;
constexpr size_t O_ST0 = O_CS3 + 1792 * 4;
constexpr size_t O_PS  = O_ST0 + L * 8;
constexpr size_t O_EP  = O_PS + 128 * 2048;
constexpr size_t O_MB  = O_EP + 2048;
constexpr int MB_LN = 0, MB_1 = 512, MB_2 = 768, MB_3 = 896, MB_C = 1024, MB_T = 1152, MB_AC = 1280, MB_AT = 1536;

__device__ __forceinline__ float bf2f(u16 v) { return __uint_as_float(((u32)v) << 16); }
__device__ __forceinline__ float bflo(u32 v) { return __uint_as_float(v << 16); }
__device__ __forceinline__ float bfhi(u32 v) { return __uint_as_float(v & 0xffff0000u); }
__device__ __forceinline__ u32 pkbf(float a, float b) {
  f32x2 v = {a, b};
  return __builtin_bit_cast(u32, __builtin_convertvector(v, bf16x2));
}
__device__ __forceinline__ float rbf(float f) { return bflo(pkbf(f, 0.f)); }
__device__ __forceinline__ u32 pkh(float a, float b) {
  f32x2 v = {a, b};
  return __builtin_bit_cast(u32, __builtin_convertvector(v, f16x2));
}
__device__ __forceinline__ u32 umin(u32 a, u32 b) { return a < b ? a : b; }
__device__ __forceinline__ u32 umax(u32 a, u32 b) { return a > b ? a : b; }
__device__ __forceinline__ bf16x2 as_bf2(u32 v) { return __builtin_bit_cast(bf16x2, v); }
__device__ __forceinline__ f16x2 as_h2(u32 v) { return __builtin_bit_cast(f16x2, v); }
__device__ __forceinline__ u32 lds_addr(const void* p) { return (u32)(size_t)(LDS_AS const void*)p; }
template <class T> __device__ __forceinline__ T lds_ld(u32 a) { return *(LDS_AS const T*)(size_t)a; }
template <class T> __device__ __forceinline__ void lds_st(u32 a, T v) { *(LDS_AS T*)(size_t)a = v; }

template <bool SC1>
__device__ __forceinline__ void glds16(const void* sbase, u32 voff, int goff, u32 lbase, int loff) {
  u32 t;
  if (SC1) asm volatile("s_add_i32 m0, %4, %5\n\tv_add_u32 %0, %3, %1\n\tglobal_load_lds_dwordx4 %0, %2 sc1"
               : "=&v"(t) : "v"(voff), "s"(sbase), "i"(goff), "s"(lbase), "i"(loff) : "memory", "scc");
  else asm volatile("s_add_i32 m0, %4, %5\n\tv_add_u32 %0, %3, %1\n\tglobal_load_lds_dwordx4 %0, %2"
               : "=&v"(t) : "v"(voff), "s"(sbase), "i"(goff), "s"(lbase), "i"(loff) : "memory", "scc");
}
__device__ __forceinline__ void glds16r(const void* sbase, u32 voff, u32 goff, u32 laddr) {
  u32 t;
  asm volatile("s_nop 4\n\ts_mov_b32 m0, %4\n\tv_add_u32 %0, %3, %1\n\tglobal_load_lds_dwordx4 %0, %2"
               : "=&v"(t) : "v"(voff), "s"(sbase), "s"(goff), "s"(laddr) : "memory");
}
template <bool SC1>
__device__ __forceinline__ void glds16_half(const void* sbase, u32 voff, int goff, u32 lbase, int loff) {
  u32 t;
  if (SC1) asm volatile("s_add_i32 m0, %4, %5\n\tv_add_u32 %0, %3, %1\n\ts_mov_b32 exec_hi, 0\n\tglobal_load_lds_dwordx4 %0, %2 sc1\n\ts_mov_b32 exec_hi, -1"
               : "=&v"(t) : "v"(voff), "s"(sbase), "i"(goff), "s"(lbase), "i"(loff) : "memory", "scc");
  else asm volatile("s_add_i32 m0, %4, %5\n\tv_add_u32 %0, %3, %1\n\ts_mov_b32 exec_hi, 0\n\tglobal_load_lds_dwordx4 %0, %2\n\ts_mov_b32 exec_hi, -1"
               : "=&v"(t) : "v"(voff), "s"(sbase), "i"(goff), "s"(lbase), "i"(loff) : "memory", "scc");
}
__device__ __forceinline__ u32x4 gload16(const void* sbase, u32 voff, int goff) {
  u32x4 r; u32 t;
  asm volatile("v_add_u32 %1, %4, %2\n\tglobal_load_dwordx4 %0, %1, %3 sc1" : "=v"(r), "=&v"(t) : "v"(voff), "s"(sbase), "i"(goff) : "memory");
  return r;
}
__device__ __forceinline__ u32x2 gload8(const void* sbase, u32 voff, int goff) {
  u32x2 r; u32 t;
  asm volatile("v_add_u32 %1, %4, %2\n\tglobal_load_dwordx2 %0, %1, %3 sc1" : "=v"(r), "=&v"(t) : "v"(voff), "s"(sbase), "i"(goff) : "memory");
  return r;
}
__device__ __forceinline__ u32x4 gload16a(const void* sbase, u32 voff, int goff) {
  u32x4 r; u32 t;
  asm volatile("v_add_u32 %1, %4, %2\n\tglobal_load_dwordx4 %0, %1, %3 sc1" : "=a"(r), "=&v"(t) : "v"(voff), "s"(sbase), "i"(goff) : "memory");
  return r;
}
__device__ __forceinline__ u32x2 gload8a(const void* sbase, u32 voff, int goff) {
  u32x2 r; u32 t;
  asm volatile("v_add_u32 %1, %4, %2\n\tglobal_load_dwordx2 %0, %1, %3 sc1" : "=a"(r), "=&v"(t) : "v"(voff), "s"(sbase), "i"(goff) : "memory");
  return r;
}
template <bool HALF>
__device__ __forceinline__ void glds16s(const void* sbase, u32 voff, u32 soff, u32 lbase, int loff) {
  u32 t;
  if (HALF) asm volatile("s_add_i32 m0, %4, %5\n\tv_add_u32 %0, %3, %1\n\ts_mov_b32 exec_hi, 0\n\tglobal_load_lds_dwordx4 %0, %2 sc1\n\ts_mov_b32 exec_hi, -1"
               : "=&v"(t) : "v"(voff), "s"(sbase), "s"(soff), "s"(lbase), "i"(loff) : "memory", "scc");
  else asm volatile("s_add_i32 m0, %4, %5\n\tv_add_u32 %0, %3, %1\n\tglobal_load_lds_dwordx4 %0, %2 sc1"
               : "=&v"(t) : "v"(voff), "s"(sbase), "s"(soff), "s"(lbase), "i"(loff) : "memory", "scc");
}
__device__ __forceinline__ u32x4 gload16as(const void* sbase, u32 voff, u32 soff, int ioff) {
  u32x4 r; u32 t;
  asm volatile("v_add_u32 %1, %4, %2\n\tglobal_load_dwordx4 %0, %1, %3 offset:%5 sc1" : "=a"(r), "=&v"(t) : "v"(voff), "s"(sbase), "s"(soff), "i"(ioff) : "memory");
  return r;
}
__device__ __forceinline__ u32x2 gload8s(const void* sbase, u32 voff, u32 soff, int ioff) {
  u32x2 r; u32 t;
  asm volatile("v_add_u32 %1, %4, %2\n\tglobal_load_dwordx2 %0, %1, %3 offset:%5 sc1" : "=v"(r), "=&v"(t) : "v"(voff), "s"(sbase), "s"(soff), "i"(ioff) : "memory");
  return r;
}
__device__ __forceinline__ void gstore16(void* sbase, u32 voff, u32x4 v) {
  asm volatile("global_store_dwordx4 %0, %1, %2 sc1\n\ts_nop 1" :: "v"(voff), "v"(v), "s"(sbase) : "memory");
}
__device__ __forceinline__ void gstore8(void* sbase, u32 voff, u32x2 v) {
  asm volatile("global_store_dwordx2 %0, %1, %2 sc1\n\ts_nop 0" :: "v"(voff), "v"(v), "s"(sbase) : "memory");
}
__device__ __forceinline__ void gstore4(void* sbase, u32 voff, u32 v) {
  asm volatile("global_store_dword %0, %1, %2 sc1\n\ts_nop 0" :: "v"(voff), "v"(v), "s"(sbase) : "memory");
}
__device__ __forceinline__ void reload16(u32x4& r, const void* sbase, u32 voff, int goff) {
  u32 t;
  asm volatile("v_add_u32 %1, %4, %2\n\tglobal_load_dwordx4 %0, %1, %3 sc1\n\ts_waitcnt vmcnt(0)" : "+v"(r), "=&v"(t) : "v"(voff), "s"(sbase), "i"(goff) : "memory");
}
__device__ __forceinline__ void reload8(u32x2& r, const void* sbase, u32 voff, int goff) {
  u32 t;
  asm volatile("v_add_u32 %1, %4, %2\n\tglobal_load_dwordx2 %0, %1, %3 sc1\n\ts_waitcnt vmcnt(0)" : "+v"(r), "=&v"(t) : "v"(voff), "s"(sbase), "i"(goff) : "memory");
}
__device__ __forceinline__ u32 mx4(u32x4 v) { return umax(umax(v[0], v[1]), umax(v[2], v[3])); }
__device__ __forceinline__ u32 mx2(u32x2 v) { return umax(v[0], v[1]); }
__device__ __forceinline__ bool unset(u32 m) { return __any(m == 0xffffffffu); }
__device__ __forceinline__ u32 san(u32 p) { return umin(p, 0xfffffffeu); }
constexpr int SPIN = 1 << 16;
__device__ __forceinline__ float qsum(float a) {
  auto r = __builtin_amdgcn_permlane16_swap(__float_as_uint(a), __float_as_uint(a), false, false);
  a = __uint_as_float(r[0]) + __uint_as_float(r[1]);
  auto r2 = __builtin_amdgcn_permlane32_swap(__float_as_uint(a), __float_as_uint(a), false, false);
  return __uint_as_float(r2[0]) + __uint_as_float(r2[1]);
}
template <class T> __device__ __forceinline__ void pin(T& x) { asm volatile("" : "+v"(x)); }
template <class T> __device__ __forceinline__ void pina(T& x) { asm volatile("" : "+a"(x)); }
#define STAMP(kid, idx)
template <int N> __device__ __forceinline__ void wait_vm() { asm volatile("s_waitcnt vmcnt(%0)" ::"i"(N) : "memory"); }
#define SCHED_FENCE() __builtin_amdgcn_sched_barrier(0)
template <class Fn, int... I> __device__ __forceinline__ void sfor_impl(std::integer_sequence<int, I...>, Fn&& f) {
  (f(std::integral_constant<int, I>{}), ...);
}
template <int N, class Fn> __device__ __forceinline__ void sfor(Fn&& f) { sfor_impl(std::make_integer_sequence<int, N>{}, f); }
template <int N> __device__ __forceinline__ void wait_vm_bar() { asm volatile("s_waitcnt vmcnt(%0)\n\ts_barrier" ::"i"(N) : "memory"); }

__device__ __forceinline__ f32x2 gelu2(f32x2 x) {
  const float c0 = -2.0f * 0.7978845608028654f * 1.4426950408889634f;
  const float c1 = c0 * 0.044715f;
  const f32x2 z = x * __builtin_elementwise_fma(x * x, f32x2{c1, c1}, f32x2{c0, c0});
  const f32x2 d = f32x2{__builtin_amdgcn_exp2f(z.x), __builtin_amdgcn_exp2f(z.y)} + 1.0f;
  return x * f32x2{__builtin_amdgcn_rcpf(d.x), __builtin_amdgcn_rcpf(d.y)};
}
__device__ __forceinline__ u32x4 widen2(u32x2 p0, u32x2 p1);
__device__ __forceinline__ u32x4 widen(u32x2 p) {
  auto a = __builtin_amdgcn_permlane16_swap(p.x, p.x, false, false);
  auto b = __builtin_amdgcn_permlane16_swap(p.y, p.y, false, false);
  return u32x4{(u32)a[0], (u32)b[0], (u32)a[1], (u32)b[1]};
}

__device__ __forceinline__ u32x4 widen2(u32x2 p0, u32x2 p1) {
  auto a = __builtin_amdgcn_permlane16_swap(p0.x, p1.x, false, false);
  auto b = __builtin_amdgcn_permlane16_swap(p0.y, p1.y, false, false);
  return u32x4{(u32)a[0], (u32)b[0], (u32)a[1], (u32)b[1]};
}
__device__ __forceinline__ f32x2 cmul(f32x2 a, f32x2 b) {
  f32x2 r;
  asm("v_pk_mul_f32 %0, %1, %2 op_sel_hi:[1,0]\n\t"
      "v_pk_fma_f32 %0, %1, %2, %0 op_sel:[1,1,0] op_sel_hi:[0,1,1] neg_lo:[1,0,0]" : "=&v"(r) : "v"(a), "v"(b));
  return r;
}
__device__ __forceinline__ u32 cmulh(u32 a, u32 b) {
  u32 r;
  asm("v_pk_mul_f16 %0, %1, %2 op_sel_hi:[1,0]\n\t"
      "v_pk_fma_f16 %0, %1, %2, %0 op_sel:[1,1,0] op_sel_hi:[0,1,1] neg_lo:[1,0,0]" : "=&v"(r) : "v"(a), "v"(b));
  return r;
}
template <int CTRL> __device__ __forceinline__ u32 dpp(u32 v) { return (u32)__builtin_amdgcn_update_dpp(0, (int)v, CTRL, 0xf, 0xf, true); }
__device__ __forceinline__ float rowsum16(float v) {
  v += __uint_as_float(dpp<0xB1>(__float_as_uint(v)));
  v += __uint_as_float(dpp<0x4E>(__float_as_uint(v)));
  v += __uint_as_float(dpp<0x141>(__float_as_uint(v)));
  v += __uint_as_float(dpp<0x140>(__float_as_uint(v)));
  return v;
}
__device__ __forceinline__ u32 wavemax(u32 v) {
  v = umax(v, dpp<0xB1>(v)); v = umax(v, dpp<0x4E>(v)); v = umax(v, dpp<0x141>(v)); v = umax(v, dpp<0x140>(v));
  auto r = __builtin_amdgcn_permlane16_swap(v, v, false, false);
  v = umax((u32)r[0], (u32)r[1]);
  auto r2 = __builtin_amdgcn_permlane32_swap(v, v, false, false);
  return umax((u32)r2[0], (u32)r2[1]);
}
__device__ __forceinline__ f32x2 cexpp(float rho, float th, float p) {
  float mag = __builtin_amdgcn_exp2f(rho * p);
  float ph = __builtin_amdgcn_fractf(th * p);
  return f32x2{mag * __builtin_amdgcn_cosf(ph), mag * __builtin_amdgcn_sinf(ph)};
}

#define PGS (512u * 1024u)
constexpr int CV_KC = 4160;
constexpr int CV_U = 4608;
constexpr int CV_CH = 4 * CV_KC + CV_U + 32;
constexpr int K2_LDS = 160 * 1024;
constexpr int KB = 1024;

__device__ __forceinline__ u32x2 dsr64(u32 a, int off) {
  u32x2 r;
  asm volatile("ds_read_b64 %0, %1 offset:%2" : "=v"(r) : "v"(a), "i"(off));
  return r;
}
__device__ __forceinline__ u32x4 dsr128(u32 a, int off) {
  u32x4 r;
  asm volatile("ds_read_b128 %0, %1 offset:%2" : "=a"(r) : "v"(a), "i"(off));
  return r;
}

constexpr int wpos(int m, int ch) {
  return m == 1 ? (ch < 3 ? (100 + 20 * ch) * KB : 20 * (ch - 3) * KB)
       : m == 2 ? (40 + 20 * ch) * KB
       : m == 3 ? 28 * ch * KB
       : ((ch + 7) & 7) * 20 * KB;
}

struct Ctx {
  char* ws; u16* out;
  u32 sb, vo;
  int lane, w, xcd, bi;
  u32 tlo, thi;
  const char* Wg[5];
  u32 wo[3];

  float m, r;
  u32x2 hv[3], xv[3];
};

template <int M> __device__ __forceinline__ void pf_piece(const Ctx& c, int ch, int i) {
  constexpr int CHB = (M == 3 ? 28 : 20) * KB, NW = M == 3 ? 4 : 3;
  const u32 wl = c.sb + c.w * (CHB / 8);
  const u32 v = M == 1 ? c.wo[i] : c.vo;
  const int go = M == 1 ? ch * 128 * D * 2 : ch * CHB + i * 1024;
  if (i == NW - 1) glds16_half<(M != 1)>(c.Wg[M], v, go, wl, wpos(M, ch) + i * 1024);
  else glds16<(M != 1)>(c.Wg[M], v, go, wl, wpos(M, ch) + i * 1024);
}
template <int M> __device__ __forceinline__ void pf_chunk(const Ctx& c, int ch) {
#pragma unroll
  for (int i = 0; i < (M == 3 ? 4 : 3); ++i) pf_piece<M>(c, ch, i);
}

template <int M> __device__ __forceinline__ void pf7_chunk(const Ctx& c, int ch) {
  constexpr int CHB = (M == 3 ? 28 : 20) * KB, NU = M == 3 ? 4 : 3;
  const char* base = c.Wg[M] - (M == 1 ? c.w * 16 * D * 2 : c.w * (CHB / 8));
#pragma unroll
  for (int i = 0; i < NU; ++i) {
    int u = (c.w - 1) * NU + i; u = u < CHB / 1024 ? u : CHB / 1024 - 1;
    u32 v = c.vo + u * 1024;
    if (M == 1) { const int x = u * 64 + c.lane, r = x / 10, cc = x - r * 10; v = (u32)((r ^ ((r >> 1) & 4)) * (D * 2) + cc * 16); }
    glds16<(M != 1)>(base, v, M == 1 ? ch * 128 * D * 2 : ch * CHB, c.sb + u * 1024, wpos(M, ch));
  }
}
#define DRAIN_BARRIER() asm volatile("s_waitcnt vmcnt(0) lgkmcnt(0)\n\ts_barrier" ::: "memory")
__device__ __forceinline__ void post(const Ctx& c, int n, int wg, int sec, int slot) {
  if ((int)threadIdx.x < n) gstore8(c.ws + O_MB, (u32)(wg * 2048 + sec + slot * 8), u32x2{c.tlo, c.thi});
}
__device__ __forceinline__ void await(const Ctx& c, int sec, int n) {
  if (c.w == 0) {
    const u32 off = (u32)(sec + (c.lane < n ? c.lane : 0) * 8);
    for (int sp = 0; sp < SPIN; ++sp) {
      u32x2 v;
      asm volatile("global_load_dwordx2 %0, %1, %2 sc1\n\ts_waitcnt vmcnt(0)" : "=v"(v) : "v"(off), "s"(c.ws + O_MB + (size_t)blockIdx.x * 2048) : "memory");
      if (__all(v.x == c.tlo && v.y == c.thi)) break;
    }
  }
}
#define WG_BARRIER() asm volatile("s_waitcnt lgkmcnt(0)\n\ts_barrier" ::: "memory")

struct In { const u16 *x, *lar, *aim, *cre, *cim, *ldt, *dsk, *w1, *w2, *w3, *w4; };

constexpr int LN_T = 100 * KB, LN_P = 1280 + 16, LN_ST = LN_T + 32 * LN_P;
__device__ __forceinline__ void ln_compute(const Ctx& c, u32x4 (&v)[5]) {
  const int row = c.w * 4 + (c.lane >> 4), i = c.lane & 15;
  const bf16x2 ones = {(__bf16)1.0f, (__bf16)1.0f};
  float s1 = 0.f, s2 = 0.f;
#pragma unroll
  for (int s = 0; s < 5; ++s)
#pragma unroll
    for (int k = 0; k < 4; ++k) {
      s1 = __builtin_amdgcn_fdot2_f32_bf16(as_bf2(v[s][k]), ones, s1, false);
      s2 = __builtin_amdgcn_fdot2_f32_bf16(as_bf2(v[s][k]), as_bf2(v[s][k]), s2, false);
    }
  const float m = rowsum16(s1) * (1.0f / D);
  const float ve = fmaxf(rowsum16(s2) * (1.0f / D) - m * m, 0.f) + 1e-5f;
  const float r = __builtin_amdgcn_rsqf(ve), mr = -m * r;
  if (i == 0) lds_st(c.sb + LN_ST + row * 8, f32x2{m, ve * r});
#pragma unroll
  for (int s = 0; s < 5; ++s) {
    u32x4 o;
#pragma unroll
    for (int k = 0; k < 4; ++k) o[k] = pkh(bflo(v[s][k]) * r + mr, bfhi(v[s][k]) * r + mr);
    lds_st(c.sb + LN_T + row * LN_P + (i + 16 * s) * 16, o);
  }
}
__device__ __forceinline__ void ln_store(const Ctx& c, int j) {
  const int lane = c.lane, gi = lane >> 4, qq = (lane & 15) >> 2, p = lane & 3;
  char* dst = c.ws + O_UT + (size_t)(252 - 4 * j) * 64;
#pragma unroll
  for (int it = 0; it < 5; ++it) {
    const int cgp = (it * 8 + c.w) * 4 + gi;
    const u32 base = c.sb + LN_T + (31 - 8 * p - qq) * LN_P + cgp * 8;
    s16x4 a = __builtin_amdgcn_ds_read_tr16_b64_v4i16((LDS_AS s16x4*)(size_t)base);
    s16x4 b = __builtin_amdgcn_ds_read_tr16_b64_v4i16((LDS_AS s16x4*)(size_t)(base - 4 * LN_P));
    u32x2 ua = __builtin_bit_cast(u32x2, a), ub = __builtin_bit_cast(u32x2, b);
    gstore16(dst, (u32)(cgp * 16384 + (lane & 15) * 16), u32x4{ua.x, ua.y, ub.x, ub.y});
  }
  if (threadIdx.x < 16) gstore16(c.ws + O_ST0 + j * 256, threadIdx.x * 16, lds_ld<u32x4>(c.sb + LN_ST + threadIdx.x * 16));
}

struct TU { const u16* W; char* Wt; int N, KS, sb2, strip, ks0, nks, wk; };
__device__ __forceinline__ TU tu_of(const In& in, char* ws, int u) {
  TU t;
  if (u < 20)      t = TU{in.w2, ws + O_W2, D, 20, 2, u >> 1, (u & 1) * 10, 10, 0};
  else if (u < 74) t = TU{in.w3, ws + O_W3, F, 20, 1, u - 20, 0, 20, 1};
  else if (u < 134) t = TU{in.w4, ws + O_W4, D, 54, 2, (u - 74) / 6, ((u - 74) % 6) * 9, 9, 0};
  else             t = TU{in.w1, nullptr, D, 20, 1, u - 134, 0, 20, 2};
  return t;
}
__device__ __forceinline__ void tu_load(const Ctx& c, const TU& t, u32 lds) {
  const int lpr2 = t.sb2 + 1;
  const char* src = (const char*)(t.W + (size_t)t.ks0 * 32 * t.N + (t.strip << (t.sb2 + 4)));
  const u32 voff = (u32)(c.lane >> lpr2) * (u32)t.N * 2u + (u32)(c.lane & ((1 << lpr2) - 1)) * 16u;
  const int ni = t.nks << t.sb2;
  const u32 step = (u32)(64 >> lpr2) * (u32)t.N * 2u;
#pragma unroll
  for (int j = 0; j < 5; ++j) {
    int i = c.w + j * 8; i = i < ni ? i : c.w;
    glds16r(src, voff, (u32)i * step, lds + i * 1024);
  }
}
__device__ __forceinline__ void tu_emit(const Ctx& c, const TU& t, u32 lds, u32 red) {
  const int lane = c.lane, w = c.w;
  const int RB = 32 << t.sb2;
  const int e = w & ((1 << t.sb2) - 1);
  const int g = lane >> 4, q = (lane & 15) >> 2, p = lane & 3;
  const int B = (t.strip << t.sb2) + e;
  int ct, f;
  if (t.wk == 1) { const int hh = B >= 54 ? 1 : 0, b2 = B - hh * 54, c8 = b2 < 48 ? b2 / 7 : 7; ct = hh * 8 + c8; f = c8 <= 6 ? b2 - c8 * 7 : b2 - 48; }
  else { ct = B / 5; f = B - ct * 5; }
  const int kstr = t.wk == 1 ? 7168 : 5120;
  char* dst = t.Wt + (size_t)(ct * t.KS + t.ks0) * kstr + f * 1024;
  const int k0 = w >> t.sb2, kst = 8 >> t.sb2;
  float acc = 0.f;
  const bf16x2 ones = {(__bf16)1.0f, (__bf16)1.0f};
  u32x4 v[5];
#pragma unroll
  for (int j = 0; j < 5; ++j) {
    int ks = k0 + j * kst; ks = ks < t.nks ? ks : k0;
    const u32 base = lds + (ks * 32 + 8 * g + q) * RB + e * 32 + p * 8;
    s16x4 a = __builtin_amdgcn_ds_read_tr16_b64_v4i16((LDS_AS s16x4*)(size_t)base);
    s16x4 b = __builtin_amdgcn_ds_read_tr16_b64_v4i16((LDS_AS s16x4*)(size_t)(base + 4 * RB));
    u32x2 ua = __builtin_bit_cast(u32x2, a), ub = __builtin_bit_cast(u32x2, b);
    v[j] = u32x4{ua.x, ua.y, ub.x, ub.y};
  }
#pragma unroll
  for (int j = 0; j < 5; ++j) {
    int ks = k0 + j * kst; ks = ks < t.nks ? ks : k0;
    if (t.wk != 2) gstore16(dst, (u32)(ks * kstr + lane * 16), v[j]);
#pragma unroll
    for (int k = 0; k < 4; ++k) acc = __builtin_amdgcn_fdot2_f32_bf16(as_bf2(v[j][k]), ones, acc, false);
  }
  if (t.wk) {
    acc = qsum(acc);
    if (lane < 16) lds_st(red + (w * 16 + lane) * 4, acc);
    WG_BARRIER();
    if (threadIdx.x < 32) {
      const u32 a = red + threadIdx.x * 4;
      gstore4(c.ws + (t.wk == 1 ? O_CS3 : O_CS1) + t.strip * 128, threadIdx.x * 4,
              __float_as_uint((lds_ld<float>(a) + lds_ld<float>(a + 128)) + (lds_ld<float>(a + 256) + lds_ld<float>(a + 384))));
    }
  }
}

constexpr int KG_SZ = 28 * KB, KG_KT = 112 * KB, TP = 528;
struct KP { u32 ldt, lar, aim, cr, ci; };
__device__ __forceinline__ u32 gload2(const void* sbase, u32 voff) {
  u32 r;
  asm volatile("global_load_ushort %0, %1, %2" : "=v"(r) : "v"(voff), "s"(sbase) : "memory");
  return r;
}
__device__ __forceinline__ u32 gload4s(const void* sbase, u32 voff) {
  u32 r;
  asm volatile("global_load_dword %0, %1, %2 sc1" : "=v"(r) : "v"(voff), "s"(sbase) : "memory");
  return r;
}
__device__ __forceinline__ u32 gload4(const void* sbase, u32 voff) {
  u32 r;
  asm volatile("global_load_dword %0, %1, %2" : "=v"(r) : "v"(voff), "s"(sbase) : "memory");
  return r;
}
__device__ __forceinline__ void kg_setA(float rho, float th, f32x2 C, u32 (&a)[21]) {
  f32x2 A[8];
  A[0] = C;
  const f32x2 E1 = cexpp(rho, th, 1.f);
  const f32x2 G1 = cexpp(rho, th, 256.f);
  const f32x2 G4 = cexpp(rho, th, 1024.f);
  const f32x2 E2 = cmul(E1, E1), E4 = cmul(E2, E2);
  A[1] = cmul(A[0], E1); A[2] = cmul(A[0], E2); A[3] = cmul(A[1], E2);
  A[4] = cmul(A[0], E4); A[5] = cmul(A[1], E4); A[6] = cmul(A[2], E4); A[7] = cmul(A[3], E4);
  const f32x2 G2 = cmul(G1, G1), G3 = cmul(G2, G1), A16 = cmul(G4, A[0]);
#pragma unroll
  for (int k = 0; k < 8; ++k) a[k] = pkh(A[k].x, A[k].y);
  const u32 g1 = pkh(G1.x, G1.y), g2 = pkh(G2.x, G2.y), g3 = pkh(G3.x, G3.y), g4 = pkh(G4.x, G4.y);
  a[16] = pkh(A16.x, A16.y);
  a[8] = cmulh(g1, a[0]); a[9] = cmulh(g1, a[2]); a[10] = cmulh(g1, a[4]); a[11] = cmulh(g1, a[6]);
  a[12] = cmulh(g2, a[0]); a[13] = cmulh(g2, a[4]); a[14] = cmulh(g3, a[0]); a[15] = cmulh(g3, a[4]);
  a[17] = cmulh(g1, a[16]); a[18] = cmulh(g2, a[16]); a[19] = cmulh(g3, a[16]); a[20] = cmulh(g4, a[16]);
}
__device__ __forceinline__ void kg_setB(float rho, float th, u32 (&b)[32]) {
  f32x2 lo[8], hi[4];
  lo[1] = cexpp(rho, -th, 8.f);
  hi[1] = cexpp(rho, -th, 64.f);
  lo[2] = cmul(lo[1], lo[1]); lo[3] = cmul(lo[2], lo[1]); lo[4] = cmul(lo[2], lo[2]);
  lo[5] = cmul(lo[4], lo[1]); lo[6] = cmul(lo[4], lo[2]); lo[7] = cmul(lo[4], lo[3]);
  hi[2] = cmul(hi[1], hi[1]); hi[3] = cmul(hi[2], hi[1]);
  b[0] = 0x00003c00u;
#pragma unroll
  for (int k = 1; k < 8; ++k) b[k] = pkh(lo[k].x, lo[k].y);
#pragma unroll
  for (int j = 1; j < 4; ++j) {
    b[8 * j] = pkh(hi[j].x, hi[j].y);
#pragma unroll
    for (int k = 1; k < 8; ++k) b[8 * j + k] = cmulh(b[8 * j], b[k]);
  }
}
__device__ __forceinline__ float kg_tables(const KP& p, u32 tab, int set, int lane) {
  const float LOG2E = 1.4426950408889634f;
  const int sh = set ? 0 : 16;
  const float dt = rbf(__builtin_amdgcn_exp2f(bflo(p.ldt) * LOG2E));
  const float rho = (-rbf(__builtin_amdgcn_exp2f(bfhi(p.lar << sh) * LOG2E)) * dt) * LOG2E;
  const float th = (bfhi(p.aim << sh) * dt) * 0.15915494309189535f;
  const u32 mx = wavemax(umax(umax(p.cr & 0x7fffu, (p.cr >> 16) & 0x7fffu), umax(p.ci & 0x7fffu, (p.ci >> 16) & 0x7fffu)));
  u32 eb = (mx >> 7) + 1u;
  eb = eb < 2u ? 2u : (eb > 252u ? 252u : eb);
  const float sc = __uint_as_float(eb << 23), isc = __uint_as_float((254u - eb) << 23);
  const u32 ta = tab + set * 256 + lane * 4;
  u32 a[21], b[32];
  kg_setA(rho, th, f32x2{bfhi(p.cr << sh) * isc, bfhi(p.ci << sh) * isc}, a);
  kg_setB(rho, th, b);
#pragma unroll
  for (int k = 0; k < 21; ++k) asm volatile("ds_write_b32 %0, %1 offset:%2" :: "v"(ta), "v"(a[k]), "i"(k * TP) : "memory");
#pragma unroll
  for (int k = 0; k < 32; ++k) asm volatile("ds_write_b32 %0, %1 offset:%2" :: "v"(ta), "v"(b[k]), "i"((21 + k) * TP) : "memory");
  return 2.0f * dt * sc;
}
struct KI { u32 pa, pb, o[4], ob; };
__device__ __forceinline__ KI kg_index(u32 tab, u32 ktb, int at, int lane) {
  const int r = lane & 15, g = lane >> 4;
  int ra = at * 16 + r; ra = ra > 20 ? 20 : ra;
  KI k;
  k.pa = tab + ra * TP + g * 16; k.pb = tab + (21 + r) * TP + g * 16;
  const int sbs = at == 0 ? (g < 2 ? 8 : (g == 2 ? 4 : 2)) : 1;
  const int base = at == 0 ? (g < 2 ? 4 * g : (g == 2 ? 256 : 384)) : (g == 0 ? 512 : 640 + (g > 1 ? 64 : 0));
  const int sk = at == 0 ? 1 : (g == 0 ? 32 : 64), x2 = (at == 0 && g == 3) ? 62 : 0;
  const u32 o = ktb + (base + r * sbs) * 2;
  k.o[0] = o; k.o[1] = o + sk * 2; k.o[2] = o + (2 * sk + x2) * 2; k.o[3] = o + (3 * sk + x2) * 2;
  k.ob = sbs * 32;
  pin(k.pa); pin(k.pb); pin(k.o[0]); pin(k.o[1]); pin(k.o[2]); pin(k.o[3]); pin(k.ob);
  return k;
}
__device__ __forceinline__ void kg_mfma(const KI& k) {
  f32x4 acc0 = {0.f, 0.f, 0.f, 0.f}, acc1 = acc0;
  f16x8 a[8], b0[8], b1[8];
#pragma unroll
  for (int s = 0; s < 8; ++s) { a[s] = lds_ld<f16x8>(k.pa + s * 64); b0[s] = lds_ld<f16x8>(k.pb + s * 64); b1[s] = lds_ld<f16x8>(k.pb + 16 * TP + s * 64); }
  SCHED_FENCE();
#pragma unroll
  for (int s = 0; s < 8; ++s) {
    acc0 = __builtin_amdgcn_mfma_f32_16x16x32_f16(a[s], b0[s], acc0, 0, 0, 0);
    acc1 = __builtin_amdgcn_mfma_f32_16x16x32_f16(a[s], b1[s], acc1, 0, 0, 0);
  }
  asm volatile("s_nop 7\n\ts_nop 7" : "+a"(acc0), "+a"(acc1));
#pragma unroll
  for (int e = 0; e < 4; ++e) { lds_st(k.o[e], (f16)acc0[e]); lds_st(k.o[e] + k.ob, (f16)acc1[e]); }
}
__device__ __forceinline__ void kg_lagidx(int t, u32 kt, u32 (&ix)[11]) {
#pragma unroll
  for (int k = 0; k < 11; ++k) {
    const int lag = t * 8 + k;
    const int tt = k < 8 ? t : t + 1;
    const int s = tt < 32 ? 0 : (tt < 64 ? 1 : (tt < 128 ? 2 : 3));
    const int add = ((1 << s) >> 1) - (s > 0 ? 1 : 0);
    ix[k] = kt + (128 * s + ((lag + add + ((lag >> s) & (s > 0 ? 1 : 0))) >> s)) * 2;
    pin(ix[k]);
  }
}
__device__ __forceinline__ void kg_expand(const u32 (&v)[11], u32 kc, int t) {
  u32 e[5], o[5];
#pragma unroll
  for (int k = 0; k < 5; ++k) { e[k] = v[2 * k] | (v[2 * k + 1] << 16); o[k] = v[2 * k + 1] | (v[2 * k + 2] << 16); }
  const u32 a = kc + 32 + t * 16;
  lds_st(a, u32x4{e[0], e[1], e[2], e[3]});
  lds_st(a + CV_KC, u32x4{o[0], o[1], o[2], o[3]});
  lds_st(a + 2 * CV_KC, u32x4{e[1], e[2], e[3], e[4]});
  lds_st(a + 3 * CV_KC, u32x4{o[1], o[2], o[3], o[4]});
  if (t == 0) {
    const u32x4 z = {0u, 0u, 0u, 0u};
#pragma unroll
    for (int cc = 0; cc < 4; ++cc) lds_st(kc + cc * CV_KC, z);
    lds_st(kc + 16, z);
    lds_st(kc + CV_KC + 16, u32x4{0u, 0u, 0u, v[0] << 16});
    lds_st(kc + 2 * CV_KC + 16, u32x4{0u, 0u, 0u, e[0]});
    lds_st(kc + 3 * CV_KC + 16, u32x4{0u, 0u, v[0] << 16, o[0]});
  }
}

__device__ __forceinline__ void conv_phase(Ctx& c, u32x2 dsv, int cg, f32x4 ksc, u32x4 uv0, u32x4 uv1, u32x4 (&stv)[2]) {
  char* ws = c.ws;
  const int lane = c.lane, w = c.w;
  const int ch = w & 3, hh = w >> 2;
  const u32 sb = c.sb;
  const u32 mya = sb + ch * CV_CH;
  const int r = lane & 15, g = lane >> 4;
  wait_vm<2>();
  pin(uv0); pin(uv1);
  {
    const u32 ua = sb + (lane & 3) * CV_CH + 4 * CV_KC + (w * 32 + (lane >> 2)) * 16;
    lds_st(ua, uv0); lds_st(ua + 256, uv1);
  }
  WG_BARRIER();
  STAMP(1, 2);

  const u32 pa = mya + ((r + 1) & 3) * CV_KC + (((r + 1) & ~3) + 8 * g) * 2 + hh * 256;
  const u32 pb = mya + 4 * CV_KC + (2032 - 16 * r + 8 * g) * 2 - 7 * 512 + hh * 256;
  f32x4 acc[8];
#pragma unroll
  for (int i = 0; i < 8; ++i) { acc[i] = f32x4{0.f, 0.f, 0.f, 0.f}; pina(acc[i]); }
  u32x2 Al[2][8], Ah[2][8];
  u32x4 B[2][8];
#pragma unroll
  for (int a = 0; a < 8; ++a) {
    Al[0][a] = dsr64(pa, a * 512); Ah[0][a] = dsr64(pa, a * 512 + 8);
    B[0][a] = dsr128(pb, (7 - a) * 512);
    if (a == 3) asm volatile("s_waitcnt lgkmcnt(6)" ::: "memory");
  }
#pragma unroll
  for (int rho = 0; rho < 4; ++rho) {
    const int cur = rho & 1, nxt = cur ^ 1;
    asm volatile("s_waitcnt lgkmcnt(0)" ::: "memory");
#pragma unroll
    for (int a = 0; a < 8; ++a) { pin(Al[cur][a]); pin(Ah[cur][a]); pina(B[cur][a]); }
    SCHED_FENCE();
    int cnt = 0;
#pragma unroll
    for (int s = 7; s >= 0; --s)
#pragma unroll
      for (int a = 0; a <= s; ++a) {
        const u32x4 av = {Al[cur][a].x, Al[cur][a].y, Ah[cur][a].x, Ah[cur][a].y};
        asm volatile("v_mfma_f32_16x16x32_f16 %0, %1, %2, %0" : "+a"(acc[s]) : "v"(av), "a"(B[cur][s - a]));
        if (rho < 3 && cnt < 24) {
          const int i = cnt / 3, kind = cnt % 3;
          if (kind == 0) Al[nxt][i] = dsr64(pa, i * 512 + (rho + 1) * 64);
          else if (kind == 1) Ah[nxt][i] = dsr64(pa, i * 512 + (rho + 1) * 64 + 8);
          else B[nxt][i] = dsr128(pb, (7 - i) * 512 + (rho + 1) * 64);
        }
        if (rho < 3 && cnt >= 25 && cnt < 34 && (cnt - 25) % 3 == 0) pf_piece<1>(c, rho, (cnt - 25) / 3);
        ++cnt;
        asm volatile("" : "+a"(acc[0]), "+a"(acc[1]), "+a"(acc[2]), "+a"(acc[3]), "+a"(acc[4]), "+a"(acc[5]), "+a"(acc[6]), "+a"(acc[7]));
        SCHED_FENCE();
      }
  }
  asm volatile("s_nop 7\n\ts_nop 7" : "+a"(acc[0]), "+a"(acc[1]), "+a"(acc[2]), "+a"(acc[3]), "+a"(acc[4]), "+a"(acc[5]), "+a"(acc[6]), "+a"(acc[7]));
  STAMP(1, 3);
  asm volatile("s_barrier" ::: "memory");
  {
    const u32 ex = mya + hh * 8192 + g * 256 + ((r ^ (4 * g)) * 16);
#pragma unroll
    for (int i = 0; i < 8; ++i) asm volatile("ds_write_b128 %0, %1 offset:%2" :: "v"(ex), "a"(acc[i]), "i"(i * 1024) : "memory");
  }
  WG_BARRIER();
  STAMP(1, 6);
  wait_vm<9>();
  {
    const int d0 = cg * 4;
    char* hb = ws + O_H + (d0 >> 5) * 1024 + ((d0 >> 2) & 1) * 512 + ((d0 & 31) >> 3) * 128;
    const float dk[4] = {bflo(dsv.x), bfhi(dsv.x), bflo(dsv.y), bfhi(dsv.y)};
#pragma unroll
    for (int i = 0; i < 2; ++i) {
      const int l = w * 256 + i * 128 + lane * 2;
      pina(stv[i]);
      const float m0 = __uint_as_float(stv[i][0]), sd0 = __uint_as_float(stv[i][1]);
      const float m1 = __uint_as_float(stv[i][2]), sd1 = __uint_as_float(stv[i][3]);
      float v0[4], v1[4];
      const int eg = (l >> 2) & 3;
      const u32 eo = (u32)((l >> 8) * 1024 + eg * 256 + ((((l >> 4) & 15) ^ (4 * eg)) * 16) + (l & 3) * 4);
#pragma unroll
      for (int k = 0; k < 4; ++k) {
        const f16x2 u = lds_ld<f16x2>(sb + k * CV_CH + 4 * CV_KC + (2046 - l) * 2);
        const f32x2 ya = lds_ld<f32x2>(sb + k * CV_CH + eo), yb = lds_ld<f32x2>(sb + k * CV_CH + 8192 + eo);
        v0[k] = (ya.x + yb.x) * ksc[k] + (m0 + (sd0 + dk[k]) * (float)u[1]);
        v1[k] = (ya.y + yb.y) * ksc[k] + (m1 + (sd1 + dk[k]) * (float)u[0]);
      }
      gstore16(hb, (u32)((l >> 4) * 20480 + (l & 15) * 8),
               u32x4{pkbf(v0[0], v0[1]), pkbf(v0[2], v0[3]), pkbf(v1[0], v1[1]), pkbf(v1[2], v1[3])});
    }
  }
}

template <int MODE> struct GP {
  static constexpr int NB = MODE == 3 ? 7 : 5;
  static constexpr int KS = MODE == 4 ? 54 : 20;
  static constexpr int SPC = MODE == 3 ? 4 : 2;
  static constexpr int NS = MODE == 4 ? 27 : (MODE == 3 ? 20 : 10);
  static constexpr int LPX = MODE == 1 ? 2 : 1;
  static constexpr int A0 = MODE == 1 ? 3 : 4;
  static constexpr int AH = MODE <= 2 ? 6 : 10;
  static constexpr int hi(int s) {
    int h = A0 + 2 * s; h = h < s + AH ? h : s + AH; return h < NS ? h : NS;
  }
  static constexpr int pfm(int s) {
    return MODE == 1 ? ((s == 6 || s == 7) ? 2 : 0) : MODE == 2 ? ((s == 6 || s == 7) ? 3 : 0)
         : MODE == 3 ? ((s == 6 || s == 8 || s == 10 || s == 12 || s == 14) ? 4 : 0)
         : ((s >= 4 && s <= 14 && !(s & 1)) ? 4 : 0);
  }
  static constexpr int pfc(int s) { return MODE == 1 ? s - 3 : MODE == 2 ? s - 5 : MODE == 3 ? (s - 6) / 2 : 8 + (s - 4) / 2; }
  static constexpr int npf(int s) { return pfm(s) == 0 ? 0 : (pfm(s) == 3 ? 4 : 3); }
  static constexpr int nx(int s) { return (hi(s + 1) - hi(s)) * LPX; }
  static constexpr int before(int s) { int n = A0 * LPX; for (int i = 0; i < s; ++i) n += nx(i) + npf(i); return n; }
  static constexpr int xend(int x) {
    if (x < A0) return (x + 1) * LPX;
    int s = 0; while (hi(s + 1) <= x) ++s;
    return before(s) + (x - hi(s) + 1) * LPX;
  }
  static constexpr bool bar(int s) {
    return MODE == 1 ? (s == 4 || s == 6) : MODE == 2 ? s == 6 : MODE == 3 ? (s == 8 || s == 12)
         : (s == 4 || s == 8 || s == 12 || s == 15 || s == 19 || s == 23);
  }
  static constexpr int waitn(int s) {
    int n = before(s) - xend(s);
    if (MODE == 4 && (s == 15 || s == 19 || s == 23)) {
      const int lim = before(s) - before(s - 8);
      n = n < lim ? n : lim;
    }
    return n;
  }
  static constexpr int SCR = (MODE <= 2 ? 140 : 0) * KB;
};

struct Tile { int tb, ct, kh, Bp, Bs, oA, oS, oB; bool has_s; };
template <int MODE> __device__ __forceinline__ Tile tile_of(const Ctx& cx) {
  Tile t;
  t.kh = MODE == 3 ? 0 : cx.w >> 2;
  if (MODE == 3) {
    t.ct = cx.bi & 15;
    t.tb = (cx.xcd * 2 + (cx.bi >> 4)) * 8 + cx.w;
    const int c8 = t.ct & 7;
    const int B0 = (t.ct >> 3) * 54 + (c8 <= 6 ? c8 * 7 : 48);
    const int odd = B0 & 1;
    t.Bp = B0 + odd; t.Bs = odd ? B0 : B0 + 6; t.has_s = c8 < 6;
    t.oA = odd; t.oS = odd ? 0 : 6; t.oB = 0;
  } else {
    t.ct = cx.bi & 7;
    t.tb = (cx.xcd * 4 + (cx.bi >> 3)) * 4 + (cx.w & 3);
    const int odd = t.ct & 1;
    t.oA = odd + 2 * t.kh; t.oS = odd ? 0 : 4; t.oB = odd + 2 * (t.kh ^ 1);
    t.Bp = t.ct * 5 + t.oA; t.Bs = t.ct * 5 + t.oS; t.has_s = t.kh == 0;
  }
  return t;
}
template <int MODE>
__device__ __forceinline__ void gemm_phase(Ctx& cx) {
  using P = GP<MODE>;
  constexpr int NB = P::NB, KS = P::KS, NS = P::NS;
  char* ws = cx.ws;
  const int lane = cx.lane, w = cx.w;
  const u32 sb = cx.sb, vo = cx.vo;
  const int c = lane & 15, q = lane >> 4;
  const Tile T = tile_of<MODE>(cx);
  const int tb = T.tb, ct = T.ct, kh = T.kh, Bp = T.Bp, Bs = T.Bs;
  const bool has_s = T.has_s;
  const char* Xg = ws + (MODE == 1 ? O_H : MODE == 2 ? O_G1 : MODE == 3 ? O_X1 : O_G3) + (size_t)tb * (KS * 1024) + kh * 2048;
  constexpr int NE = MODE == 3 ? 7 : 3;

  u32x4 xf[NS];
  u32x2 xl[NS], xh[NS];
  const u32 vol = vo - kh * 1024;
  u32 vsh[4];
  if (MODE == 2) {
#pragma unroll
    for (int g = 0; g < 4; ++g) {
      const int l2 = 16 + c - (g == 0 ? 1 : g == 1 ? -1 : g == 2 ? 2 : -2);
      vsh[g] = (u32)((l2 >> 4) * 20480 + q * 256 + (l2 & 15) * 16);
    }
  }
  auto issueX = [&](int s, int i) __attribute__((always_inline)) {
    const int off = ((s / P::SPC) * 4 + s % P::SPC) * 1024;
    if (MODE == 2) {
      const int g0 = (off >> 10) / 5, g1 = ((off >> 10) + 2) / 5;
      xf[s] = gload16a(Xg - 20480, (g0 == g1 || !kh) ? vsh[g0] : vsh[g1], off);
      return;
    }
    if (MODE == 1) { if (i == 0) xl[s] = gload8(Xg, lane * 8, off); else xh[s] = gload8(Xg, lane * 8, off + 512); }
    else xf[s] = gload16a(Xg, (MODE == 4 && s == NS - 1) ? vol : vo, off);
  };
#pragma unroll
  for (int s = 0; s < P::A0; ++s)
#pragma unroll
    for (int i = 0; i < P::LPX; ++i) issueX(s, i);

  f32x4 acc[NB];
#pragma unroll
  for (int f = 0; f < NB; ++f) { acc[f] = f32x4{0.f, 0.f, 0.f, 0.f}; pina(acc[f]); }
  float s1 = 0.f, s2 = 0.f;
  const bf16x2 ones = {(__bf16)1.0f, (__bf16)1.0f};
  constexpr int NT = MODE == 1 ? 2 : 1, BS = MODE == 1 ? 32 : 1024;
  u32 rb[NT][3][3];
#pragma unroll
  for (int t = 0; t < NT; ++t)
#pragma unroll
    for (int k = 0; k < 3; ++k) {
      const u32 b0 = sb + k * 65536 + kh * (2 * NB * 1024) + (MODE == 1 ? (8 * q + 4 * (t ^ (q & 1)) + (c >> 2)) * 160 + (c & 3) * 8 : vo);
      rb[t][0][k] = b0 + T.oA * BS; rb[t][1][k] = b0 + T.oS * BS; rb[t][2][k] = b0 + T.oB * BS;
      pin(rb[t][0][k]); pin(rb[t][1][k]); if (MODE != 3) pin(rb[t][2][k]);
    }
  auto rdW = [&](int s, int f) __attribute__((always_inline)) -> u32x4 {
    const int off = wpos(MODE, s / P::SPC) + (s % P::SPC) * NB * 1024 + (f < NE - 1 ? f : (f == NE - 1 ? 0 : f - NE)) * BS;
    const int kind = f < NE - 1 ? 0 : (f == NE - 1 ? 1 : 2);
    if (MODE != 1) {
      u32 base = rb[0][kind][off >> 16];
      if (MODE == 4 && s == NS - 1) base -= kh * (NB * 1024);
      return lds_ld<u32x4>(base + (off & 65535));
    }
    const s16x4 a = __builtin_amdgcn_ds_read_tr16_b64_v4i16((LDS_AS s16x4*)(size_t)(rb[0][kind][off >> 16] + (off & 65535)));
    const s16x4 b = __builtin_amdgcn_ds_read_tr16_b64_v4i16((LDS_AS s16x4*)(size_t)(rb[NT - 1][kind][off >> 16] + (off & 65535)));
    const u32x2 ua = __builtin_bit_cast(u32x2, a), ub = __builtin_bit_cast(u32x2, b);
    return u32x4{ua.x, ua.y, ub.x, ub.y};
  };
  u32x4 wf[2][NB];
#pragma unroll
  for (int f = 0; f < NB; ++f) wf[0][f] = rdW(0, f);
  u32 csw[2];
  u32x2 psv[4];

  sfor<NS>([&](auto S_) __attribute__((always_inline)) {
    constexpr int s = decltype(S_)::value;
    if constexpr (P::bar(s)) wait_vm_bar<P::waitn(s)>();
    else wait_vm<P::waitn(s)>();
    if (MODE == 1) { pin(xl[s]); pin(xh[s]); xf[s] = u32x4{xl[s].x, xl[s].y, xh[s].x, xh[s].y}; }
    else pina(xf[s]);
    SCHED_FENCE();
#pragma unroll
    for (int f = 0; f < NB; ++f) {
      if (s + 1 < NS) wf[(s + 1) & 1][f] = rdW(s + 1, f);
      if (MODE == 1) asm volatile("v_mfma_f32_16x16x32_bf16 %0, %1, %2, %0" : "+a"(acc[f]) : "v"(wf[s & 1][f]), "v"(xf[s]));
      else asm volatile("v_mfma_f32_16x16x32_bf16 %0, %1, %2, %0" : "+a"(acc[f]) : "v"(wf[s & 1][f]), "a"(xf[s]));
      if (MODE == 1 && f < 4) {
        bf16x2 v = as_bf2(xf[s][f]);
        s1 = __builtin_amdgcn_fdot2_f32_bf16(v, ones, s1, false);
        s2 = __builtin_amdgcn_fdot2_f32_bf16(v, v, s2, false);
      }
      constexpr int nx = P::nx(s);
      if (f < nx) issueX(P::hi(s) + f / P::LPX, f % P::LPX);
      else if (f - nx < P::npf(s)) pf_piece<(P::pfm(s) ? P::pfm(s) : 1)>(cx, P::pfc(s), f - nx);
      if ((MODE == 1 || MODE == 3) && s == 2 && f == 4) {
        const char* cs = ws + (MODE == 1 ? O_CS1 : O_CS3);
#pragma unroll
        for (int k = 0; k < (MODE == 3 ? 2 : 1); ++k) {
          const int sl = k * 4 + (c >> 2);
          csw[k] = gload4s(cs, (u32)((sl >= NE - 1 ? Bs : Bp + sl) * 64 + q * 16 + (c & 3) * 4));
        }
      }
      if (MODE == 3 && s == 3 && f >= 3) psv[f - 3] = gload8(ws + O_PS + tb * 2048, lane * 8, (f - 3) * 512);
      if (MODE == 1 && s == NS - 1 && f < 3) {
        const int B = f < 2 ? Bp + f : Bs;
        cx.hv[f] = gload8a(ws + O_H + (size_t)tb * 20480 + (B >> 1) * 1024 + (B & 1) * 256, (u32)((q & 1) * 512 + (q >> 1) * 128 + c * 8), 0);
      }
      SCHED_FENCE();
    }
  });
  asm volatile("s_nop 7\n\ts_nop 7" ::: "memory");
#pragma unroll
  for (int f = 0; f < NB; ++f) pina(acc[f]);
  STAMP(1 + MODE, 14);
  asm volatile("s_barrier" ::: "memory");
  if (MODE != 3) {
    auto wr = [&](u32 a, f32x4& v) __attribute__((always_inline)) { asm volatile("ds_write_b128 %0, %1" :: "v"(a), "a"(v) : "memory"); };
    const u32 base = sb + P::SCR + (w & 3) * 5120 + vo;
    const u32 mine = base + kh * 2048, theirs = base + (kh ^ 1) * 2048;
    wr(mine, acc[3]); wr(mine + 1024, acc[4]);
    if (kh) wr(mine + 2048, acc[2]);
    if (MODE == 1) {
      s1 = qsum(s1); s2 = qsum(s2);
      lds_st(sb + 28 * KB + w * 512 + lane * 8, f32x2{s1, s2});
    }
    WG_BARRIER();
    acc[0] += lds_ld<f32x4>(theirs); acc[1] += lds_ld<f32x4>(theirs + 1024); acc[2] += lds_ld<f32x4>(theirs + 2048);
    if (MODE == 1) { f32x2 o = lds_ld<f32x2>(sb + 28 * KB + (w ^ 4) * 512 + lane * 8); s1 += o.x; s2 += o.y; }
  }
  STAMP(1 + MODE, 13);
  const u32 sofs = (u32)(Bs * 512 + (q >> 1) * 256);
  const int gq = (q & 1) * 2 + (q >> 1);
  if (MODE == 1 || MODE == 3) {
    pin(csw[0]); if (MODE == 3) pin(csw[1]);
    float a = s1, bq = s2;
    if (MODE == 3) {
#pragma unroll
      for (int t = 0; t < 4; ++t) { pin(psv[t]); a += __uint_as_float(psv[t].x); bq += __uint_as_float(psv[t].y); }
      a = qsum(a); bq = qsum(bq);
    }
    const float m = a * (1.0f / D);
    const float var = fmaxf(bq * (1.0f / D) - m * m, 0.f);
    const float r = __builtin_amdgcn_rsqf(var + 1e-5f);
    float rr = r;
    if (MODE == 1) { rr = r * __builtin_amdgcn_rsqf(var * r * r + 1e-5f); cx.m = m; cx.r = r; }
    const float mr = -m * rr;
    const u32 tofs = (u32)(tb * (MODE == 1 ? 20480 : 54 * 1024) + c * 16);
    char* dst = ws + (MODE == 1 ? O_G1 : O_G3);
    auto act = [&](auto F_) __attribute__((always_inline)) -> u32x2 {
      constexpr int f = decltype(F_)::value;
      const f32x4 cs = {__uint_as_float(dpp<0x150 + (f & 3) * 4>(csw[f >> 2])), __uint_as_float(dpp<0x151 + (f & 3) * 4>(csw[f >> 2])),
                        __uint_as_float(dpp<0x152 + (f & 3) * 4>(csw[f >> 2])), __uint_as_float(dpp<0x153 + (f & 3) * 4>(csw[f >> 2]))};
      const f32x4 xx = acc[f] * rr + cs * mr;
      const f32x2 o0 = gelu2(f32x2{xx[0], xx[1]}), o1 = gelu2(f32x2{xx[2], xx[3]});
      return u32x2{pkbf(o0.x, o0.y), pkbf(o1.x, o1.y)};
    };
    sfor<(NE - 1) / 2>([&](auto J_) __attribute__((always_inline)) {
      constexpr int j = decltype(J_)::value;
      if (MODE == 3) { pf_piece<4>(cx, 5 + j, 0); pf_piece<4>(cx, 5 + j, 1); } else { pf_piece<3>(cx, 0, 0); pf_piece<3>(cx, 0, 1); }
      const u32x2 p0 = act(std::integral_constant<int, 2 * j>{});
      if (MODE == 3) pf_piece<4>(cx, 5 + j, 2); else { pf_piece<3>(cx, 0, 2); pf_piece<3>(cx, 0, 3); }
      const u32x2 p1 = act(std::integral_constant<int, 2 * j + 1>{});
      gstore16(dst, tofs + (u32)((Bp + 2 * j) * 512 + gq * 256), widen2(p0, p1));
    });
    if (has_s) { const u32x4 v = widen(act(std::integral_constant<int, NE - 1>{})); if (!(q & 1)) gstore16(dst, tofs + sofs, v); }
  } else if (MODE == 2) {
    const float m = cx.m, r = cx.r;
    pina(cx.hv[0]); pina(cx.hv[1]); pina(cx.hv[2]);
    u32 p[6];
#pragma unroll
    for (int k = 0; k < 6; ++k) {
      const u32 hv = cx.hv[k >> 1][k & 1];
      p[k] = pkbf((bflo(hv) - m) * r + acc[k >> 1][(k & 1) * 2], (bfhi(hv) - m) * r + acc[k >> 1][(k & 1) * 2 + 1]);
    }
#pragma unroll
    for (int k = 0; k < 3; ++k) cx.xv[k] = u32x2{p[2 * k], p[2 * k + 1]};
    gstore16(ws + O_X1 + (size_t)tb * 20480 + Bp * 512, (u32)(gq * 256 + c * 16), widen2(cx.xv[0], cx.xv[1]));
    if (has_s) { const u32x4 v = widen(cx.xv[2]); if (!(q & 1)) gstore16(ws + O_X1 + (size_t)tb * 20480, sofs + c * 16, v); }
    if (!has_s) { p[4] = 0u; p[5] = 0u; }
#pragma unroll
    for (int k = 0; k < 6; ++k) {
      s1 = __builtin_amdgcn_fdot2_f32_bf16(as_bf2(p[k]), ones, s1, false);
      s2 = __builtin_amdgcn_fdot2_f32_bf16(as_bf2(p[k]), as_bf2(p[k]), s2, false);
    }
    s1 = qsum(s1); s2 = qsum(s2);
    if (q == 0) gstore8(ws + O_PS + tb * 2048 + (ct * 2 + kh) * 128, c * 8, u32x2{__float_as_uint(s1), __float_as_uint(s2)});
  } else {
    u32 p[6];
#pragma unroll
    for (int k = 0; k < 6; ++k) {
      const u32 hv = cx.hv[k >> 1][k & 1], xv = cx.xv[k >> 1][k & 1];
      p[k] = pkbf(bflo(hv) + bflo(xv) + acc[k >> 1][(k & 1) * 2], bfhi(hv) + bfhi(xv) + acc[k >> 1][(k & 1) * 2 + 1]);
    }
    u16* orow = cx.out + (size_t)(tb * 16 + c) * D;
    *(u32x4*)(orow + Bp * 16 + gq * 8) = widen2(u32x2{p[0], p[1]}, u32x2{p[2], p[3]});
    if (has_s) *(u32x2*)(orow + Bs * 16 + q * 4) = u32x2{p[4], p[5]};
  }
  STAMP(1 + MODE, 15);
}

__global__ __launch_bounds__(512, 2) __attribute__((amdgpu_waves_per_eu(2, 2)))
void k_main(In in, char* __restrict__ ws, u16* __restrict__ out) {
  __shared__ __attribute__((aligned(16))) char smem[K2_LDS];
  STAMP(0, 0);
  asm volatile("" :: "s"(in.x), "s"(in.lar), "s"(in.aim), "s"(in.cre), "s"(in.cim), "s"(in.ldt), "s"(in.dsk), "s"(in.w1), "s"(in.w2), "s"(in.w3), "s"(in.w4), "s"(ws), "s"(out));
  Ctx c;
  c.ws = ws; c.out = out;
  c.lane = threadIdx.x & 63; c.w = __builtin_amdgcn_readfirstlane(threadIdx.x >> 6);
  c.sb = lds_addr(smem); c.vo = c.lane * 16;
  c.xcd = blockIdx.x & 7; c.bi = blockIdx.x >> 3;
  const int rt = c.xcd * 4 + (c.bi >> 3), ct = c.bi & 7, rt3 = c.xcd * 2 + (c.bi >> 4);
  auto setup = [&]() __attribute__((always_inline)) {
  c.Wg[1] = (const char*)(in.w1 + ct * 80 + c.w * 16 * D);
  c.Wg[2] = ws + O_W2 + (size_t)ct * (100 * KB) + c.w * 2560;
  c.Wg[3] = ws + O_W3 + (size_t)(c.bi & 15) * (140 * KB) + c.w * 3584;
  c.Wg[4] = ws + O_W4 + (size_t)ct * (270 * KB) + c.w * 2560;
#pragma unroll
  for (int i = 0; i < 3; ++i) {
    const int u = i * 64 + c.lane, r = (u / 10) & 15, cc = u % 10;
    c.wo[i] = (u32)((r ^ ((r >> 3) << 2)) * (D * 2) + cc * 16);
  }
#pragma unroll
  for (int i = 0; i < 3; ++i) pin(c.wo[i]);
  };
  const bool isconv = c.bi >= 8 && c.bi < 28;
  const int cg = c.xcd * 20 + c.bi - 8;
  const u32 epo = (c.w == 0 && c.lane >= 1 && c.lane <= 16) ? (u32)(2048 + blockIdx.x * 2048 + (c.lane - 1) * 128) : 0u;
  auto mktag = [&](u32x2& epv) __attribute__((always_inline)) {
    pin(epv);
    const unsigned long long ep = ((unsigned long long)epv.y << 32) | epv.x;
    unsigned long long t = (ep ^ (ep >> 31)) * 0x9E3779B97F4A7C15ull + 0x632BE59BD9B4E019ull;
    t ^= t >> 29;
    c.tlo = __builtin_amdgcn_readfirstlane((u32)t); c.thi = __builtin_amdgcn_readfirstlane((u32)(t >> 32));
  };
  const int tun = (c.bi < 8 ? c.bi : c.bi - 20) * 8 + c.xcd;
  if (c.bi < 8) {
    u32x4 xv[5];
    const char* xr = (const char*)(in.x + (size_t)(tun * 32 + c.w * 4) * D);
    const u32 xo = (u32)(c.lane >> 4) * (D * 2) + (u32)(c.lane & 15) * 16u;
#pragma unroll
    for (int i = 0; i < 5; ++i) xv[i] = gload16(xr, xo, i * 256);
    u32x2 epv = gload8(ws + O_EP, epo, 0);
    wait_vm<0>();
#pragma unroll
    for (int i = 0; i < 5; ++i) pin(xv[i]);
    mktag(epv);
    ln_compute(c, xv);
    WG_BARRIER();
    ln_store(c, tun);
    STAMP(0, 1);
    asm volatile("s_waitcnt vmcnt(0)\n\ts_barrier" ::: "memory");
    post(c, 160, 64 + threadIdx.x, MB_LN, tun);
    STAMP(0, 2);
  }
  if (__builtin_expect(isconv, 1)) {
    const int kch = c.w >> 1, set = c.w & 1, d = cg * 4 + kch;
    const u32 n4 = (u32)(d * NM * 2 + c.lane * 4);
    KP kp;
    kp.lar = gload4(in.lar, n4); kp.aim = gload4(in.aim, n4); kp.ldt = gload2(in.ldt, (u32)d * 2u);
    kp.cr = gload4(in.cre, n4); kp.ci = gload4(in.cim, n4);
    u32x2 dsv = gload8(in.dsk, (u32)cg * 8u, 0);
    u32x2 epv = gload8(ws + O_EP, epo, 0);
    setup();
    const u32 tab = c.sb + kch * KG_SZ;
    const KI ki = kg_index(tab, c.sb + KG_KT + kch * 2048, set, c.lane);
    u32 ix[11];
    const int gq = c.w >> 2;
    kg_lagidx(threadIdx.x & 255, c.sb + KG_KT + gq * 2048, ix);
    wait_vm<0>();
    pin(kp.lar); pin(kp.aim); pin(kp.ldt); pin(kp.cr); pin(kp.ci); pin(dsv);
    mktag(epv);
    {
      const int k = cg * 8 + c.w;
      const u16* p = k < 540 ? in.w4 : (k < 1080 ? in.w3 : in.w1);
      const int u = k < 540 ? k : (k < 1080 ? k - 540 : k - 1080);
      asm volatile("s_mov_b32 m0, %2\n\ts_nop 0\n\tglobal_load_lds_dword %0, %1" :: "v"((u32)u * 4096u + (u32)c.lane * 64u), "s"(p), "s"(c.sb + 150 * KB) : "memory");
    }
    STAMP(0, 1);
    const float ks = kg_tables(kp, tab, set, c.lane);
    if (set == 0 && c.lane == 0) lds_st(c.sb + KG_KT + 8192 + kch * 4, ks);
    WG_BARRIER();
    STAMP(0, 2);
    kg_mfma(ki);
    await(c, MB_LN, 64);
    WG_BARRIER();
    STAMP(0, 3);
    u32x4 uv0 = gload16(ws + O_UT + (size_t)cg * 16384 + c.w * 2048, c.vo, 0), uv1 = gload16(ws + O_UT + (size_t)cg * 16384 + c.w * 2048, c.vo, 1024);
    u32x4 stv[2];
#pragma unroll
    for (int i = 0; i < 2; ++i) stv[i] = gload16a(ws + O_ST0 + c.w * 2048, c.vo, i * 1024);
    u32 v0[11], v1[11];
#pragma unroll
    for (int k = 0; k < 11; ++k) { v0[k] = lds_ld<u16>(ix[k]); v1[k] = lds_ld<u16>(ix[k] + 4096); }
    SCHED_FENCE();
    kg_expand(v0, c.sb + gq * CV_CH, threadIdx.x & 255);
    kg_expand(v1, c.sb + (2 + gq) * CV_CH, threadIdx.x & 255);
    if (c.w < 4 && c.lane < 32) lds_st(c.sb + c.w * CV_CH + 4 * CV_KC + 4096 + c.lane * 16, u32x4{0u, 0u, 0u, 0u});
    const f32x4 ksc = lds_ld<f32x4>(c.sb + KG_KT + 8192);
    STAMP(1, 0);
    conv_phase(c, dsv, cg, ksc, uv0, uv1, stv);
  } else {
    u32x2 epv = gload8(ws + O_EP, epo, 0);
    if (c.bi == 30 && c.w >= 6) {
      const u32 o = (u32)c.lane * PGS;
      const char* p = c.w == 6 ? ws : (const char*)out;
      asm volatile("s_mov_b32 m0, %2\n\ts_nop 0\n\tglobal_load_lds_dword %0, %1" :: "v"(o < (c.w == 6 ? (u32)O_MB : (u32)(L * D * 2)) ? o : 0u), "s"(p), "s"(c.sb + 150 * KB) : "memory");
    }
    const int nu = (tun >= 64 || tun < 26) ? 2 : 1, u0 = tun < 64 ? 64 + tun : tun - 64, ud = tun < 64 ? 64 : 32;
    for (int k = 0; k < nu; ++k) tu_load(c, tu_of(in, ws, u0 + k * ud), c.sb + k * 40 * KB);
    setup();
    if (tun >= 94) {
      char* z = ws + (tun == 94 ? O_G1 - 20480 : O_G1 + (size_t)L * D * 2);
      for (int i = threadIdx.x; i < 1280; i += 512) gstore16(z, i * 16u, u32x4{0u, 0u, 0u, 0u});
    }
    asm volatile("s_waitcnt vmcnt(0)\n\ts_barrier" ::: "memory");
    mktag(epv);
    for (int k = 0; k < nu; ++k) tu_emit(c, tu_of(in, ws, u0 + k * ud), c.sb + k * 40 * KB, c.sb + (98 + k) * KB);
    STAMP(0, 3);
    DRAIN_BARRIER();
    post(c, 1, 248 + c.xcd, MB_AT, c.bi < 8 ? c.bi : c.bi - 20);
#pragma unroll
    for (int k = 0; k < 3; ++k) pf_chunk<1>(c, k);
    asm volatile("s_waitcnt vmcnt(0)" ::: "memory");
  }
  STAMP(1, 15);
  if (isconv) { DRAIN_BARRIER(); post(c, 1, 248 + c.xcd, MB_AC, c.bi - 8); }
  if (c.bi == 31) {
    await(c, MB_AT, 12);
#pragma unroll
    for (int k = 0; k < 4; ++k) post(c, 64, k * 64 + threadIdx.x, MB_T, c.xcd);
  }
  if (c.w) { pf7_chunk<1>(c, 3); pf7_chunk<1>(c, 4); }
  await(c, MB_T, 8);
  asm volatile("s_barrier" ::: "memory");
  if (c.w) { pf7_chunk<2>(c, 0); pf7_chunk<2>(c, 1); pf7_chunk<2>(c, 2); }
  if (c.bi == 31) {
    await(c, MB_AC, 20);
#pragma unroll
    for (int k = 0; k < 4; ++k) post(c, 64, k * 64 + threadIdx.x, MB_C, c.xcd);
  }
  await(c, MB_C, 8);
  if (blockIdx.x == 0 && threadIdx.x == 0) gstore8(ws + O_EP, 0u, u32x2{c.tlo, c.thi});
  asm volatile("s_barrier" ::: "memory");
  STAMP(2, 0);
  gemm_phase<1>(c);
  DRAIN_BARRIER();
  {
    const int t = threadIdx.x, r2 = (rt + (t >> 3) - 1) & 31;
    post(c, 24, (((r2 & 3) * 8 + (t & 7)) << 3) + (r2 >> 2), MB_1, (2 - (t >> 3)) * 8 + ct);
  }
  await(c, MB_1, 24);
  asm volatile("s_barrier" ::: "memory");
  STAMP(3, 0);
  gemm_phase<2>(c);
  DRAIN_BARRIER();
  post(c, 16, (((c.bi & 16) + threadIdx.x) << 3) + c.xcd, MB_2, c.bi & 15);
  if (c.w) { pf7_chunk<3>(c, 3); pf7_chunk<3>(c, 4); }
  await(c, MB_2, 16);
  asm volatile("s_barrier" ::: "memory");
  STAMP(4, 0);
  gemm_phase<3>(c);
  DRAIN_BARRIER();
  post(c, 16, (((c.bi & 16) + threadIdx.x) << 3) + c.xcd, MB_3, c.bi & 15);
  await(c, MB_3, 16);
  asm volatile("s_barrier" ::: "memory");
  STAMP(5, 0);
  gemm_phase<4>(c);
}

}

extern "C" void kernel_launch(void* const* d_in, const int* in_sizes, int n_in, void* d_out, int out_size, void* d_ws,
                              size_t ws_size, hipStream_t stream) {
  In in;
  in.x = (const u16*)d_in[0]; in.lar = (const u16*)d_in[1]; in.aim = (const u16*)d_in[2]; in.cre = (const u16*)d_in[3];
  in.cim = (const u16*)d_in[4]; in.ldt = (const u16*)d_in[5]; in.dsk = (const u16*)d_in[6];
  in.w1 = (const u16*)d_in[8]; in.w2 = (const u16*)d_in[9]; in.w3 = (const u16*)d_in[10]; in.w4 = (const u16*)d_in[11];
  k_main<<<256, 512, 0, stream>>>(in, (char*)d_ws, (u16*)d_out);
}
```

```cpp
#include <hip/hip_runtime.h>
#include <hip/hip_bf16.h>
#include <stdint.h>
#include <utility>

typedef unsigned short u16;
typedef unsigned u32;
typedef _Float16 f16;
typedef __attribute__((ext_vector_type(2))) _Float16 f16x2;
typedef __attribute__((ext_vector_type(8))) _Float16 f16x8;
typedef __attribute__((ext_vector_type(2))) __bf16 bf16x2;
typedef __attribute__((ext_vector_type(8))) __bf16 bf16x8;
typedef __attribute__((ext_vector_type(2))) float f32x2;
typedef __attribute__((ext_vector_type(4))) float f32x4;
typedef __attribute__((ext_vector_type(2))) unsigned u32x2;
typedef __attribute__((ext_vector_type(4))) unsigned u32x4;
typedef __attribute__((ext_vector_type(4))) short s16x4;
#define LDS_AS __attribute__((address_space(3)))

namespace {

constexpr int L = 2048, D = 640, NM = 128, F = 1728;
constexpr size_t O_UT  = 0;
constexpr size_t O_H   = O_UT  + (size_t)D * L * 2;
constexpr size_t O_G1  = O_H   + (size_t)L * D * 2 + 20480;
constexpr size_t O_X1  = O_G1  + (size_t)L * D * 2 + 20480;
constexpr size_t O_G3  = O_X1  + (size_t)L * D * 2;
constexpr size_t O_W2  = O_G3  + (size_t)L * F * 2;
constexpr size_t O_W3  = O_W2  + (size_t)D * D * 2;
constexpr size_t O_W4  = O_W3  + (size_t)16 * 20 * 7 * 1024;
constexpr size_t O_CS1 = O_W4  + (size_t)D * F * 2 + 16384;
constexpr size_t O_CS3 = O_CS1 + D * 4;
constexpr size_t O_ST0 = O_CS3 + 1792 * 4;
constexpr size_t O_PS  = O_ST0 + L * 8;
constexpr size_t O_EP  = O_PS + 128 * 2048;
constexpr size_t O_MB  = O_EP + 2048;
constexpr int MB_LN = 0, MB_1 = 512, MB_2 = 768, MB_3 = 896, MB_C = 1024, MB_T = 1152, MB_AC = 1280, MB_AT = 1536;

__device__ __forceinline__ float bf2f(u16 v) { return __uint_as_float(((u32)v) << 16); }
__device__ __forceinline__ float bflo(u32 v) { return __uint_as_float(v << 16); }
__device__ __forceinline__ float bfhi(u32 v) { return __uint_as_float(v & 0xffff0000u); }
__device__ __forceinline__ u32 pkbf(float a, float b) {
  f32x2 v = {a, b};
  return __builtin_bit_cast(u32, __builtin_convertvector(v, bf16x2));
}
__device__ __forceinline__ float rbf(float f) { return bflo(pkbf(f, 0.f)); }
__device__ __forceinline__ u32 pkh(float a, float b) {
  f32x2 v = {a, b};
  return __builtin_bit_cast(u32, __builtin_convertvector(v, f16x2));
}
__device__ __forceinline__ u32 umin(u32 a, u32 b) { return a < b ? a : b; }
__device__ __forceinline__ u32 umax(u32 a, u32 b) { return a > b ? a : b; }
__device__ __forceinline__ bf16x2 as_bf2(u32 v) { return __builtin_bit_cast(bf16x2, v); }
__device__ __forceinline__ f16x2 as_h2(u32 v) { return __builtin_bit_cast(f16x2, v); }
__device__ __forceinline__ u32 lds_addr(const void* p) { return (u32)(size_t)(LDS_AS const void*)p; }
template <class T> __device__ __forceinline__ T lds_ld(u32 a) { return *(LDS_AS const T*)(size_t)a; }
template <class T> __device__ __forceinline__ void lds_st(u32 a, T v) { *(LDS_AS T*)(size_t)a = v; }

template <bool SC1>
__device__ __forceinline__ void glds16(const void* sbase, u32 voff, int goff, u32 lbase, int loff) {
  u32 t;
  if (SC1) asm volatile("s_add_i32 m0, %4, %5\n\tv_add_u32 %0, %3, %1\n\tglobal_load_lds_dwordx4 %0, %2 sc1"
               : "=&v"(t) : "v"(voff), "s"(sbase), "i"(goff), "s"(lbase), "i"(loff) : "memory", "scc");
  else asm volatile("s_add_i32 m0, %4, %5\n\tv_add_u32 %0, %3, %1\n\tglobal_load_lds_dwordx4 %0, %2"
               : "=&v"(t) : "v"(voff), "s"(sbase), "i"(goff), "s"(lbase), "i"(loff) : "memory", "scc");
}
__device__ __forceinline__ void glds16r(const void* sbase, u32 voff, u32 goff, u32 laddr) {
  u32 t;
  asm volatile("s_nop 4\n\ts_mov_b32 m0, %4\n\tv_add_u32 %0, %3, %1\n\tglobal_load_lds_dwordx4 %0, %2"
               : "=&v"(t) : "v"(voff), "s"(sbase), "s"(goff), "s"(laddr) : "memory");
}
template <bool SC1>
__device__ __forceinline__ void glds16_half(const void* sbase, u32 voff, int goff, u32 lbase, int loff) {
  u32 t;
  if (SC1) asm volatile("s_add_i32 m0, %4, %5\n\tv_add_u32 %0, %3, %1\n\ts_mov_b32 exec_hi, 0\n\tglobal_load_lds_dwordx4 %0, %2 sc1\n\ts_mov_b32 exec_hi, -1"
               : "=&v"(t) : "v"(voff), "s"(sbase), "i"(goff), "s"(lbase), "i"(loff) : "memory", "scc");
  else asm volatile("s_add_i32 m0, %4, %5\n\tv_add_u32 %0, %3, %1\n\ts_mov_b32 exec_hi, 0\n\tglobal_load_lds_dwordx4 %0, %2\n\ts_mov_b32 exec_hi, -1"
               : "=&v"(t) : "v"(voff), "s"(sbase), "i"(goff), "s"(lbase), "i"(loff) : "memory", "scc");
}
__device__ __forceinline__ u32x4 gload16(const void* sbase, u32 voff, int goff) {
  u32x4 r; u32 t;
  asm volatile("v_add_u32 %1, %4, %2\n\tglobal_load_dwordx4 %0, %1, %3 sc1" : "=v"(r), "=&v"(t) : "v"(voff), "s"(sbase), "i"(goff) : "memory");
  return r;
}
__device__ __forceinline__ u32x2 gload8(const void* sbase, u32 voff, int goff) {
  u32x2 r; u32 t;
  asm volatile("v_add_u32 %1, %4, %2\n\tglobal_load_dwordx2 %0, %1, %3 sc1" : "=v"(r), "=&v"(t) : "v"(voff), "s"(sbase), "i"(goff) : "memory");
  return r;
}
__device__ __forceinline__ u32x4 gload16a(const void* sbase, u32 voff, int goff) {
  u32x4 r; u32 t;
  asm volatile("v_add_u32 %1, %4, %2\n\tglobal_load_dwordx4 %0, %1, %3 sc1" : "=a"(r), "=&v"(t) : "v"(voff), "s"(sbase), "i"(goff) : "memory");
  return r;
}
__device__ __forceinline__ u32x2 gload8a(const void* sbase, u32 voff, int goff) {
  u32x2 r; u32 t;
  asm volatile("v_add_u32 %1, %4, %2\n\tglobal_load_dwordx2 %0, %1, %3 sc1" : "=a"(r), "=&v"(t) : "v"(voff), "s"(sbase), "i"(goff) : "memory");
  return r;
}
template <bool HALF>
__device__ __forceinline__ void glds16s(const void* sbase, u32 voff, u32 soff, u32 lbase, int loff) {
  u32 t;
  if (HALF) asm volatile("s_add_i32 m0, %4, %5\n\tv_add_u32 %0, %3, %1\n\ts_mov_b32 exec_hi, 0\n\tglobal_load_lds_dwordx4 %0, %2 sc1\n\ts_mov_b32 exec_hi, -1"
               : "=&v"(t) : "v"(voff), "s"(sbase), "s"(soff), "s"(lbase), "i"(loff) : "memory", "scc");
  else asm volatile("s_add_i32 m0, %4, %5\n\tv_add_u32 %0, %3, %1\n\tglobal_load_lds_dwordx4 %0, %2 sc1"
               : "=&v"(t) : "v"(voff), "s"(sbase), "s"(soff), "s"(lbase), "i"(loff) : "memory", "scc");
}
__device__ __forceinline__ u32x4 gload16as(const void* sbase, u32 voff, u32 soff, int ioff) {
  u32x4 r; u32 t;
  asm volatile("v_add_u32 %1, %4, %2\n\tglobal_load_dwordx4 %0, %1, %3 offset:%5 sc1" : "=a"(r), "=&v"(t) : "v"(voff), "s"(sbase), "s"(soff), "i"(ioff) : "memory");
  return r;
}
__device__ __forceinline__ u32x2 gload8s(const void* sbase, u32 voff, u32 soff, int ioff) {
  u32x2 r; u32 t;
  asm volatile("v_add_u32 %1, %4, %2\n\tglobal_load_dwordx2 %0, %1, %3 offset:%5 sc1" : "=v"(r), "=&v"(t) : "v"(voff), "s"(sbase), "s"(soff), "i"(ioff) : "memory");
  return r;
}
__device__ __forceinline__ u32x4 gload16ap(const void* sbase, u32 voff) {
  u32x4 r;
  asm volatile("global_load_dwordx4 %0, %1, %2" : "=a"(r) : "v"(voff), "s"(sbase) : "memory");
  return r;
}
__device__ __forceinline__ void gstore16(void* sbase, u32 voff, u32x4 v) {
  asm volatile("global_store_dwordx4 %0, %1, %2 sc1\n\ts_nop 1" :: "v"(voff), "v"(v), "s"(sbase) : "memory");
}
__device__ __forceinline__ void gstore8(void* sbase, u32 voff, u32x2 v) {
  asm volatile("global_store_dwordx2 %0, %1, %2 sc1\n\ts_nop 0" :: "v"(voff), "v"(v), "s"(sbase) : "memory");
}
__device__ __forceinline__ void gstore4(void* sbase, u32 voff, u32 v) {
  asm volatile("global_store_dword %0, %1, %2 sc1\n\ts_nop 0" :: "v"(voff), "v"(v), "s"(sbase) : "memory");
}
__device__ __forceinline__ void reload16(u32x4& r, const void* sbase, u32 voff, int goff) {
  u32 t;
  asm volatile("v_add_u32 %1, %4, %2\n\tglobal_load_dwordx4 %0, %1, %3 sc1\n\ts_waitcnt vmcnt(0)" : "+v"(r), "=&v"(t) : "v"(voff), "s"(sbase), "i"(goff) : "memory");
}
__device__ __forceinline__ void reload8(u32x2& r, const void* sbase, u32 voff, int goff) {
  u32 t;
  asm volatile("v_add_u32 %1, %4, %2\n\tglobal_load_dwordx2 %0, %1, %3 sc1\n\ts_waitcnt vmcnt(0)" : "+v"(r), "=&v"(t) : "v"(voff), "s"(sbase), "i"(goff) : "memory");
}
__device__ __forceinline__ u32 mx4(u32x4 v) { return umax(umax(v[0], v[1]), umax(v[2], v[3])); }
__device__ __forceinline__ u32 mx2(u32x2 v) { return umax(v[0], v[1]); }
__device__ __forceinline__ bool unset(u32 m) { return __any(m == 0xffffffffu); }
__device__ __forceinline__ u32 san(u32 p) { return umin(p, 0xfffffffeu); }
constexpr int SPIN = 1 << 16;
__device__ __forceinline__ float qsum(float a) {
  auto r = __builtin_amdgcn_permlane16_swap(__float_as_uint(a), __float_as_uint(a), false, false);
  a = __uint_as_float(r[0]) + __uint_as_float(r[1]);
  auto r2 = __builtin_amdgcn_permlane32_swap(__float_as_uint(a), __float_as_uint(a), false, false);
  return __uint_as_float(r2[0]) + __uint_as_float(r2[1]);
}
template <class T> __device__ __forceinline__ void pin(T& x) { asm volatile("" : "+v"(x)); }
template <class T> __device__ __forceinline__ void pina(T& x) { asm volatile("" : "+a"(x)); }
#define STAMP(kid, idx)
template <int N> __device__ __forceinline__ void wait_vm() { asm volatile("s_waitcnt vmcnt(%0)" ::"i"(N) : "memory"); }
#define SCHED_FENCE() __builtin_amdgcn_sched_barrier(0)
template <class Fn, int... I> __device__ __forceinline__ void sfor_impl(std::integer_sequence<int, I...>, Fn&& f) {
  (f(std::integral_constant<int, I>{}), ...);
}
template <int N, class Fn> __device__ __forceinline__ void sfor(Fn&& f) { sfor_impl(std::make_integer_sequence<int, N>{}, f); }
template <int N> __device__ __forceinline__ void wait_vm_bar() { asm volatile("s_waitcnt vmcnt(%0)\n\ts_barrier" ::"i"(N) : "memory"); }

__device__ __forceinline__ f32x2 gelu2(f32x2 x) {
  const float c0 = -2.0f * 0.7978845608028654f * 1.4426950408889634f;
  const float c1 = c0 * 0.044715f;
  const f32x2 z = x * __builtin_elementwise_fma(x * x, f32x2{c1, c1}, f32x2{c0, c0});
  const f32x2 d = f32x2{__builtin_amdgcn_exp2f(z.x), __builtin_amdgcn_exp2f(z.y)} + 1.0f;
  return x * f32x2{__builtin_amdgcn_rcpf(d.x), __builtin_amdgcn_rcpf(d.y)};
}
__device__ __forceinline__ u32x4 widen2(u32x2 p0, u32x2 p1);
__device__ __forceinline__ u32x4 widen(u32x2 p) {
  auto a = __builtin_amdgcn_permlane16_swap(p.x, p.x, false, false);
  auto b = __builtin_amdgcn_permlane16_swap(p.y, p.y, false, false);
  return u32x4{(u32)a[0], (u32)b[0], (u32)a[1], (u32)b[1]};
}

__device__ __forceinline__ u32x4 widen2(u32x2 p0, u32x2 p1) {
  auto a = __builtin_amdgcn_permlane16_swap(p0.x, p1.x, false, false);
  auto b = __builtin_amdgcn_permlane16_swap(p0.y, p1.y, false, false);
  return u32x4{(u32)a[0], (u32)b[0], (u32)a[1], (u32)b[1]};
}
__device__ __forceinline__ f32x2 cmul(f32x2 a, f32x2 b) {
  f32x2 r;
  asm("v_pk_mul_f32 %0, %1, %2 op_sel_hi:[1,0]\n\t"
      "v_pk_fma_f32 %0, %1, %2, %0 op_sel:[1,1,0] op_sel_hi:[0,1,1] neg_lo:[1,0,0]" : "=&v"(r) : "v"(a), "v"(b));
  return r;
}
__device__ __forceinline__ u32 cmulh(u32 a, u32 b) {
  u32 r;
  asm("v_pk_mul_f16 %0, %1, %2 op_sel_hi:[1,0]\n\t"
      "v_pk_fma_f16 %0, %1, %2, %0 op_sel:[1,1,0] op_sel_hi:[0,1,1] neg_lo:[1,0,0]" : "=&v"(r) : "v"(a), "v"(b));
  return r;
}
template <int CTRL> __device__ __forceinline__ u32 dpp(u32 v) { return (u32)__builtin_amdgcn_update_dpp(0, (int)v, CTRL, 0xf, 0xf, true); }
__device__ __forceinline__ float rowsum16(float v) {
  v += __uint_as_float(dpp<0xB1>(__float_as_uint(v)));
  v += __uint_as_float(dpp<0x4E>(__float_as_uint(v)));
  v += __uint_as_float(dpp<0x141>(__float_as_uint(v)));
  v += __uint_as_float(dpp<0x140>(__float_as_uint(v)));
  return v;
}
__device__ __forceinline__ u32 wavemax(u32 v) {
  v = umax(v, dpp<0xB1>(v)); v = umax(v, dpp<0x4E>(v)); v = umax(v, dpp<0x141>(v)); v = umax(v, dpp<0x140>(v));
  auto r = __builtin_amdgcn_permlane16_swap(v, v, false, false);
  v = umax((u32)r[0], (u32)r[1]);
  auto r2 = __builtin_amdgcn_permlane32_swap(v, v, false, false);
  return umax((u32)r2[0], (u32)r2[1]);
}
__device__ __forceinline__ f32x2 cexpp(float rho, float th, float p) {
  float mag = __builtin_amdgcn_exp2f(rho * p);
  float ph = __builtin_amdgcn_fractf(th * p);
  return f32x2{mag * __builtin_amdgcn_cosf(ph), mag * __builtin_amdgcn_sinf(ph)};
}

#define PGS (512u * 1024u)
constexpr int CV_KC = 4160;
constexpr int CV_U = 4608;
constexpr int CV_CH = 4 * CV_KC + CV_U + 32;
constexpr int K2_LDS = 160 * 1024;
constexpr int KB = 1024;

__device__ __forceinline__ u32x2 dsr64(u32 a, int off) {
  u32x2 r;
  asm volatile("ds_read_b64 %0, %1 offset:%2" : "=v"(r) : "v"(a), "i"(off));
  return r;
}
__device__ __forceinline__ u32x4 dsr128(u32 a, int off) {
  u32x4 r;
  asm volatile("ds_read_b128 %0, %1 offset:%2" : "=a"(r) : "v"(a), "i"(off));
  return r;
}

constexpr int wpos(int m, int ch) {
  return m == 1 ? (ch < 3 ? (100 + 20 * ch) * KB : 20 * (ch - 3) * KB)
       : m == 2 ? (40 + 20 * ch) * KB
       : m == 3 ? 28 * ch * KB
       : ((ch + 7) & 7) * 20 * KB;
}

struct Ctx {
  char* ws; u16* out;
  u32 sb, vo;
  int lane, w, xcd, bi;
  u32 tlo, thi;
  const char* Wg[5];
  u32 wo[3];

  float m, r;
  u32x2 hv[3], xv[3];
};

template <int M> __device__ __forceinline__ void pf_piece(const Ctx& c, int ch, int i) {
  constexpr int CHB = (M == 3 ? 28 : 20) * KB, NW = M == 3 ? 4 : 3;
  const u32 wl = c.sb + c.w * (CHB / 8);
  const u32 v = M == 1 ? c.wo[i] : c.vo;
  const int go = M == 1 ? ch * 128 * D * 2 : ch * CHB + i * 1024;
  if (i == NW - 1) glds16_half<(M != 1)>(c.Wg[M], v, go, wl, wpos(M, ch) + i * 1024);
  else glds16<(M != 1)>(c.Wg[M], v, go, wl, wpos(M, ch) + i * 1024);
}
template <int M> __device__ __forceinline__ void pf_chunk(const Ctx& c, int ch) {
#pragma unroll
  for (int i = 0; i < (M == 3 ? 4 : 3); ++i) pf_piece<M>(c, ch, i);
}

template <int M> __device__ __forceinline__ void pf7_chunk(const Ctx& c, int ch) {
  constexpr int CHB = (M == 3 ? 28 : 20) * KB, NU = M == 3 ? 4 : 3;
  const char* base = c.Wg[M] - (M == 1 ? c.w * 16 * D * 2 : c.w * (CHB / 8));
#pragma unroll
  for (int i = 0; i < NU; ++i) {
    int u = (c.w - 1) * NU + i; u = u < CHB / 1024 ? u : CHB / 1024 - 1;
    u32 v = c.vo + u * 1024;
    if (M == 1) { const int x = u * 64 + c.lane, r = x / 10, cc = x - r * 10; v = (u32)((r ^ ((r >> 1) & 4)) * (D * 2) + cc * 16); }
    glds16<(M != 1)>(base, v, M == 1 ? ch * 128 * D * 2 : ch * CHB, c.sb + u * 1024, wpos(M, ch));
  }
}
#define DRAIN_BARRIER() asm volatile("s_waitcnt vmcnt(0) lgkmcnt(0)\n\ts_barrier" ::: "memory")
__device__ __forceinline__ void post(const Ctx& c, int n, int wg, int sec, int slot) {
  if ((int)threadIdx.x < n) gstore8(c.ws + O_MB, (u32)(wg * 2048 + sec + slot * 8), u32x2{c.tlo, c.thi});
}
__device__ __forceinline__ void await(const Ctx& c, int sec, int n) {
  if (c.w == 0) {
    const u32 off = (u32)(sec + (c.lane < n ? c.lane : 0) * 8);
    for (int sp = 0; sp < SPIN; ++sp) {
      u32x2 v;
      asm volatile("global_load_dwordx2 %0, %1, %2 sc1\n\ts_waitcnt vmcnt(0)" : "=v"(v) : "v"(off), "s"(c.ws + O_MB + (size_t)blockIdx.x * 2048) : "memory");
      if (__all(v.x == c.tlo && v.y == c.thi)) break;
    }
  }
}
#define WG_BARRIER() asm volatile("s_waitcnt lgkmcnt(0)\n\ts_barrier" ::: "memory")

struct In { const u16 *x, *lar, *aim, *cre, *cim, *ldt, *dsk, *w1, *w2, *w3, *w4; };

constexpr int LN_T = 100 * KB, LN_P = 1280 + 16, LN_ST = LN_T + 32 * LN_P;
__device__ __forceinline__ void ln_compute(const Ctx& c, u32x4 (&v)[5]) {
  const int row = c.w * 4 + (c.lane >> 4), i = c.lane & 15;
  const bf16x2 ones = {(__bf16)1.0f, (__bf16)1.0f};
  float s1 = 0.f, s2 = 0.f;
#pragma unroll
  for (int s = 0; s < 5; ++s)
#pragma unroll
    for (int k = 0; k < 4; ++k) {
      s1 = __builtin_amdgcn_fdot2_f32_bf16(as_bf2(v[s][k]), ones, s1, false);
      s2 = __builtin_amdgcn_fdot2_f32_bf16(as_bf2(v[s][k]), as_bf2(v[s][k]), s2, false);
    }
  const float m = rowsum16(s1) * (1.0f / D);
  const float ve = fmaxf(rowsum16(s2) * (1.0f / D) - m * m, 0.f) + 1e-5f;
  const float r = __builtin_amdgcn_rsqf(ve), mr = -m * r;
  if (i == 0) lds_st(c.sb + LN_ST + row * 8, f32x2{m, ve * r});
#pragma unroll
  for (int s = 0; s < 5; ++s) {
    u32x4 o;
#pragma unroll
    for (int k = 0; k < 4; ++k) o[k] = pkh(bflo(v[s][k]) * r + mr, bfhi(v[s][k]) * r + mr);
    lds_st(c.sb + LN_T + row * LN_P + (i + 16 * s) * 16, o);
  }
}
__device__ __forceinline__ void ln_store(const Ctx& c, int j) {
  const int lane = c.lane, gi = lane >> 4, qq = (lane & 15) >> 2, p = lane & 3;
  char* dst = c.ws + O_UT + (size_t)(252 - 4 * j) * 64;
#pragma unroll
  for (int it = 0; it < 5; ++it) {
    const int cgp = (it * 8 + c.w) * 4 + gi;
    const u32 base = c.sb + LN_T + (31 - 8 * p - qq) * LN_P + cgp * 8;
    s16x4 a = __builtin_amdgcn_ds_read_tr16_b64_v4i16((LDS_AS s16x4*)(size_t)base);
    s16x4 b = __builtin_amdgcn_ds_read_tr16_b64_v4i16((LDS_AS s16x4*)(size_t)(base - 4 * LN_P));
    u32x2 ua = __builtin_bit_cast(u32x2, a), ub = __builtin_bit_cast(u32x2, b);
    gstore16(dst, (u32)(cgp * 16384 + (lane & 15) * 16), u32x4{ua.x, ua.y, ub.x, ub.y});
  }
  if (threadIdx.x < 16) gstore16(c.ws + O_ST0 + j * 256, threadIdx.x * 16, lds_ld<u32x4>(c.sb + LN_ST + threadIdx.x * 16));
}

struct TU { const u16* W; char* Wt; int N, KS, sb2, strip, ks0, nks, wk; };
__device__ __forceinline__ TU tu_of(const In& in, char* ws, int u) {
  TU t;
  if (u < 20)      t = TU{in.w2, ws + O_W2, D, 20, 2, u >> 1, (u & 1) * 10, 10, 0};
  else if (u < 74) t = TU{in.w3, ws + O_W3, F, 20, 1, u - 20, 0, 20, 1};
  else if (u < 134) t = TU{in.w4, ws + O_W4, D, 54, 2, (u - 74) / 6, ((u - 74) % 6) * 9, 9, 0};
  else             t = TU{in.w1, nullptr, D, 20, 1, u - 134, 0, 20, 2};
  return t;
}
__device__ __forceinline__ void tu_load(const Ctx& c, const TU& t, u32 lds) {
  const int lpr2 = t.sb2 + 1;
  const char* src = (const char*)(t.W + (size_t)t.ks0 * 32 * t.N + (t.strip << (t.sb2 + 4)));
  const u32 voff = (u32)(c.lane >> lpr2) * (u32)t.N * 2u + (u32)(c.lane & ((1 << lpr2) - 1)) * 16u;
  const int ni = t.nks << t.sb2;
  const u32 step = (u32)(64 >> lpr2) * (u32)t.N * 2u;
#pragma unroll
  for (int j = 0; j < 5; ++j) {
    int i = c.w + j * 8; i = i < ni ? i : c.w;
    glds16r(src, voff, (u32)i * step, lds + i * 1024);
  }
}
__device__ __forceinline__ void tu_emit(const Ctx& c, const TU& t, u32 lds, u32 red) {
  const int lane = c.lane, w = c.w;
  const int RB = 32 << t.sb2;
  const int e = w & ((1 << t.sb2) - 1);
  const int g = lane >> 4, q = (lane & 15) >> 2, p = lane & 3;
  const int B = (t.strip << t.sb2) + e;
  int ct, f;
  if (t.wk == 1) { const int hh = B >= 54 ? 1 : 0, b2 = B - hh * 54, c8 = b2 < 48 ? b2 / 7 : 7; ct = hh * 8 + c8; f = c8 <= 6 ? b2 - c8 * 7 : b2 - 48; }
  else { ct = B / 5; f = B - ct * 5; }
  const int kstr = t.wk == 1 ? 7168 : 5120;
  char* dst = t.Wt + (size_t)(ct * t.KS + t.ks0) * kstr + f * 1024;
  const int k0 = w >> t.sb2, kst = 8 >> t.sb2;
  float acc = 0.f;
  const bf16x2 ones = {(__bf16)1.0f, (__bf16)1.0f};
  u32x4 v[5];
#pragma unroll
  for (int j = 0; j < 5; ++j) {
    int ks = k0 + j * kst; ks = ks < t.nks ? ks : k0;
    const u32 base = lds + (ks * 32 + 8 * g + q) * RB + e * 32 + p * 8;
    s16x4 a = __builtin_amdgcn_ds_read_tr16_b64_v4i16((LDS_AS s16x4*)(size_t)base);
    s16x4 b = __builtin_amdgcn_ds_read_tr16_b64_v4i16((LDS_AS s16x4*)(size_t)(base + 4 * RB));
    u32x2 ua = __builtin_bit_cast(u32x2, a), ub = __builtin_bit_cast(u32x2, b);
    v[j] = u32x4{ua.x, ua.y, ub.x, ub.y};
  }
#pragma unroll
  for (int j = 0; j < 5; ++j) {
    int ks = k0 + j * kst; ks = ks < t.nks ? ks : k0;
    if (t.wk != 2) gstore16(dst, (u32)(ks * kstr + lane * 16), v[j]);
#pragma unroll
    for (int k = 0; k < 4; ++k) acc = __builtin_amdgcn_fdot2_f32_bf16(as_bf2(v[j][k]), ones, acc, false);
  }
  if (t.wk) {
    acc = qsum(acc);
    if (lane < 16) lds_st(red + (w * 16 + lane) * 4, acc);
    WG_BARRIER();
    if (threadIdx.x < 32) {
      const u32 a = red + threadIdx.x * 4;
      gstore4(c.ws + (t.wk == 1 ? O_CS3 : O_CS1) + t.strip * 128, threadIdx.x * 4,
              __float_as_uint((lds_ld<float>(a) + lds_ld<float>(a + 128)) + (lds_ld<float>(a + 256) + lds_ld<float>(a + 384))));
    }
  }
}

constexpr int KG_SZ = 28 * KB, KG_KT = 112 * KB, TP = 528;
struct KP { u32 ldt, lar, aim, cr, ci; };
__device__ __forceinline__ u32 gload2(const void* sbase, u32 voff) {
  u32 r;
  asm volatile("global_load_ushort %0, %1, %2" : "=v"(r) : "v"(voff), "s"(sbase) : "memory");
  return r;
}
__device__ __forceinline__ u32 gload4s(const void* sbase, u32 voff) {
  u32 r;
  asm volatile("global_load_dword %0, %1, %2 sc1" : "=v"(r) : "v"(voff), "s"(sbase) : "memory");
  return r;
}
__device__ __forceinline__ u32 gload4(const void* sbase, u32 voff) {
  u32 r;
  asm volatile("global_load_dword %0, %1, %2" : "=v"(r) : "v"(voff), "s"(sbase) : "memory");
  return r;
}
__device__ __forceinline__ void kg_setA(float rho, float th, f32x2 C, u32 (&a)[21]) {
  f32x2 A[8];
  A[0] = C;
  const f32x2 E1 = cexpp(rho, th, 1.f);
  const f32x2 G1 = cexpp(rho, th, 256.f);
  const f32x2 G4 = cexpp(rho, th, 1024.f);
  const f32x2 E2 = cmul(E1, E1), E4 = cmul(E2, E2);
  A[1] = cmul(A[0], E1); A[2] = cmul(A[0], E2); A[3] = cmul(A[1], E2);
  A[4] = cmul(A[0], E4); A[5] = cmul(A[1], E4); A[6] = cmul(A[2], E4); A[7] = cmul(A[3], E4);
  const f32x2 G2 = cmul(G1, G1), G3 = cmul(G2, G1), A16 = cmul(G4, A[0]);
#pragma unroll
  for (int k = 0; k < 8; ++k) a[k] = pkh(A[k].x, A[k].y);
  const u32 g1 = pkh(G1.x, G1.y), g2 = pkh(G2.x, G2.y), g3 = pkh(G3.x, G3.y), g4 = pkh(G4.x, G4.y);
  a[16] = pkh(A16.x, A16.y);
  a[8] = cmulh(g1, a[0]); a[9] = cmulh(g1, a[2]); a[10] = cmulh(g1, a[4]); a[11] = cmulh(g1, a[6]);
  a[12] = cmulh(g2, a[0]); a[13] = cmulh(g2, a[4]); a[14] = cmulh(g3, a[0]); a[15] = cmulh(g3, a[4]);
  a[17] = cmulh(g1, a[16]); a[18] = cmulh(g2, a[16]); a[19] = cmulh(g3, a[16]); a[20] = cmulh(g4, a[16]);
}
__device__ __forceinline__ void kg_setB(float rho, float th, u32 (&b)[32]) {
  f32x2 lo[8], hi[4];
  lo[1] = cexpp(rho, -th, 8.f);
  hi[1] = cexpp(rho, -th, 64.f);
  lo[2] = cmul(lo[1], lo[1]); lo[3] = cmul(lo[2], lo[1]); lo[4] = cmul(lo[2], lo[2]);
  lo[5] = cmul(lo[4], lo[1]); lo[6] = cmul(lo[4], lo[2]); lo[7] = cmul(lo[4], lo[3]);
  hi[2] = cmul(hi[1], hi[1]); hi[3] = cmul(hi[2], hi[1]);
  b[0] = 0x00003c00u;
#pragma unroll
  for (int k = 1; k < 8; ++k) b[k] = pkh(lo[k].x, lo[k].y);
#pragma unroll
  for (int j = 1; j < 4; ++j) {
    b[8 * j] = pkh(hi[j].x, hi[j].y);
#pragma unroll
    for (int k = 1; k < 8; ++k) b[8 * j + k] = cmulh(b[8 * j], b[k]);
  }
}
__device__ __forceinline__ float kg_tables(const KP& p, u32 tab, int set, int lane) {
  const float LOG2E = 1.4426950408889634f;
  const int sh = set ? 0 : 16;
  const float dt = rbf(__builtin_amdgcn_exp2f(bflo(p.ldt) * LOG2E));
  const float rho = (-rbf(__builtin_amdgcn_exp2f(bfhi(p.lar << sh) * LOG2E)) * dt) * LOG2E;
  const float th = (bfhi(p.aim << sh) * dt) * 0.15915494309189535f;
  const u32 mx = wavemax(umax(umax(p.cr & 0x7fffu, (p.cr >> 16) & 0x7fffu), umax(p.ci & 0x7fffu, (p.ci >> 16) & 0x7fffu)));
  u32 eb = (mx >> 7) + 1u;
  eb = eb < 2u ? 2u : (eb > 252u ? 252u : eb);
  const float sc = __uint_as_float(eb << 23), isc = __uint_as_float((254u - eb) << 23);
  const u32 ta = tab + set * 256 + lane * 4;
  u32 a[21], b[32];
  kg_setA(rho, th, f32x2{bfhi(p.cr << sh) * isc, bfhi(p.ci << sh) * isc}, a);
  kg_setB(rho, th, b);
#pragma unroll
  for (int k = 0; k < 21; ++k) asm volatile("ds_write_b32 %0, %1 offset:%2" :: "v"(ta), "v"(a[k]), "i"(k * TP) : "memory");
#pragma unroll
  for (int k = 0; k < 32; ++k) asm volatile("ds_write_b32 %0, %1 offset:%2" :: "v"(ta), "v"(b[k]), "i"((21 + k) * TP) : "memory");
  return 2.0f * dt * sc;
}
struct KI { u32 pa, pb, o[4], ob; };
__device__ __forceinline__ KI kg_index(u32 tab, u32 ktb, int at, int lane) {
  const int r = lane & 15, g = lane >> 4;
  int ra = at * 16 + r; ra = ra > 20 ? 20 : ra;
  KI k;
  k.pa = tab + ra * TP + g * 16; k.pb = tab + (21 + r) * TP + g * 16;
  const int sbs = at == 0 ? (g < 2 ? 8 : (g == 2 ? 4 : 2)) : 1;
  const int base = at == 0 ? (g < 2 ? 4 * g : (g == 2 ? 256 : 384)) : (g == 0 ? 512 : 640 + (g > 1 ? 64 : 0));
  const int sk = at == 0 ? 1 : (g == 0 ? 32 : 64), x2 = (at == 0 && g == 3) ? 62 : 0;
  const u32 o = ktb + (base + r * sbs) * 2;
  k.o[0] = o; k.o[1] = o + sk * 2; k.o[2] = o + (2 * sk + x2) * 2; k.o[3] = o + (3 * sk + x2) * 2;
  k.ob = sbs * 32;
  pin(k.pa); pin(k.pb); pin(k.o[0]); pin(k.o[1]); pin(k.o[2]); pin(k.o[3]); pin(k.ob);
  return k;
}
__device__ __forceinline__ void kg_mfma(const KI& k) {
  f32x4 acc0 = {0.f, 0.f, 0.f, 0.f}, acc1 = acc0;
  f16x8 a[8], b0[8], b1[8];
#pragma unroll
  for (int s = 0; s < 8; ++s) { a[s] = lds_ld<f16x8>(k.pa + s * 64); b0[s] = lds_ld<f16x8>(k.pb + s * 64); b1[s] = lds_ld<f16x8>(k.pb + 16 * TP + s * 64); }
  SCHED_FENCE();
#pragma unroll
  for (int s = 0; s < 8; ++s) {
    acc0 = __builtin_amdgcn_mfma_f32_16x16x32_f16(a[s], b0[s], acc0, 0, 0, 0);
    acc1 = __builtin_amdgcn_mfma_f32_16x16x32_f16(a[s], b1[s], acc1, 0, 0, 0);
  }
  asm volatile("s_nop 7\n\ts_nop 7" : "+a"(acc0), "+a"(acc1));
#pragma unroll
  for (int e = 0; e < 4; ++e) { lds_st(k.o[e], (f16)acc0[e]); lds_st(k.o[e] + k.ob, (f16)acc1[e]); }
}
__device__ __forceinline__ void kg_lagidx(int t, u32 kt, u32 (&ix)[11]) {
#pragma unroll
  for (int k = 0; k < 11; ++k) {
    const int lag = t * 8 + k;
    const int tt = k < 8 ? t : t + 1;
    const int s = tt < 32 ? 0 : (tt < 64 ? 1 : (tt < 128 ? 2 : 3));
    const int add = ((1 << s) >> 1) - (s > 0 ? 1 : 0);
    ix[k] = kt + (128 * s + ((lag + add + ((lag >> s) & (s > 0 ? 1 : 0))) >> s)) * 2;
    pin(ix[k]);
  }
}
__device__ __forceinline__ void kg_expand(const u32 (&v)[11], u32 kc, int t) {
  u32 e[5], o[5];
#pragma unroll
  for (int k = 0; k < 5; ++k) { e[k] = v[2 * k] | (v[2 * k + 1] << 16); o[k] = v[2 * k + 1] | (v[2 * k + 2] << 16); }
  const u32 a = kc + 32 + t * 16;
  lds_st(a, u32x4{e[0], e[1], e[2], e[3]});
  lds_st(a + CV_KC, u32x4{o[0], o[1], o[2], o[3]});
  lds_st(a + 2 * CV_KC, u32x4{e[1], e[2], e[3], e[4]});
  lds_st(a + 3 * CV_KC, u32x4{o[1], o[2], o[3], o[4]});
  if (t == 0) {
    const u32x4 z = {0u, 0u, 0u, 0u};
#pragma unroll
    for (int cc = 0; cc < 4; ++cc) lds_st(kc + cc * CV_KC, z);
    lds_st(kc + 16, z);
    lds_st(kc + CV_KC + 16, u32x4{0u, 0u, 0u, v[0] << 16});
    lds_st(kc + 2 * CV_KC + 16, u32x4{0u, 0u, 0u, e[0]});
    lds_st(kc + 3 * CV_KC + 16, u32x4{0u, 0u, v[0] << 16, o[0]});
  }
}

__device__ __forceinline__ void conv_phase(Ctx& c, u32x2 dsv, int cg, f32x4 ksc, u32x4 uv0, u32x4 uv1, u32x4 (&stv)[2]) {
  char* ws = c.ws;
  const int lane = c.lane, w = c.w;
  const int ch = w & 3, hh = w >> 2;
  const u32 sb = c.sb;
  const u32 mya = sb + ch * CV_CH;
  const int r = lane & 15, g = lane >> 4;
  wait_vm<2>();
  pin(uv0); pin(uv1);
  {
    const u32 ua = sb + (lane & 3) * CV_CH + 4 * CV_KC + (w * 32 + (lane >> 2)) * 16;
    lds_st(ua, uv0); lds_st(ua + 256, uv1);
  }
  WG_BARRIER();
  STAMP(1, 2);

  const u32 pa = mya + ((r + 1) & 3) * CV_KC + (((r + 1) & ~3) + 8 * g) * 2 + hh * 256;
  const u32 pb = mya + 4 * CV_KC + (2032 - 16 * r + 8 * g) * 2 - 7 * 512 + hh * 256;
  f32x4 acc[8];
#pragma unroll
  for (int i = 0; i < 8; ++i) { acc[i] = f32x4{0.f, 0.f, 0.f, 0.f}; pina(acc[i]); }
  u32x2 Al[2][8], Ah[2][8];
  u32x4 B[2][8], w1r[5];
#pragma unroll
  for (int a = 0; a < 8; ++a) {
    Al[0][a] = dsr64(pa, a * 512); Ah[0][a] = dsr64(pa, a * 512 + 8);
    B[0][a] = dsr128(pb, (7 - a) * 512);
    if (a == 3) asm volatile("s_waitcnt lgkmcnt(6)" ::: "memory");
  }
#pragma unroll
  for (int rho = 0; rho < 4; ++rho) {
    const int cur = rho & 1, nxt = cur ^ 1;
    asm volatile("s_waitcnt lgkmcnt(0)" ::: "memory");
#pragma unroll
    for (int a = 0; a < 8; ++a) { pin(Al[cur][a]); pin(Ah[cur][a]); pina(B[cur][a]); }
    SCHED_FENCE();
    int cnt = 0;
#pragma unroll
    for (int s = 7; s >= 0; --s)
#pragma unroll
      for (int a = 0; a <= s; ++a) {
        const u32x4 av = {Al[cur][a].x, Al[cur][a].y, Ah[cur][a].x, Ah[cur][a].y};
        asm volatile("v_mfma_f32_16x16x32_f16 %0, %1, %2, %0" : "+a"(acc[s]) : "v"(av), "a"(B[cur][s - a]));
        if (rho < 3 && cnt < 24) {
          const int i = cnt / 3, kind = cnt % 3;
          if (kind == 0) Al[nxt][i] = dsr64(pa, i * 512 + (rho + 1) * 64);
          else if (kind == 1) Ah[nxt][i] = dsr64(pa, i * 512 + (rho + 1) * 64 + 8);
          else B[nxt][i] = dsr128(pb, (7 - i) * 512 + (rho + 1) * 64);
        }
        if (rho < 3 && cnt >= 25 && cnt < 34 && (cnt - 25) % 3 == 0) pf_piece<1>(c, rho, (cnt - 25) / 3);
        if (rho == 3 && cnt < 25 && cnt % 5 == 0) {
          const int x = (w * 5 + cnt / 5) * 64 + lane, rr = x / 10, cc = x - rr * 10;
          w1r[cnt / 5] = gload16ap(c.Wg[1] - w * 16 * D * 2 + 3 * 128 * D * 2, (u32)((rr ^ ((rr >> 1) & 4)) * (D * 2) + cc * 16));
        }
        ++cnt;
        asm volatile("" : "+a"(acc[0]), "+a"(acc[1]), "+a"(acc[2]), "+a"(acc[3]), "+a"(acc[4]), "+a"(acc[5]), "+a"(acc[6]), "+a"(acc[7]));
        SCHED_FENCE();
      }
  }
  asm volatile("s_nop 7\n\ts_nop 7" : "+a"(acc[0]), "+a"(acc[1]), "+a"(acc[2]), "+a"(acc[3]), "+a"(acc[4]), "+a"(acc[5]), "+a"(acc[6]), "+a"(acc[7]));
  STAMP(1, 3);
  asm volatile("s_barrier" ::: "memory");
  {
    const u32 ex = mya + hh * 8192 + g * 256 + ((r ^ (4 * g)) * 16);
#pragma unroll
    for (int i = 0; i < 8; ++i) asm volatile("ds_write_b128 %0, %1 offset:%2" :: "v"(ex), "a"(acc[i]), "i"(i * 1024) : "memory");
  }
  WG_BARRIER();
  STAMP(1, 6);
  wait_vm<14>();
  {
    const int d0 = cg * 4;
    char* hb = ws + O_H + (d0 >> 5) * 1024 + ((d0 >> 2) & 1) * 512 + ((d0 & 31) >> 3) * 128;
    const float dk[4] = {bflo(dsv.x), bfhi(dsv.x), bflo(dsv.y), bfhi(dsv.y)};
#pragma unroll
    for (int i = 0; i < 2; ++i) {
      const int l = w * 256 + i * 128 + lane * 2;
      pina(stv[i]);
      const float m0 = __uint_as_float(stv[i][0]), sd0 = __uint_as_float(stv[i][1]);
      const float m1 = __uint_as_float(stv[i][2]), sd1 = __uint_as_float(stv[i][3]);
      float v0[4], v1[4];
      const int eg = (l >> 2) & 3;
      const u32 eo = (u32)((l >> 8) * 1024 + eg * 256 + ((((l >> 4) & 15) ^ (4 * eg)) * 16) + (l & 3) * 4);
#pragma unroll
      for (int k = 0; k < 4; ++k) {
        const f16x2 u = lds_ld<f16x2>(sb + k * CV_CH + 4 * CV_KC + (2046 - l) * 2);
        const f32x2 ya = lds_ld<f32x2>(sb + k * CV_CH + eo), yb = lds_ld<f32x2>(sb + k * CV_CH + 8192 + eo);
        v0[k] = (ya.x + yb.x) * ksc[k] + (m0 + (sd0 + dk[k]) * (float)u[1]);
        v1[k] = (ya.y + yb.y) * ksc[k] + (m1 + (sd1 + dk[k]) * (float)u[0]);
      }
      gstore16(hb, (u32)((l >> 4) * 20480 + (l & 15) * 8),
               u32x4{pkbf(v0[0], v0[1]), pkbf(v0[2], v0[3]), pkbf(v1[0], v1[1]), pkbf(v1[2], v1[3])});
    }
  }
  DRAIN_BARRIER();
  post(c, 1, 248 + c.xcd, MB_AC, c.bi - 8);
#pragma unroll
  for (int i = 0; i < 5; ++i) asm volatile("ds_write_b128 %0, %1 offset:%2" :: "v"(sb + w * 5120 + c.vo), "a"(w1r[i]), "i"(i * 1024) : "memory");
  asm volatile("s_waitcnt lgkmcnt(0)" ::: "memory");
}

template <int MODE> struct GP {
  static constexpr int NB = MODE == 3 ? 7 : 5;
  static constexpr int KS = MODE == 4 ? 54 : 20;
  static constexpr int SPC = MODE == 3 ? 4 : 2;
  static constexpr int NS = MODE == 4 ? 27 : (MODE == 3 ? 20 : 10);
  static constexpr int LPX = MODE == 1 ? 2 : 1;
  static constexpr int A0 = MODE == 1 ? 3 : 4;
  static constexpr int AH = MODE <= 2 ? 6 : 10;
  static constexpr int hi(int s) {
    int h = A0 + 2 * s; h = h < s + AH ? h : s + AH; return h < NS ? h : NS;
  }
  static constexpr int pfm(int s) {
    return MODE == 1 ? ((s == 6 || s == 7) ? 2 : 0) : MODE == 2 ? ((s == 6 || s == 7) ? 3 : 0)
         : MODE == 3 ? ((s == 6 || s == 8 || s == 10 || s == 12 || s == 14) ? 4 : 0)
         : ((s >= 4 && s <= 14 && !(s & 1)) ? 4 : 0);
  }
  static constexpr int pfc(int s) { return MODE == 1 ? s - 3 : MODE == 2 ? s - 5 : MODE == 3 ? (s - 6) / 2 : 8 + (s - 4) / 2; }
  static constexpr int npf(int s) { return pfm(s) == 0 ? 0 : (pfm(s) == 3 ? 4 : 3); }
  static constexpr int nx(int s) { return (hi(s + 1) - hi(s)) * LPX; }
  static constexpr int before(int s) { int n = A0 * LPX; for (int i = 0; i < s; ++i) n += nx(i) + npf(i); return n; }
  static constexpr int xend(int x) {
    if (x < A0) return (x + 1) * LPX;
    int s = 0; while (hi(s + 1) <= x) ++s;
    return before(s) + (x - hi(s) + 1) * LPX;
  }
  static constexpr bool bar(int s) {
    return MODE == 1 ? (s == 4 || s == 6) : MODE == 2 ? s == 6 : MODE == 3 ? (s == 8 || s == 12)
         : (s == 4 || s == 8 || s == 12 || s == 15 || s == 19 || s == 23);
  }
  static constexpr int waitn(int s) {
    int n = before(s) - xend(s);
    if (MODE == 4 && (s == 15 || s == 19 || s == 23)) {
      const int lim = before(s) - before(s - 8);
      n = n < lim ? n : lim;
    }
    return n;
  }
  static constexpr int SCR = (MODE <= 2 ? 140 : 0) * KB;
};

struct Tile { int tb, ct, kh, Bp, Bs, oA, oS, oB; bool has_s; };
template <int MODE> __device__ __forceinline__ Tile tile_of(const Ctx& cx) {
  Tile t;
  t.kh = MODE == 3 ? 0 : cx.w >> 2;
  if (MODE == 3) {
    t.ct = cx.bi & 15;
    t.tb = (cx.xcd * 2 + (cx.bi >> 4)) * 8 + cx.w;
    const int c8 = t.ct & 7;
    const int B0 = (t.ct >> 3) * 54 + (c8 <= 6 ? c8 * 7 : 48);
    const int odd = B0 & 1;
    t.Bp = B0 + odd; t.Bs = odd ? B0 : B0 + 6; t.has_s = c8 < 6;
    t.oA = odd; t.oS = odd ? 0 : 6; t.oB = 0;
  } else {
    t.ct = cx.bi & 7;
    t.tb = (cx.xcd * 4 + (cx.bi >> 3)) * 4 + (cx.w & 3);
    const int odd = t.ct & 1;
    t.oA = odd + 2 * t.kh; t.oS = odd ? 0 : 4; t.oB = odd + 2 * (t.kh ^ 1);
    t.Bp = t.ct * 5 + t.oA; t.Bs = t.ct * 5 + t.oS; t.has_s = t.kh == 0;
  }
  return t;
}
template <int MODE>
__device__ __forceinline__ void gemm_phase(Ctx& cx) {
  using P = GP<MODE>;
  constexpr int NB = P::NB, KS = P::KS, NS = P::NS;
  char* ws = cx.ws;
  const int lane = cx.lane, w = cx.w;
  const u32 sb = cx.sb, vo = cx.vo;
  const int c = lane & 15, q = lane >> 4;
  const Tile T = tile_of<MODE>(cx);
  const int tb = T.tb, ct = T.ct, kh = T.kh, Bp = T.Bp, Bs = T.Bs;
  const bool has_s = T.has_s;
  const char* Xg = ws + (MODE == 1 ? O_H : MODE == 2 ? O_G1 : MODE == 3 ? O_X1 : O_G3) + (size_t)tb * (KS * 1024) + kh * 2048;
  constexpr int NE = MODE == 3 ? 7 : 3;

  u32x4 xf[NS];
  u32x2 xl[NS], xh[NS];
  const u32 vol = vo - kh * 1024;
  u32 vsh[4];
  if (MODE == 2) {
#pragma unroll
    for (int g = 0; g < 4; ++g) {
      const int l2 = 16 + c - (g == 0 ? 1 : g == 1 ? -1 : g == 2 ? 2 : -2);
      vsh[g] = (u32)((l2 >> 4) * 20480 + q * 256 + (l2 & 15) * 16);
    }
  }
  auto issueX = [&](int s, int i) __attribute__((always_inline)) {
    const int off = ((s / P::SPC) * 4 + s % P::SPC) * 1024;
    if (MODE == 2) {
      const int g0 = (off >> 10) / 5, g1 = ((off >> 10) + 2) / 5;
      xf[s] = gload16a(Xg - 20480, (g0 == g1 || !kh) ? vsh[g0] : vsh[g1], off);
      return;
    }
    if (MODE == 1) { if (i == 0) xl[s] = gload8(Xg, lane * 8, off); else xh[s] = gload8(Xg, lane * 8, off + 512); }
    else xf[s] = gload16a(Xg, (MODE == 4 && s == NS - 1) ? vol : vo, off);
  };
#pragma unroll
  for (int s = 0; s < P::A0; ++s)
#pragma unroll
    for (int i = 0; i < P::LPX; ++i) issueX(s, i);

  f32x4 acc[NB];
#pragma unroll
  for (int f = 0; f < NB; ++f) { acc[f] = f32x4{0.f, 0.f, 0.f, 0.f}; pina(acc[f]); }
  float s1 = 0.f, s2 = 0.f;
  const bf16x2 ones = {(__bf16)1.0f, (__bf16)1.0f};
  f32x4 asum = {0.f, 0.f, 0.f, 0.f}, asq = asum;
  u32x4 onesf = {0x3f803f80u, 0x3f803f80u, 0x3f803f80u, 0x3f803f80u};
  if (MODE == 1) { pina(asum); pina(asq); pin(onesf); }
  constexpr int NT = MODE == 1 ? 2 : 1, BS = MODE == 1 ? 32 : 1024;
  u32 rb[NT][3][3];
#pragma unroll
  for (int t = 0; t < NT; ++t)
#pragma unroll
    for (int k = 0; k < 3; ++k) {
      const u32 b0 = sb + k * 65536 + kh * (2 * NB * 1024) + (MODE == 1 ? (8 * q + 4 * (t ^ (q & 1)) + (c >> 2)) * 160 + (c & 3) * 8 : vo);
      rb[t][0][k] = b0 + T.oA * BS; rb[t][1][k] = b0 + T.oS * BS; rb[t][2][k] = b0 + T.oB * BS;
      pin(rb[t][0][k]); pin(rb[t][1][k]); if (MODE != 3) pin(rb[t][2][k]);
    }
  auto rdW = [&](int s, int f) __attribute__((always_inline)) -> u32x4 {
    const int off = wpos(MODE, s / P::SPC) + (s % P::SPC) * NB * 1024 + (f < NE - 1 ? f : (f == NE - 1 ? 0 : f - NE)) * BS;
    const int kind = f < NE - 1 ? 0 : (f == NE - 1 ? 1 : 2);
    if (MODE != 1) {
      u32 base = rb[0][kind][off >> 16];
      if (MODE == 4 && s == NS - 1) base -= kh * (NB * 1024);
      return lds_ld<u32x4>(base + (off & 65535));
    }
    const s16x4 a = __builtin_amdgcn_ds_read_tr16_b64_v4i16((LDS_AS s16x4*)(size_t)(rb[0][kind][off >> 16] + (off & 65535)));
    const s16x4 b = __builtin_amdgcn_ds_read_tr16_b64_v4i16((LDS_AS s16x4*)(size_t)(rb[NT - 1][kind][off >> 16] + (off & 65535)));
    const u32x2 ua = __builtin_bit_cast(u32x2, a), ub = __builtin_bit_cast(u32x2, b);
    return u32x4{ua.x, ua.y, ub.x, ub.y};
  };
  u32x4 wf[2][NB];
#pragma unroll
  for (int f = 0; f < NB; ++f) wf[0][f] = rdW(0, f);
  u32 csw[2];
  u32x2 psv[4];

  sfor<NS>([&](auto S_) __attribute__((always_inline)) {
    constexpr int s = decltype(S_)::value;
    if constexpr (P::bar(s)) wait_vm_bar<P::waitn(s)>();
    else wait_vm<P::waitn(s)>();
    if (MODE == 1) { pin(xl[s]); pin(xh[s]); xf[s] = u32x4{xl[s].x, xl[s].y, xh[s].x, xh[s].y}; }
    else pina(xf[s]);
    SCHED_FENCE();
#pragma unroll
    for (int f = 0; f < NB; ++f) {
      if (s + 1 < NS) wf[(s + 1) & 1][f] = rdW(s + 1, f);
      if (MODE == 1) asm volatile("v_mfma_f32_16x16x32_bf16 %0, %1, %2, %0" : "+a"(acc[f]) : "v"(wf[s & 1][f]), "v"(xf[s]));
      else asm volatile("v_mfma_f32_16x16x32_bf16 %0, %1, %2, %0" : "+a"(acc[f]) : "v"(wf[s & 1][f]), "a"(xf[s]));
      if (MODE == 1 && f == 1) asm volatile("v_mfma_f32_16x16x32_bf16 %0, %1, %2, %0" : "+a"(asum) : "v"(onesf), "v"(xf[s]));
      if (MODE == 1 && f == 3) asm volatile("v_mfma_f32_16x16x32_bf16 %0, %1, %1, %0" : "+a"(asq) : "v"(xf[s]));
      constexpr int nx = P::nx(s);
      if (f < nx) issueX(P::hi(s) + f / P::LPX, f % P::LPX);
      else if (f - nx < P::npf(s)) pf_piece<(P::pfm(s) ? P::pfm(s) : 1)>(cx, P::pfc(s), f - nx);
      if ((MODE == 1 || MODE == 3) && s == 2 && f == 4) {
        const char* cs = ws + (MODE == 1 ? O_CS1 : O_CS3);
#pragma unroll
        for (int k = 0; k < (MODE == 3 ? 2 : 1); ++k) {
          const int sl = k * 4 + (c >> 2);
          csw[k] = gload4s(cs, (u32)((sl >= NE - 1 ? Bs : Bp + sl) * 64 + q * 16 + (c & 3) * 4));
        }
      }
      if (MODE == 3 && s == 3 && f >= 3) psv[f - 3] = gload8(ws + O_PS + tb * 2048, lane * 8, (f - 3) * 512);
      if (MODE == 1 && s == NS - 1 && f < 3) {
        const int B = f < 2 ? Bp + f : Bs;
        cx.hv[f] = gload8a(ws + O_H + (size_t)tb * 20480 + (B >> 1) * 1024 + (B & 1) * 256, (u32)((q & 1) * 512 + (q >> 1) * 128 + c * 8), 0);
      }
      SCHED_FENCE();
    }
  });
  asm volatile("s_nop 7\n\ts_nop 7" ::: "memory");
#pragma unroll
  for (int f = 0; f < NB; ++f) pina(acc[f]);
  STAMP(1 + MODE, 14);
  asm volatile("s_barrier" ::: "memory");
  if (MODE != 3) {
    auto wr = [&](u32 a, f32x4& v) __attribute__((always_inline)) { asm volatile("ds_write_b128 %0, %1" :: "v"(a), "a"(v) : "memory"); };
    const u32 base = sb + P::SCR + (w & 3) * 5120 + vo;
    const u32 mine = base + kh * 2048, theirs = base + (kh ^ 1) * 2048;
    wr(mine, acc[3]); wr(mine + 1024, acc[4]);
    if (kh) wr(mine + 2048, acc[2]);
    if (MODE == 1) {
      pina(asum); pina(asq);
      const float dg = (c & 2) ? ((c & 1) ? asq[3] : asq[2]) : ((c & 1) ? asq[1] : asq[0]);
      s1 = asum[0]; s2 = qsum(q == (c >> 2) ? dg : 0.f);
      lds_st(sb + 28 * KB + w * 512 + lane * 8, f32x2{s1, s2});
    }
    WG_BARRIER();
    acc[0] += lds_ld<f32x4>(theirs); acc[1] += lds_ld<f32x4>(theirs + 1024); acc[2] += lds_ld<f32x4>(theirs + 2048);
    if (MODE == 1) { f32x2 o = lds_ld<f32x2>(sb + 28 * KB + (w ^ 4) * 512 + lane * 8); s1 += o.x; s2 += o.y; }
  }
  STAMP(1 + MODE, 13);
  const u32 sofs = (u32)(Bs * 512 + (q >> 1) * 256);
  const int gq = (q & 1) * 2 + (q >> 1);
  if (MODE == 1 || MODE == 3) {
    pin(csw[0]); if (MODE == 3) pin(csw[1]);
    float a = s1, bq = s2;
    if (MODE == 3) {
#pragma unroll
      for (int t = 0; t < 4; ++t) { pin(psv[t]); a += __uint_as_float(psv[t].x); bq += __uint_as_float(psv[t].y); }
      a = qsum(a); bq = qsum(bq);
    }
    const float m = a * (1.0f / D);
    const float var = fmaxf(bq * (1.0f / D) - m * m, 0.f);
    const float r = __builtin_amdgcn_rsqf(var + 1e-5f);
    float rr = r;
    if (MODE == 1) { rr = r * __builtin_amdgcn_rsqf(var * r * r + 1e-5f); cx.m = m; cx.r = r; }
    const float mr = -m * rr;
    const u32 tofs = (u32)(tb * (MODE == 1 ? 20480 : 54 * 1024) + c * 16);
    char* dst = ws + (MODE == 1 ? O_G1 : O_G3);
    auto act = [&](auto F_) __attribute__((always_inline)) -> u32x2 {
      constexpr int f = decltype(F_)::value;
      const f32x4 cs = {__uint_as_float(dpp<0x150 + (f & 3) * 4>(csw[f >> 2])), __uint_as_float(dpp<0x151 + (f & 3) * 4>(csw[f >> 2])),
                        __uint_as_float(dpp<0x152 + (f & 3) * 4>(csw[f >> 2])), __uint_as_float(dpp<0x153 + (f & 3) * 4>(csw[f >> 2]))};
      const f32x4 xx = acc[f] * rr + cs * mr;
      const f32x2 o0 = gelu2(f32x2{xx[0], xx[1]}), o1 = gelu2(f32x2{xx[2], xx[3]});
      return u32x2{pkbf(o0.x, o0.y), pkbf(o1.x, o1.y)};
    };
    sfor<(NE - 1) / 2>([&](auto J_) __attribute__((always_inline)) {
      constexpr int j = decltype(J_)::value;
      if (MODE == 3) { pf_piece<4>(cx, 5 + j, 0); pf_piece<4>(cx, 5 + j, 1); } else { pf_piece<3>(cx, 0, 0); pf_piece<3>(cx, 0, 1); }
      const u32x2 p0 = act(std::integral_constant<int, 2 * j>{});
      if (MODE == 3) pf_piece<4>(cx, 5 + j, 2); else { pf_piece<3>(cx, 0, 2); pf_piece<3>(cx, 0, 3); }
      const u32x2 p1 = act(std::integral_constant<int, 2 * j + 1>{});
      gstore16(dst, tofs + (u32)((Bp + 2 * j) * 512 + gq * 256), widen2(p0, p1));
    });
    if (has_s) { const u32x4 v = widen(act(std::integral_constant<int, NE - 1>{})); if (!(q & 1)) gstore16(dst, tofs + sofs, v); }
  } else if (MODE == 2) {
    const float m = cx.m, r = cx.r;
    pina(cx.hv[0]); pina(cx.hv[1]); pina(cx.hv[2]);
    u32 p[6];
#pragma unroll
    for (int k = 0; k < 6; ++k) {
      const u32 hv = cx.hv[k >> 1][k & 1];
      p[k] = pkbf((bflo(hv) - m) * r + acc[k >> 1][(k & 1) * 2], (bfhi(hv) - m) * r + acc[k >> 1][(k & 1) * 2 + 1]);
    }
#pragma unroll
    for (int k = 0; k < 3; ++k) cx.xv[k] = u32x2{p[2 * k], p[2 * k + 1]};
    gstore16(ws + O_X1 + (size_t)tb * 20480 + Bp * 512, (u32)(gq * 256 + c * 16), widen2(cx.xv[0], cx.xv[1]));
    if (has_s) { const u32x4 v = widen(cx.xv[2]); if (!(q & 1)) gstore16(ws + O_X1 + (size_t)tb * 20480, sofs + c * 16, v); }
    if (!has_s) { p[4] = 0u; p[5] = 0u; }
#pragma unroll
    for (int k = 0; k < 6; ++k) {
      s1 = __builtin_amdgcn_fdot2_f32_bf16(as_bf2(p[k]), ones, s1, false);
      s2 = __builtin_amdgcn_fdot2_f32_bf16(as_bf2(p[k]), as_bf2(p[k]), s2, false);
    }
    s1 = qsum(s1); s2 = qsum(s2);
    if (q == 0) gstore8(ws + O_PS + tb * 2048 + (ct * 2 + kh) * 128, c * 8, u32x2{__float_as_uint(s1), __float_as_uint(s2)});
  } else {
    u32 p[6];
#pragma unroll
    for (int k = 0; k < 6; ++k) {
      const u32 hv = cx.hv[k >> 1][k & 1], xv = cx.xv[k >> 1][k & 1];
      p[k] = pkbf(bflo(hv) + bflo(xv) + acc[k >> 1][(k & 1) * 2], bfhi(hv) + bfhi(xv) + acc[k >> 1][(k & 1) * 2 + 1]);
    }
    u16* orow = cx.out + (size_t)(tb * 16 + c) * D;
    *(u32x4*)(orow + Bp * 16 + gq * 8) = widen2(u32x2{p[0], p[1]}, u32x2{p[2], p[3]});
    if (has_s) *(u32x2*)(orow + Bs * 16 + q * 4) = u32x2{p[4], p[5]};
  }
  STAMP(1 + MODE, 15);
}

__global__ __launch_bounds__(512, 2) __attribute__((amdgpu_waves_per_eu(2, 2)))
void k_main(In in, char* __restrict__ ws, u16* __restrict__ out) {
  __shared__ __attribute__((aligned(16))) char smem[K2_LDS];
  STAMP(0, 0);
  asm volatile("" :: "s"(in.x), "s"(in.lar), "s"(in.aim), "s"(in.cre), "s"(in.cim), "s"(in.ldt), "s"(in.dsk), "s"(in.w1), "s"(in.w2), "s"(in.w3), "s"(in.w4), "s"(ws), "s"(out));
  Ctx c;
  c.ws = ws; c.out = out;
  c.lane = threadIdx.x & 63; c.w = __builtin_amdgcn_readfirstlane(threadIdx.x >> 6);
  c.sb = lds_addr(smem); c.vo = c.lane * 16;
  c.xcd = blockIdx.x & 7; c.bi = blockIdx.x >> 3;
  const int rt = c.xcd * 4 + (c.bi >> 3), ct = c.bi & 7, rt3 = c.xcd * 2 + (c.bi >> 4);
  auto setup = [&]() __attribute__((always_inline)) {
  c.Wg[1] = (const char*)(in.w1 + ct * 80 + c.w * 16 * D);
  c.Wg[2] = ws + O_W2 + (size_t)ct * (100 * KB) + c.w * 2560;
  c.Wg[3] = ws + O_W3 + (size_t)(c.bi & 15) * (140 * KB) + c.w * 3584;
  c.Wg[4] = ws + O_W4 + (size_t)ct * (270 * KB) + c.w * 2560;
#pragma unroll
  for (int i = 0; i < 3; ++i) {
    const int u = i * 64 + c.lane, r = (u / 10) & 15, cc = u % 10;
    c.wo[i] = (u32)((r ^ ((r >> 3) << 2)) * (D * 2) + cc * 16);
  }
#pragma unroll
  for (int i = 0; i < 3; ++i) pin(c.wo[i]);
  };
  const bool isconv = c.bi >= 8 && c.bi < 28;
  const int cg = c.xcd * 20 + c.bi - 8;
  const u32 epo = (c.w == 0 && c.lane >= 1 && c.lane <= 16) ? (u32)(2048 + blockIdx.x * 2048 + (c.lane - 1) * 128) : 0u;
  auto mktag = [&](u32x2& epv) __attribute__((always_inline)) {
    pin(epv);
    const unsigned long long ep = ((unsigned long long)epv.y << 32) | epv.x;
    unsigned long long t = (ep ^ (ep >> 31)) * 0x9E3779B97F4A7C15ull + 0x632BE59BD9B4E019ull;
    t ^= t >> 29;
    c.tlo = __builtin_amdgcn_readfirstlane((u32)t); c.thi = __builtin_amdgcn_readfirstlane((u32)(t >> 32));
  };
  const int tun = (c.bi < 8 ? c.bi : c.bi - 20) * 8 + c.xcd;
  if (c.bi < 8) {
    u32x4 xv[5];
    const char* xr = (const char*)(in.x + (size_t)(tun * 32 + c.w * 4) * D);
    const u32 xo = (u32)(c.lane >> 4) * (D * 2) + (u32)(c.lane & 15) * 16u;
#pragma unroll
    for (int i = 0; i < 5; ++i) xv[i] = gload16(xr, xo, i * 256);
    u32x2 epv = gload8(ws + O_EP, epo, 0);
    wait_vm<0>();
#pragma unroll
    for (int i = 0; i < 5; ++i) pin(xv[i]);
    mktag(epv);
    ln_compute(c, xv);
    WG_BARRIER();
    ln_store(c, tun);
    STAMP(0, 1);
    asm volatile("s_waitcnt vmcnt(0)\n\ts_barrier" ::: "memory");
    post(c, 160, 64 + threadIdx.x, MB_LN, tun);
    STAMP(0, 2);
  }
  if (__builtin_expect(isconv, 1)) {
    const int kch = c.w >> 1, set = c.w & 1, d = cg * 4 + kch;
    const u32 n4 = (u32)(d * NM * 2 + c.lane * 4);
    KP kp;
    kp.lar = gload4(in.lar, n4); kp.aim = gload4(in.aim, n4); kp.ldt = gload2(in.ldt, (u32)d * 2u);
    kp.cr = gload4(in.cre, n4); kp.ci = gload4(in.cim, n4);
    u32x2 dsv = gload8(in.dsk, (u32)cg * 8u, 0);
    u32x2 epv = gload8(ws + O_EP, epo, 0);
    setup();
    const u32 tab = c.sb + kch * KG_SZ;
    const KI ki = kg_index(tab, c.sb + KG_KT + kch * 2048, set, c.lane);
    u32 ix[11];
    const int gq = c.w >> 2;
    kg_lagidx(threadIdx.x & 255, c.sb + KG_KT + gq * 2048, ix);
    wait_vm<0>();
    pin(kp.lar); pin(kp.aim); pin(kp.ldt); pin(kp.cr); pin(kp.ci); pin(dsv);
    mktag(epv);
    {
      const int k = cg * 8 + c.w;
      const u16* p = k < 540 ? in.w4 : (k < 1080 ? in.w3 : in.w1);
      const int u = k < 540 ? k : (k < 1080 ? k - 540 : k - 1080);
      asm volatile("s_mov_b32 m0, %2\n\ts_nop 0\n\tglobal_load_lds_dword %0, %1" :: "v"((u32)u * 4096u + (u32)c.lane * 64u), "s"(p), "s"(c.sb + 150 * KB) : "memory");
    }
    STAMP(0, 1);
    const float ks = kg_tables(kp, tab, set, c.lane);
    if (set == 0 && c.lane == 0) lds_st(c.sb + KG_KT + 8192 + kch * 4, ks);
    WG_BARRIER();
    STAMP(0, 2);
    kg_mfma(ki);
    await(c, MB_LN, 64);
    WG_BARRIER();
    STAMP(0, 3);
    u32x4 uv0 = gload16(ws + O_UT + (size_t)cg * 16384 + c.w * 2048, c.vo, 0), uv1 = gload16(ws + O_UT + (size_t)cg * 16384 + c.w * 2048, c.vo, 1024);
    u32x4 stv[2];
#pragma unroll
    for (int i = 0; i < 2; ++i) stv[i] = gload16a(ws + O_ST0 + c.w * 2048, c.vo, i * 1024);
    u32 v0[11], v1[11];
#pragma unroll
    for (int k = 0; k < 11; ++k) { v0[k] = lds_ld<u16>(ix[k]); v1[k] = lds_ld<u16>(ix[k] + 4096); }
    SCHED_FENCE();
    kg_expand(v0, c.sb + gq * CV_CH, threadIdx.x & 255);
    kg_expand(v1, c.sb + (2 + gq) * CV_CH, threadIdx.x & 255);
    if (c.w < 4 && c.lane < 32) lds_st(c.sb + c.w * CV_CH + 4 * CV_KC + 4096 + c.lane * 16, u32x4{0u, 0u, 0u, 0u});
    const f32x4 ksc = lds_ld<f32x4>(c.sb + KG_KT + 8192);
    STAMP(1, 0);
    conv_phase(c, dsv, cg, ksc, uv0, uv1, stv);
  } else {
    u32x2 epv = gload8(ws + O_EP, epo, 0);
    if (c.bi == 30 && c.w >= 6) {
      const u32 o = (u32)c.lane * PGS;
      const char* p = c.w == 6 ? ws : (const char*)out;
      asm volatile("s_mov_b32 m0, %2\n\ts_nop 0\n\tglobal_load_lds_dword %0, %1" :: "v"(o < (c.w == 6 ? (u32)O_MB : (u32)(L * D * 2)) ? o : 0u), "s"(p), "s"(c.sb + 150 * KB) : "memory");
    }
    const int nu = (tun >= 64 || tun < 26) ? 2 : 1, u0 = tun < 64 ? 64 + tun : tun - 64, ud = tun < 64 ? 64 : 32;
    for (int k = 0; k < nu; ++k) tu_load(c, tu_of(in, ws, u0 + k * ud), c.sb + k * 40 * KB);
    setup();
    if (tun >= 94) {
      char* z = ws + (tun == 94 ? O_G1 - 20480 : O_G1 + (size_t)L * D * 2);
      for (int i = threadIdx.x; i < 1280; i += 512) gstore16(z, i * 16u, u32x4{0u, 0u, 0u, 0u});
    }
    asm volatile("s_waitcnt vmcnt(0)\n\ts_barrier" ::: "memory");
    mktag(epv);
    for (int k = 0; k < nu; ++k) tu_emit(c, tu_of(in, ws, u0 + k * ud), c.sb + k * 40 * KB, c.sb + (98 + k) * KB);
    STAMP(0, 3);
    DRAIN_BARRIER();
    post(c, 1, 248 + c.xcd, MB_AT, c.bi < 8 ? c.bi : c.bi - 20);
#pragma unroll
    for (int k = 0; k < 5; ++k) pf_chunk<1>(c, k);
    asm volatile("s_waitcnt vmcnt(0)" ::: "memory");
  }
  STAMP(1, 15);
  if (c.bi == 31) {
    await(c, MB_AT, 12);
#pragma unroll
    for (int k = 0; k < 4; ++k) post(c, 64, k * 64 + threadIdx.x, MB_T, c.xcd);
  }
  await(c, MB_T, 8);
  asm volatile("s_barrier" ::: "memory");
  if (c.w) { pf7_chunk<2>(c, 0); pf7_chunk<2>(c, 1); pf7_chunk<2>(c, 2); }
  if (c.bi == 31) {
    await(c, MB_AC, 20);
#pragma unroll
    for (int k = 0; k < 4; ++k) post(c, 64, k * 64 + threadIdx.x, MB_C, c.xcd);
  }
  await(c, MB_C, 8);
  if (blockIdx.x == 0 && threadIdx.x == 0) gstore8(ws + O_EP, 0u, u32x2{c.tlo, c.thi});
  asm volatile("s_barrier" ::: "memory");
  STAMP(2, 0);
  gemm_phase<1>(c);
  DRAIN_BARRIER();
  {
    const int t = threadIdx.x, r2 = (rt + (t >> 3) - 1) & 31;
    post(c, 24, (((r2 & 3) * 8 + (t & 7)) << 3) + (r2 >> 2), MB_1, (2 - (t >> 3)) * 8 + ct);
  }
  await(c, MB_1, 24);
  asm volatile("s_barrier" ::: "memory");
  STAMP(3, 0);
  gemm_phase<2>(c);
  DRAIN_BARRIER();
  post(c, 16, (((c.bi & 16) + threadIdx.x) << 3) + c.xcd, MB_2, c.bi & 15);
  if (c.w) { pf7_chunk<3>(c, 3); pf7_chunk<3>(c, 4); }
  await(c, MB_2, 16);
  asm volatile("s_barrier" ::: "memory");
  STAMP(4, 0);
  gemm_phase<3>(c);
  DRAIN_BARRIER();
  post(c, 16, (((c.bi & 16) + threadIdx.x) << 3) + c.xcd, MB_3, c.bi & 15);
  await(c, MB_3, 16);
  asm volatile("s_barrier" ::: "memory");
  STAMP(5, 0);
  gemm_phase<4>(c);
}

}

extern "C" void kernel_launch(void* const* d_in, const int* in_sizes, int n_in, void* d_out, int out_size, void* d_ws,
                              size_t ws_size, hipStream_t stream) {
  In in;
  in.x = (const u16*)d_in[0]; in.lar = (const u16*)d_in[1]; in.aim = (const u16*)d_in[2]; in.cre = (const u16*)d_in[3];
  in.cim = (const u16*)d_in[4]; in.ldt = (const u16*)d_in[5]; in.dsk = (const u16*)d_in[6];
  in.w1 = (const u16*)d_in[8]; in.w2 = (const u16*)d_in[9]; in.w3 = (const u16*)d_in[10]; in.w4 = (const u16*)d_in[11];
  k_main<<<256, 512, 0, stream>>>(in, (char*)d_ws, (u16*)d_out);
}
```

```cpp
#include <hip/hip_runtime.h>
#include <hip/hip_bf16.h>
#include <stdint.h>
#include <utility>

typedef unsigned short u16;
typedef unsigned u32;
typedef _Float16 f16;
typedef __attribute__((ext_vector_type(2))) _Float16 f16x2;
typedef __attribute__((ext_vector_type(8))) _Float16 f16x8;
typedef __attribute__((ext_vector_type(2))) __bf16 bf16x2;
typedef __attribute__((ext_vector_type(8))) __bf16 bf16x8;
typedef __attribute__((ext_vector_type(2))) float f32x2;
typedef __attribute__((ext_vector_type(4))) float f32x4;
typedef __attribute__((ext_vector_type(2))) unsigned u32x2;
typedef __attribute__((ext_vector_type(4))) unsigned u32x4;
typedef __attribute__((ext_vector_type(4))) short s16x4;
#define LDS_AS __attribute__((address_space(3)))

namespace {

constexpr int L = 2048, D = 640, NM = 128, F = 1728;
constexpr size_t O_UT  = 0;
constexpr size_t O_H   = O_UT  + (size_t)D * L * 2;
constexpr size_t O_G1  = O_H   + (size_t)L * D * 2 + 20480;
constexpr size_t O_X1  = O_G1  + (size_t)L * D * 2 + 20480;
constexpr size_t O_G3  = O_X1  + (size_t)L * D * 2;
constexpr size_t O_W2  = O_G3  + (size_t)L * F * 2;
constexpr size_t O_W3  = O_W2  + (size_t)D * D * 2;
constexpr size_t O_W4  = O_W3  + (size_t)16 * 20 * 7 * 1024;
constexpr size_t O_CS1 = O_W4  + (size_t)D * F * 2 + 16384;
constexpr size_t O_CS3 = O_CS1 + D * 4;
constexpr size_t O_ST0 = O_CS3 + 1792 * 4;
constexpr size_t O_PS  = O_ST0 + L * 8;
constexpr size_t O_EP  = O_PS + 128 * 2048;
constexpr size_t O_MB  = O_EP + 2048;
constexpr int MB_LN = 0, MB_1 = 512, MB_2 = 768, MB_3 = 896, MB_C = 1024, MB_T = 1152, MB_AC = 1280, MB_AT = 1536;

__device__ __forceinline__ float bf2f(u16 v) { return __uint_as_float(((u32)v) << 16); }
__device__ __forceinline__ float bflo(u32 v) { return __uint_as_float(v << 16); }
__device__ __forceinline__ float bfhi(u32 v) { return __uint_as_float(v & 0xffff0000u); }
__device__ __forceinline__ u32 pkbf(float a, float b) {
  f32x2 v = {a, b};
  return __builtin_bit_cast(u32, __builtin_convertvector(v, bf16x2));
}
__device__ __forceinline__ float rbf(float f) { return bflo(pkbf(f, 0.f)); }
__device__ __forceinline__ u32 pkh(float a, float b) {
  f32x2 v = {a, b};
  return __builtin_bit_cast(u32, __builtin_convertvector(v, f16x2));
}
__device__ __forceinline__ u32 umin(u32 a, u32 b) { return a < b ? a : b; }
__device__ __forceinline__ u32 umax(u32 a, u32 b) { return a > b ? a : b; }
__device__ __forceinline__ bf16x2 as_bf2(u32 v) { return __builtin_bit_cast(bf16x2, v); }
__device__ __forceinline__ f16x2 as_h2(u32 v) { return __builtin_bit_cast(f16x2, v); }
__device__ __forceinline__ u32 lds_addr(const void* p) { return (u32)(size_t)(LDS_AS const void*)p; }
template <class T> __device__ __forceinline__ T lds_ld(u32 a) { return *(LDS_AS const T*)(size_t)a; }
template <class T> __device__ __forceinline__ void lds_st(u32 a, T v) { *(LDS_AS T*)(size_t)a = v; }

template <bool SC1>
__device__ __forceinline__ void glds16(const void* sbase, u32 voff, int goff, u32 lbase, int loff) {
  u32 t;
  if (SC1) asm volatile("s_add_i32 m0, %4, %5\n\tv_add_u32 %0, %3, %1\n\tglobal_load_lds_dwordx4 %0, %2 sc1"
               : "=&v"(t) : "v"(voff), "s"(sbase), "i"(goff), "s"(lbase), "i"(loff) : "memory", "scc");
  else asm volatile("s_add_i32 m0, %4, %5\n\tv_add_u32 %0, %3, %1\n\tglobal_load_lds_dwordx4 %0, %2"
               : "=&v"(t) : "v"(voff), "s"(sbase), "i"(goff), "s"(lbase), "i"(loff) : "memory", "scc");
}
__device__ __forceinline__ void glds16r(const void* sbase, u32 voff, u32 goff, u32 laddr) {
  u32 t;
  asm volatile("s_nop 4\n\ts_mov_b32 m0, %4\n\tv_add_u32 %0, %3, %1\n\tglobal_load_lds_dwordx4 %0, %2"
               : "=&v"(t) : "v"(voff), "s"(sbase), "s"(goff), "s"(laddr) : "memory");
}
template <bool SC1>
__device__ __forceinline__ void glds16_half(const void* sbase, u32 voff, int goff, u32 lbase, int loff) {
  u32 t;
  if (SC1) asm volatile("s_add_i32 m0, %4, %5\n\tv_add_u32 %0, %3, %1\n\ts_mov_b32 exec_hi, 0\n\tglobal_load_lds_dwordx4 %0, %2 sc1\n\ts_mov_b32 exec_hi, -1"
               : "=&v"(t) : "v"(voff), "s"(sbase), "i"(goff), "s"(lbase), "i"(loff) : "memory", "scc");
  else asm volatile("s_add_i32 m0, %4, %5\n\tv_add_u32 %0, %3, %1\n\ts_mov_b32 exec_hi, 0\n\tglobal_load_lds_dwordx4 %0, %2\n\ts_mov_b32 exec_hi, -1"
               : "=&v"(t) : "v"(voff), "s"(sbase), "i"(goff), "s"(lbase), "i"(loff) : "memory", "scc");
}
__device__ __forceinline__ u32x4 gload16(const void* sbase, u32 voff, int goff) {
  u32x4 r; u32 t;
  asm volatile("v_add_u32 %1, %4, %2\n\tglobal_load_dwordx4 %0, %1, %3 sc1" : "=v"(r), "=&v"(t) : "v"(voff), "s"(sbase), "i"(goff) : "memory");
  return r;
}
__device__ __forceinline__ u32x2 gload8(const void* sbase, u32 voff, int goff) {
  u32x2 r; u32 t;
  asm volatile("v_add_u32 %1, %4, %2\n\tglobal_load_dwordx2 %0, %1, %3 sc1" : "=v"(r), "=&v"(t) : "v"(voff), "s"(sbase), "i"(goff) : "memory");
  return r;
}
__device__ __forceinline__ u32x4 gload16a(const void* sbase, u32 voff, int goff) {
  u32x4 r; u32 t;
  asm volatile("v_add_u32 %1, %4, %2\n\tglobal_load_dwordx4 %0, %1, %3 sc1" : "=a"(r), "=&v"(t) : "v"(voff), "s"(sbase), "i"(goff) : "memory");
  return r;
}
__device__ __forceinline__ u32x2 gload8a(const void* sbase, u32 voff, int goff) {
  u32x2 r; u32 t;
  asm volatile("v_add_u32 %1, %4, %2\n\tglobal_load_dwordx2 %0, %1, %3 sc1" : "=a"(r), "=&v"(t) : "v"(voff), "s"(sbase), "i"(goff) : "memory");
  return r;
}
template <bool HALF>
__device__ __forceinline__ void glds16s(const void* sbase, u32 voff, u32 soff, u32 lbase, int loff) {
  u32 t;
  if (HALF) asm volatile("s_add_i32 m0, %4, %5\n\tv_add_u32 %0, %3, %1\n\ts_mov_b32 exec_hi, 0\n\tglobal_load_lds_dwordx4 %0, %2 sc1\n\ts_mov_b32 exec_hi, -1"
               : "=&v"(t) : "v"(voff), "s"(sbase), "s"(soff), "s"(lbase), "i"(loff) : "memory", "scc");
  else asm volatile("s_add_i32 m0, %4, %5\n\tv_add_u32 %0, %3, %1\n\tglobal_load_lds_dwordx4 %0, %2 sc1"
               : "=&v"(t) : "v"(voff), "s"(sbase), "s"(soff), "s"(lbase), "i"(loff) : "memory", "scc");
}
__device__ __forceinline__ u32x4 gload16as(const void* sbase, u32 voff, u32 soff, int ioff) {
  u32x4 r; u32 t;
  asm volatile("v_add_u32 %1, %4, %2\n\tglobal_load_dwordx4 %0, %1, %3 offset:%5 sc1" : "=a"(r), "=&v"(t) : "v"(voff), "s"(sbase), "s"(soff), "i"(ioff) : "memory");
  return r;
}
__device__ __forceinline__ u32x2 gload8s(const void* sbase, u32 voff, u32 soff, int ioff) {
  u32x2 r; u32 t;
  asm volatile("v_add_u32 %1, %4, %2\n\tglobal_load_dwordx2 %0, %1, %3 offset:%5 sc1" : "=v"(r), "=&v"(t) : "v"(voff), "s"(sbase), "s"(soff), "i"(ioff) : "memory");
  return r;
}
__device__ __forceinline__ u32x4 gload16ap(const void* sbase, u32 voff) {
  u32x4 r;
  asm volatile("global_load_dwordx4 %0, %1, %2" : "=a"(r) : "v"(voff), "s"(sbase) : "memory");
  return r;
}
__device__ __forceinline__ void gstore16(void* sbase, u32 voff, u32x4 v) {
  asm volatile("global_store_dwordx4 %0, %1, %2 sc1\n\ts_nop 1" :: "v"(voff), "v"(v), "s"(sbase) : "memory");
}
__device__ __forceinline__ void gstore8(void* sbase, u32 voff, u32x2 v) {
  asm volatile("global_store_dwordx2 %0, %1, %2 sc1\n\ts_nop 0" :: "v"(voff), "v"(v), "s"(sbase) : "memory");
}
__device__ __forceinline__ void gstore4(void* sbase, u32 voff, u32 v) {
  asm volatile("global_store_dword %0, %1, %2 sc1\n\ts_nop 0" :: "v"(voff), "v"(v), "s"(sbase) : "memory");
}
__device__ __forceinline__ void reload16(u32x4& r, const void* sbase, u32 voff, int goff) {
  u32 t;
  asm volatile("v_add_u32 %1, %4, %2\n\tglobal_load_dwordx4 %0, %1, %3 sc1\n\ts_waitcnt vmcnt(0)" : "+v"(r), "=&v"(t) : "v"(voff), "s"(sbase), "i"(goff) : "memory");
}
__device__ __forceinline__ void reload8(u32x2& r, const void* sbase, u32 voff, int goff) {
  u32 t;
  asm volatile("v_add_u32 %1, %4, %2\n\tglobal_load_dwordx2 %0, %1, %3 sc1\n\ts_waitcnt vmcnt(0)" : "+v"(r), "=&v"(t) : "v"(voff), "s"(sbase), "i"(goff) : "memory");
}
__device__ __forceinline__ u32 mx4(u32x4 v) { return umax(umax(v[0], v[1]), umax(v[2], v[3])); }
__device__ __forceinline__ u32 mx2(u32x2 v) { return umax(v[0], v[1]); }
__device__ __forceinline__ bool unset(u32 m) { return __any(m == 0xffffffffu); }
__device__ __forceinline__ u32 san(u32 p) { return umin(p, 0xfffffffeu); }
constexpr int SPIN = 1 << 16;
__device__ __forceinline__ float qsum(float a) {
  auto r = __builtin_amdgcn_permlane16_swap(__float_as_uint(a), __float_as_uint(a), false, false);
  a = __uint_as_float(r[0]) + __uint_as_float(r[1]);
  auto r2 = __builtin_amdgcn_permlane32_swap(__float_as_uint(a), __float_as_uint(a), false, false);
  return __uint_as_float(r2[0]) + __uint_as_float(r2[1]);
}
template <class T> __device__ __forceinline__ void pin(T& x) { asm volatile("" : "+v"(x)); }
template <class T> __device__ __forceinline__ void pina(T& x) { asm volatile("" : "+a"(x)); }
#define STAMP(kid, idx)
template <int N> __device__ __forceinline__ void wait_vm() { asm volatile("s_waitcnt vmcnt(%0)" ::"i"(N) : "memory"); }
#define SCHED_FENCE() __builtin_amdgcn_sched_barrier(0)
template <class Fn, int... I> __device__ __forceinline__ void sfor_impl(std::integer_sequence<int, I...>, Fn&& f) {
  (f(std::integral_constant<int, I>{}), ...);
}
template <int N, class Fn> __device__ __forceinline__ void sfor(Fn&& f) { sfor_impl(std::make_integer_sequence<int, N>{}, f); }
template <int N> __device__ __forceinline__ void wait_vm_bar() { asm volatile("s_waitcnt vmcnt(%0)\n\ts_barrier" ::"i"(N) : "memory"); }

__device__ __forceinline__ f32x2 gelu2(f32x2 x) {
  const float c0 = -2.0f * 0.7978845608028654f * 1.4426950408889634f;
  const float c1 = c0 * 0.044715f;
  const f32x2 z = x * __builtin_elementwise_fma(x * x, f32x2{c1, c1}, f32x2{c0, c0});
  const f32x2 d = f32x2{__builtin_amdgcn_exp2f(z.x), __builtin_amdgcn_exp2f(z.y)} + 1.0f;
  return x * f32x2{__builtin_amdgcn_rcpf(d.x), __builtin_amdgcn_rcpf(d.y)};
}
__device__ __forceinline__ u32x4 widen2(u32x2 p0, u32x2 p1);
__device__ __forceinline__ u32x4 widen(u32x2 p) {
  auto a = __builtin_amdgcn_permlane16_swap(p.x, p.x, false, false);
  auto b = __builtin_amdgcn_permlane16_swap(p.y, p.y, false, false);
  return u32x4{(u32)a[0], (u32)b[0], (u32)a[1], (u32)b[1]};
}

__device__ __forceinline__ u32x4 widen2(u32x2 p0, u32x2 p1) {
  auto a = __builtin_amdgcn_permlane16_swap(p0.x, p1.x, false, false);
  auto b = __builtin_amdgcn_permlane16_swap(p0.y, p1.y, false, false);
  return u32x4{(u32)a[0], (u32)b[0], (u32)a[1], (u32)b[1]};
}
__device__ __forceinline__ f32x2 cmul(f32x2 a, f32x2 b) {
  f32x2 r;
  asm("v_pk_mul_f32 %0, %1, %2 op_sel_hi:[1,0]\n\t"
      "v_pk_fma_f32 %0, %1, %2, %0 op_sel:[1,1,0] op_sel_hi:[0,1,1] neg_lo:[1,0,0]" : "=&v"(r) : "v"(a), "v"(b));
  return r;
}
__device__ __forceinline__ u32 cmulh(u32 a, u32 b) {
  u32 r;
  asm("v_pk_mul_f16 %0, %1, %2 op_sel_hi:[1,0]\n\t"
      "v_pk_fma_f16 %0, %1, %2, %0 op_sel:[1,1,0] op_sel_hi:[0,1,1] neg_lo:[1,0,0]" : "=&v"(r) : "v"(a), "v"(b));
  return r;
}
template <int CTRL> __device__ __forceinline__ u32 dpp(u32 v) { return (u32)__builtin_amdgcn_update_dpp(0, (int)v, CTRL, 0xf, 0xf, true); }
__device__ __forceinline__ float rowsum16(float v) {
  v += __uint_as_float(dpp<0xB1>(__float_as_uint(v)));
  v += __uint_as_float(dpp<0x4E>(__float_as_uint(v)));
  v += __uint_as_float(dpp<0x141>(__float_as_uint(v)));
  v += __uint_as_float(dpp<0x140>(__float_as_uint(v)));
  return v;
}
__device__ __forceinline__ u32 wavemax(u32 v) {
  v = umax(v, dpp<0xB1>(v)); v = umax(v, dpp<0x4E>(v)); v = umax(v, dpp<0x141>(v)); v = umax(v, dpp<0x140>(v));
  auto r = __builtin_amdgcn_permlane16_swap(v, v, false, false);
  v = umax((u32)r[0], (u32)r[1]);
  auto r2 = __builtin_amdgcn_permlane32_swap(v, v, false, false);
  return umax((u32)r2[0], (u32)r2[1]);
}
__device__ __forceinline__ f32x2 cexpp(float rho, float th, float p) {
  float mag = __builtin_amdgcn_exp2f(rho * p);
  float ph = __builtin_amdgcn_fractf(th * p);
  return f32x2{mag * __builtin_amdgcn_cosf(ph), mag * __builtin_amdgcn_sinf(ph)};
}

#define PGS (512u * 1024u)
constexpr int CV_KC = 4160;
constexpr int CV_U = 4608;
constexpr int CV_CH = 4 * CV_KC + CV_U + 32;
constexpr int K2_LDS = 160 * 1024;
constexpr int KB = 1024;

__device__ __forceinline__ u32x2 dsr64(u32 a, int off) {
  u32x2 r;
  asm volatile("ds_read_b64 %0, %1 offset:%2" : "=v"(r) : "v"(a), "i"(off));
  return r;
}
__device__ __forceinline__ u32x4 dsr128(u32 a, int off) {
  u32x4 r;
  asm volatile("ds_read_b128 %0, %1 offset:%2" : "=a"(r) : "v"(a), "i"(off));
  return r;
}

#define CV_ACC "+a"(acc[0]), "+a"(acc[1]), "+a"(acc[2]), "+a"(acc[3]), "+a"(acc[4]), "+a"(acc[5]), "+a"(acc[6]), "+a"(acc[7])
#define CV_CASE(S)                                                                                                                        \
  if constexpr (SI == S) {                                                                                                                \
    if constexpr (KIND == 0) asm volatile("v_mfma_f32_16x16x32_f16 %" #S ", %8, %9, %" #S : CV_ACC : "v"(a), "a"(b));                     \
    else if constexpr (KIND == 1)                                                                                                         \
      asm volatile("v_mfma_f32_16x16x32_f16 %" #S ", %9, %10, %" #S "\n\tds_read_b64 %8, %11 offset:%12"                                 \
                   : CV_ACC, "=&v"(d2) : "v"(a), "a"(b), "v"(ad), "i"(OFF));                                                              \
    else                                                                                                                                  \
      asm volatile("v_mfma_f32_16x16x32_f16 %" #S ", %9, %10, %" #S "\n\tds_read_b128 %8, %11 offset:%12"                                \
                   : CV_ACC, "=&a"(d4) : "v"(a), "a"(b), "v"(ad), "i"(OFF));                                                              \
  }
template <int SI, int KIND, int OFF>
__device__ __forceinline__ void cv_step(f32x4 (&acc)[8], u32x4 a, u32x4 b, u32 ad, u32x2& d2, u32x4& d4) {
  CV_CASE(0) CV_CASE(1) CV_CASE(2) CV_CASE(3) CV_CASE(4) CV_CASE(5) CV_CASE(6) CV_CASE(7)
}
constexpr int cv_a(int cnt) { if (cnt == 34) return 7; if (cnt == 35) return 6; int a = 0; while (cnt >= 8 - a) { cnt -= 8 - a; ++a; } return a; }
constexpr int cv_s(int cnt) { if (cnt == 33 || cnt == 34) return cnt == 33 ? 6 : 7; if (cnt == 35) return 7; int a = 0; while (cnt >= 8 - a) { cnt -= 8 - a; ++a; } return a + cnt; }

constexpr int wpos(int m, int ch) {
  return m == 1 ? (ch < 3 ? (100 + 20 * ch) * KB : 20 * (ch - 3) * KB)
       : m == 2 ? (40 + 20 * ch) * KB
       : m == 3 ? 28 * ch * KB
       : ((ch + 7) & 7) * 20 * KB;
}

struct Ctx {
  char* ws; u16* out;
  u32 sb, vo;
  int lane, w, xcd, bi;
  u32 tlo, thi;
  const char* Wg[5];
  u32 wo[3];

  float m, r;
  u32x2 hv[3], xv[3];
};

template <int M> __device__ __forceinline__ void pf_piece(const Ctx& c, int ch, int i) {
  constexpr int CHB = (M == 3 ? 28 : 20) * KB, NW = M == 3 ? 4 : 3;
  const u32 wl = c.sb + c.w * (CHB / 8);
  const u32 v = M == 1 ? c.wo[i] : c.vo;
  const int go = M == 1 ? ch * 128 * D * 2 : ch * CHB + i * 1024;
  if (i == NW - 1) glds16_half<(M != 1)>(c.Wg[M], v, go, wl, wpos(M, ch) + i * 1024);
  else glds16<(M != 1)>(c.Wg[M], v, go, wl, wpos(M, ch) + i * 1024);
}
template <int M> __device__ __forceinline__ void pf_chunk(const Ctx& c, int ch) {
#pragma unroll
  for (int i = 0; i < (M == 3 ? 4 : 3); ++i) pf_piece<M>(c, ch, i);
}

template <int M> __device__ __forceinline__ void pf7_chunk(const Ctx& c, int ch) {
  constexpr int CHB = (M == 3 ? 28 : 20) * KB, NU = M == 3 ? 4 : 3;
  const char* base = c.Wg[M] - (M == 1 ? c.w * 16 * D * 2 : c.w * (CHB / 8));
#pragma unroll
  for (int i = 0; i < NU; ++i) {
    int u = (c.w - 1) * NU + i; u = u < CHB / 1024 ? u : CHB / 1024 - 1;
    u32 v = c.vo + u * 1024;
    if (M == 1) { const int x = u * 64 + c.lane, r = x / 10, cc = x - r * 10; v = (u32)((r ^ ((r >> 1) & 4)) * (D * 2) + cc * 16); }
    glds16<(M != 1)>(base, v, M == 1 ? ch * 128 * D * 2 : ch * CHB, c.sb + u * 1024, wpos(M, ch));
  }
}
#define DRAIN_BARRIER() asm volatile("s_waitcnt vmcnt(0) lgkmcnt(0)\n\ts_barrier" ::: "memory")
__device__ __forceinline__ void post(const Ctx& c, int n, int wg, int sec, int slot) {
  if ((int)threadIdx.x < n) gstore8(c.ws + O_MB, (u32)(wg * 2048 + sec + slot * 8), u32x2{c.tlo, c.thi});
}
__device__ __forceinline__ void await(const Ctx& c, int sec, int n) {
  if (c.w == 0) {
    const u32 off = (u32)(sec + (c.lane < n ? c.lane : 0) * 8);
    for (int sp = 0; sp < SPIN; ++sp) {
      u32x2 v;
      asm volatile("global_load_dwordx2 %0, %1, %2 sc1\n\ts_waitcnt vmcnt(0)" : "=v"(v) : "v"(off), "s"(c.ws + O_MB + (size_t)blockIdx.x * 2048) : "memory");
      if (__all(v.x == c.tlo && v.y == c.thi)) break;
    }
  }
}
#define WG_BARRIER() asm volatile("s_waitcnt lgkmcnt(0)\n\ts_barrier" ::: "memory")

struct In { const u16 *x, *lar, *aim, *cre, *cim, *ldt, *dsk, *w1, *w2, *w3, *w4; };

constexpr int LN_T = 100 * KB, LN_P = 1280 + 16, LN_ST = LN_T + 32 * LN_P;
__device__ __forceinline__ void ln_compute(const Ctx& c, u32x4 (&v)[5]) {
  const int row = c.w * 4 + (c.lane >> 4), i = c.lane & 15;
  const bf16x2 ones = {(__bf16)1.0f, (__bf16)1.0f};
  float s1 = 0.f, s2 = 0.f;
#pragma unroll
  for (int s = 0; s < 5; ++s)
#pragma unroll
    for (int k = 0; k < 4; ++k) {
      s1 = __builtin_amdgcn_fdot2_f32_bf16(as_bf2(v[s][k]), ones, s1, false);
      s2 = __builtin_amdgcn_fdot2_f32_bf16(as_bf2(v[s][k]), as_bf2(v[s][k]), s2, false);
    }
  const float m = rowsum16(s1) * (1.0f / D);
  const float ve = fmaxf(rowsum16(s2) * (1.0f / D) - m * m, 0.f) + 1e-5f;
  const float r = __builtin_amdgcn_rsqf(ve), mr = -m * r;
  if (i == 0) lds_st(c.sb + LN_ST + row * 8, f32x2{m, ve * r});
#pragma unroll
  for (int s = 0; s < 5; ++s) {
    u32x4 o;
#pragma unroll
    for (int k = 0; k < 4; ++k) o[k] = pkh(bflo(v[s][k]) * r + mr, bfhi(v[s][k]) * r + mr);
    lds_st(c.sb + LN_T + row * LN_P + (i + 16 * s) * 16, o);
  }
}
__device__ __forceinline__ void ln_store(const Ctx& c, int j) {
  const int lane = c.lane, gi = lane >> 4, qq = (lane & 15) >> 2, p = lane & 3;
  char* dst = c.ws + O_UT + (size_t)(252 - 4 * j) * 64;
#pragma unroll
  for (int it = 0; it < 5; ++it) {
    const int cgp = (it * 8 + c.w) * 4 + gi;
    const u32 base = c.sb + LN_T + (31 - 8 * p - qq) * LN_P + cgp * 8;
    s16x4 a = __builtin_amdgcn_ds_read_tr16_b64_v4i16((LDS_AS s16x4*)(size_t)base);
    s16x4 b = __builtin_amdgcn_ds_read_tr16_b64_v4i16((LDS_AS s16x4*)(size_t)(base - 4 * LN_P));
    u32x2 ua = __builtin_bit_cast(u32x2, a), ub = __builtin_bit_cast(u32x2, b);
    gstore16(dst, (u32)(cgp * 16384 + (lane & 15) * 16), u32x4{ua.x, ua.y, ub.x, ub.y});
  }
  if (threadIdx.x < 16) gstore16(c.ws + O_ST0 + j * 256, threadIdx.x * 16, lds_ld<u32x4>(c.sb + LN_ST + threadIdx.x * 16));
}

struct TU { const u16* W; char* Wt; int N, KS, sb2, strip, ks0, nks, wk; };
__device__ __forceinline__ TU tu_of(const In& in, char* ws, int u) {
  TU t;
  if (u < 20)      t = TU{in.w2, ws + O_W2, D, 20, 2, u >> 1, (u & 1) * 10, 10, 0};
  else if (u < 74) t = TU{in.w3, ws + O_W3, F, 20, 1, u - 20, 0, 20, 1};
  else if (u < 134) t = TU{in.w4, ws + O_W4, D, 54, 2, (u - 74) / 6, ((u - 74) % 6) * 9, 9, 0};
  else             t = TU{in.w1, nullptr, D, 20, 1, u - 134, 0, 20, 2};
  return t;
}
__device__ __forceinline__ void tu_load(const Ctx& c, const TU& t, u32 lds) {
  const int lpr2 = t.sb2 + 1;
  const char* src = (const char*)(t.W + (size_t)t.ks0 * 32 * t.N + (t.strip << (t.sb2 + 4)));
  const u32 voff = (u32)(c.lane >> lpr2) * (u32)t.N * 2u + (u32)(c.lane & ((1 << lpr2) - 1)) * 16u;
  const int ni = t.nks << t.sb2;
  const u32 step = (u32)(64 >> lpr2) * (u32)t.N * 2u;
#pragma unroll
  for (int j = 0; j < 5; ++j) {
    int i = c.w + j * 8; i = i < ni ? i : c.w;
    glds16r(src, voff, (u32)i * step, lds + i * 1024);
  }
}
__device__ __forceinline__ void tu_emit(const Ctx& c, const TU& t, u32 lds, u32 red) {
  const int lane = c.lane, w = c.w;
  const int RB = 32 << t.sb2;
  const int e = w & ((1 << t.sb2) - 1);
  const int g = lane >> 4, q = (lane & 15) >> 2, p = lane & 3;
  const int B = (t.strip << t.sb2) + e;
  int ct, f;
  if (t.wk == 1) { const int hh = B >= 54 ? 1 : 0, b2 = B - hh * 54, c8 = b2 < 48 ? b2 / 7 : 7; ct = hh * 8 + c8; f = c8 <= 6 ? b2 - c8 * 7 : b2 - 48; }
  else { ct = B / 5; f = B - ct * 5; }
  const int kstr = t.wk == 1 ? 7168 : 5120;
  char* dst = t.Wt + (size_t)(ct * t.KS + t.ks0) * kstr + f * 1024;
  const int k0 = w >> t.sb2, kst = 8 >> t.sb2;
  float acc = 0.f;
  const bf16x2 ones = {(__bf16)1.0f, (__bf16)1.0f};
  u32x4 v[5];
#pragma unroll
  for (int j = 0; j < 5; ++j) {
    int ks = k0 + j * kst; ks = ks < t.nks ? ks : k0;
    const u32 base = lds + (ks * 32 + 8 * g + q) * RB + e * 32 + p * 8;
    s16x4 a = __builtin_amdgcn_ds_read_tr16_b64_v4i16((LDS_AS s16x4*)(size_t)base);
    s16x4 b = __builtin_amdgcn_ds_read_tr16_b64_v4i16((LDS_AS s16x4*)(size_t)(base + 4 * RB));
    u32x2 ua = __builtin_bit_cast(u32x2, a), ub = __builtin_bit_cast(u32x2, b);
    v[j] = u32x4{ua.x, ua.y, ub.x, ub.y};
  }
#pragma unroll
  for (int j = 0; j < 5; ++j) {
    int ks = k0 + j * kst; ks = ks < t.nks ? ks : k0;
    if (t.wk != 2) gstore16(dst, (u32)(ks * kstr + lane * 16), v[j]);
#pragma unroll
    for (int k = 0; k < 4; ++k) acc = __builtin_amdgcn_fdot2_f32_bf16(as_bf2(v[j][k]), ones, acc, false);
  }
  if (t.wk) {
    acc = qsum(acc);
    if (lane < 16) lds_st(red + (w * 16 + lane) * 4, acc);
    WG_BARRIER();
    if (threadIdx.x < 32) {
      const u32 a = red + threadIdx.x * 4;
      gstore4(c.ws + (t.wk == 1 ? O_CS3 : O_CS1) + t.strip * 128, threadIdx.x * 4,
              __float_as_uint((lds_ld<float>(a) + lds_ld<float>(a + 128)) + (lds_ld<float>(a + 256) + lds_ld<float>(a + 384))));
    }
  }
}

constexpr int KG_SZ = 28 * KB, KG_KT = 112 * KB, TP = 528;
struct KP { u32 ldt, lar, aim, cr, ci; };
__device__ __forceinline__ u32 gload2(const void* sbase, u32 voff) {
  u32 r;
  asm volatile("global_load_ushort %0, %1, %2" : "=v"(r) : "v"(voff), "s"(sbase) : "memory");
  return r;
}
__device__ __forceinline__ u32 gload4s(const void* sbase, u32 voff) {
  u32 r;
  asm volatile("global_load_dword %0, %1, %2 sc1" : "=v"(r) : "v"(voff), "s"(sbase) : "memory");
  return r;
}
__device__ __forceinline__ u32 gload4(const void* sbase, u32 voff) {
  u32 r;
  asm volatile("global_load_dword %0, %1, %2" : "=v"(r) : "v"(voff), "s"(sbase) : "memory");
  return r;
}
__device__ __forceinline__ void kg_setA(float rho, float th, f32x2 C, u32 (&a)[21]) {
  f32x2 A[8];
  A[0] = C;
  const f32x2 E1 = cexpp(rho, th, 1.f);
  const f32x2 G1 = cexpp(rho, th, 256.f);
  const f32x2 G4 = cexpp(rho, th, 1024.f);
  const f32x2 E2 = cmul(E1, E1), E4 = cmul(E2, E2);
  A[1] = cmul(A[0], E1); A[2] = cmul(A[0], E2); A[3] = cmul(A[1], E2);
  A[4] = cmul(A[0], E4); A[5] = cmul(A[1], E4); A[6] = cmul(A[2], E4); A[7] = cmul(A[3], E4);
  const f32x2 G2 = cmul(G1, G1), G3 = cmul(G2, G1), A16 = cmul(G4, A[0]);
#pragma unroll
  for (int k = 0; k < 8; ++k) a[k] = pkh(A[k].x, A[k].y);
  const u32 g1 = pkh(G1.x, G1.y), g2 = pkh(G2.x, G2.y), g3 = pkh(G3.x, G3.y), g4 = pkh(G4.x, G4.y);
  a[16] = pkh(A16.x, A16.y);
  a[8] = cmulh(g1, a[0]); a[9] = cmulh(g1, a[2]); a[10] = cmulh(g1, a[4]); a[11] = cmulh(g1, a[6]);
  a[12] = cmulh(g2, a[0]); a[13] = cmulh(g2, a[4]); a[14] = cmulh(g3, a[0]); a[15] = cmulh(g3, a[4]);
  a[17] = cmulh(g1, a[16]); a[18] = cmulh(g2, a[16]); a[19] = cmulh(g3, a[16]); a[20] = cmulh(g4, a[16]);
}
__device__ __forceinline__ void kg_setB(float rho, float th, u32 (&b)[32]) {
  f32x2 lo[8], hi[4];
  lo[1] = cexpp(rho, -th, 8.f);
  hi[1] = cexpp(rho, -th, 64.f);
  lo[2] = cmul(lo[1], lo[1]); lo[3] = cmul(lo[2], lo[1]); lo[4] = cmul(lo[2], lo[2]);
  lo[5] = cmul(lo[4], lo[1]); lo[6] = cmul(lo[4], lo[2]); lo[7] = cmul(lo[4], lo[3]);
  hi[2] = cmul(hi[1], hi[1]); hi[3] = cmul(hi[2], hi[1]);
  b[0] = 0x00003c00u;
#pragma unroll
  for (int k = 1; k < 8; ++k) b[k] = pkh(lo[k].x, lo[k].y);
#pragma unroll
  for (int j = 1; j < 4; ++j) {
    b[8 * j] = pkh(hi[j].x, hi[j].y);
#pragma unroll
    for (int k = 1; k < 8; ++k) b[8 * j + k] = cmulh(b[8 * j], b[k]);
  }
}
__device__ __forceinline__ float kg_tables(const KP& p, u32 tab, int set, int lane) {
  const float LOG2E = 1.4426950408889634f;
  const int sh = set ? 0 : 16;
  const float dt = rbf(__builtin_amdgcn_exp2f(bflo(p.ldt) * LOG2E));
  const float rho = (-rbf(__builtin_amdgcn_exp2f(bfhi(p.lar << sh) * LOG2E)) * dt) * LOG2E;
  const float th = (bfhi(p.aim << sh) * dt) * 0.15915494309189535f;
  const u32 mx = wavemax(umax(umax(p.cr & 0x7fffu, (p.cr >> 16) & 0x7fffu), umax(p.ci & 0x7fffu, (p.ci >> 16) & 0x7fffu)));
  u32 eb = (mx >> 7) + 1u;
  eb = eb < 2u ? 2u : (eb > 252u ? 252u : eb);
  const float sc = __uint_as_float(eb << 23), isc = __uint_as_float((254u - eb) << 23);
  const u32 ta = tab + set * 256 + lane * 4;
  u32 a[21], b[32];
  kg_setA(rho, th, f32x2{bfhi(p.cr << sh) * isc, bfhi(p.ci << sh) * isc}, a);
  kg_setB(rho, th, b);
#pragma unroll
  for (int k = 0; k < 21; ++k) asm volatile("ds_write_b32 %0, %1 offset:%2" :: "v"(ta), "v"(a[k]), "i"(k * TP) : "memory");
#pragma unroll
  for (int k = 0; k < 32; ++k) asm volatile("ds_write_b32 %0, %1 offset:%2" :: "v"(ta), "v"(b[k]), "i"((21 + k) * TP) : "memory");
  return 2.0f * dt * sc;
}
struct KI { u32 pa, pb, o[4], ob; };
__device__ __forceinline__ KI kg_index(u32 tab, u32 ktb, int at, int lane) {
  const int r = lane & 15, g = lane >> 4;
  int ra = at * 16 + r; ra = ra > 20 ? 20 : ra;
  KI k;
  k.pa = tab + ra * TP + g * 16; k.pb = tab + (21 + r) * TP + g * 16;
  const int sbs = at == 0 ? (g < 2 ? 8 : (g == 2 ? 4 : 2)) : 1;
  const int base = at == 0 ? (g < 2 ? 4 * g : (g == 2 ? 256 : 384)) : (g == 0 ? 512 : 640 + (g > 1 ? 64 : 0));
  const int sk = at == 0 ? 1 : (g == 0 ? 32 : 64), x2 = (at == 0 && g == 3) ? 62 : 0;
  const u32 o = ktb + (base + r * sbs) * 2;
  k.o[0] = o; k.o[1] = o + sk * 2; k.o[2] = o + (2 * sk + x2) * 2; k.o[3] = o + (3 * sk + x2) * 2;
  k.ob = sbs * 32;
  pin(k.pa); pin(k.pb); pin(k.o[0]); pin(k.o[1]); pin(k.o[2]); pin(k.o[3]); pin(k.ob);
  return k;
}
__device__ __forceinline__ void kg_mfma(const KI& k) {
  f32x4 acc0 = {0.f, 0.f, 0.f, 0.f}, acc1 = acc0;
  f16x8 a[8], b0[8], b1[8];
#pragma unroll
  for (int s = 0; s < 8; ++s) { a[s] = lds_ld<f16x8>(k.pa + s * 64); b0[s] = lds_ld<f16x8>(k.pb + s * 64); b1[s] = lds_ld<f16x8>(k.pb + 16 * TP + s * 64); }
  SCHED_FENCE();
#pragma unroll
  for (int s = 0; s < 8; ++s) {
    acc0 = __builtin_amdgcn_mfma_f32_16x16x32_f16(a[s], b0[s], acc0, 0, 0, 0);
    acc1 = __builtin_amdgcn_mfma_f32_16x16x32_f16(a[s], b1[s], acc1, 0, 0, 0);
  }
  asm volatile("s_nop 7\n\ts_nop 7" : "+a"(acc0), "+a"(acc1));
#pragma unroll
  for (int e = 0; e < 4; ++e) { lds_st(k.o[e], (f16)acc0[e]); lds_st(k.o[e] + k.ob, (f16)acc1[e]); }
}
__device__ __forceinline__ void kg_lagidx(int t, u32 kt, u32 (&ix)[11]) {
#pragma unroll
  for (int k = 0; k < 11; ++k) {
    const int lag = t * 8 + k;
    const int tt = k < 8 ? t : t + 1;
    const int s = tt < 32 ? 0 : (tt < 64 ? 1 : (tt < 128 ? 2 : 3));
    const int add = ((1 << s) >> 1) - (s > 0 ? 1 : 0);
    ix[k] = kt + (128 * s + ((lag + add + ((lag >> s) & (s > 0 ? 1 : 0))) >> s)) * 2;
    pin(ix[k]);
  }
}
__device__ __forceinline__ void kg_expand(const u32 (&v)[11], u32 kc, int t) {
  u32 e[5], o[5];
#pragma unroll
  for (int k = 0; k < 5; ++k) { e[k] = v[2 * k] | (v[2 * k + 1] << 16); o[k] = v[2 * k + 1] | (v[2 * k + 2] << 16); }
  const u32 a = kc + 32 + t * 16;
  lds_st(a, u32x4{e[0], e[1], e[2], e[3]});
  lds_st(a + CV_KC, u32x4{o[0], o[1], o[2], o[3]});
  lds_st(a + 2 * CV_KC, u32x4{e[1], e[2], e[3], e[4]});
  lds_st(a + 3 * CV_KC, u32x4{o[1], o[2], o[3], o[4]});
  if (t == 0) {
    const u32x4 z = {0u, 0u, 0u, 0u};
#pragma unroll
    for (int cc = 0; cc < 4; ++cc) lds_st(kc + cc * CV_KC, z);
    lds_st(kc + 16, z);
    lds_st(kc + CV_KC + 16, u32x4{0u, 0u, 0u, v[0] << 16});
    lds_st(kc + 2 * CV_KC + 16, u32x4{0u, 0u, 0u, e[0]});
    lds_st(kc + 3 * CV_KC + 16, u32x4{0u, 0u, v[0] << 16, o[0]});
  }
}

__device__ __forceinline__ void conv_phase(Ctx& c, u32x2 dsv, int cg, f32x4 ksc, u32x4 uv0, u32x4 uv1, u32x4 (&stv)[2]) {
  char* ws = c.ws;
  const int lane = c.lane, w = c.w;
  const int ch = w & 3, hh = w >> 2;
  const u32 sb = c.sb;
  const u32 mya = sb + ch * CV_CH;
  const int r = lane & 15, g = lane >> 4;
  wait_vm<2>();
  pin(uv0); pin(uv1);
  {
    const u32 ua = sb + (lane & 3) * CV_CH + 4 * CV_KC + (w * 32 + (lane >> 2)) * 16;
    lds_st(ua, uv0); lds_st(ua + 256, uv1);
  }
  WG_BARRIER();
  STAMP(1, 2);

  const u32 pa = mya + ((r + 1) & 3) * CV_KC + (((r + 1) & ~3) + 8 * g) * 2 + hh * 256;
  const u32 pb = mya + 4 * CV_KC + (2032 - 16 * r + 8 * g) * 2 - 7 * 512 + hh * 256;
  f32x4 acc[8];
#pragma unroll
  for (int i = 0; i < 8; ++i) { acc[i] = f32x4{0.f, 0.f, 0.f, 0.f}; pina(acc[i]); }
  u32x2 Al[2][8], Ah[2][8];
  u32x4 B[2][8], w1r[5];
#pragma unroll
  for (int a = 0; a < 8; ++a) {
    Al[0][a] = dsr64(pa, a * 512); Ah[0][a] = dsr64(pa, a * 512 + 8);
    B[0][a] = dsr128(pb, (7 - a) * 512);
    if (a == 3) asm volatile("s_waitcnt lgkmcnt(6)" ::: "memory");
  }
  sfor<4>([&](auto R_) __attribute__((always_inline)) {
    constexpr int rho = decltype(R_)::value, cur = rho & 1, nxt = cur ^ 1;
    asm volatile("s_waitcnt lgkmcnt(0)" ::: "memory");
#pragma unroll
    for (int a = 0; a < 8; ++a) { pin(Al[cur][a]); pin(Ah[cur][a]); pina(B[cur][a]); }
    SCHED_FENCE();
    sfor<36>([&](auto C_) __attribute__((always_inline)) {
      constexpr int cnt = decltype(C_)::value, s = cv_s(cnt), a = cv_a(cnt), i = cnt < 24 ? cnt / 3 : 0, kind = cnt % 3;
      constexpr bool rd = rho < 3 && cnt < 24;
      const u32x4 av = {Al[cur][a].x, Al[cur][a].y, Ah[cur][a].x, Ah[cur][a].y};
      if constexpr (!rd) cv_step<s, 0, 0>(acc, av, B[cur][s - a], pa, Al[nxt][0], B[nxt][0]);
      else if constexpr (kind == 0) cv_step<s, 1, i * 512 + (rho + 1) * 64>(acc, av, B[cur][s - a], pa, Al[nxt][i], B[nxt][i]);
      else if constexpr (kind == 1) cv_step<s, 1, i * 512 + (rho + 1) * 64 + 8>(acc, av, B[cur][s - a], pa, Ah[nxt][i], B[nxt][i]);
      else cv_step<s, 2, (7 - i) * 512 + (rho + 1) * 64>(acc, av, B[cur][s - a], pb, Al[nxt][i], B[nxt][i]);
      if (rho < 3 && cnt >= 25 && cnt < 34 && (cnt - 25) % 3 == 0) pf_piece<1>(c, rho, (cnt - 25) / 3);
      if (rho == 3 && cnt < 25 && cnt % 5 == 0) {
        const int x = (w * 5 + cnt / 5) * 64 + lane, rr = x / 10, cc = x - rr * 10;
        w1r[cnt / 5] = gload16ap(c.Wg[1] - w * 16 * D * 2 + 3 * 128 * D * 2, (u32)((rr ^ ((rr >> 1) & 4)) * (D * 2) + cc * 16));
      }
      SCHED_FENCE();
    });
  });
  asm volatile("s_nop 7\n\ts_nop 7" : "+a"(acc[0]), "+a"(acc[1]), "+a"(acc[2]), "+a"(acc[3]), "+a"(acc[4]), "+a"(acc[5]), "+a"(acc[6]), "+a"(acc[7]));
  STAMP(1, 3);
  asm volatile("s_barrier" ::: "memory");
  {
    const u32 ex = mya + hh * 8192 + g * 256 + ((r ^ (4 * g)) * 16);
#pragma unroll
    for (int i = 0; i < 8; ++i) asm volatile("ds_write_b128 %0, %1 offset:%2" :: "v"(ex), "a"(acc[i]), "i"(i * 1024) : "memory");
  }
  WG_BARRIER();
  STAMP(1, 6);
  wait_vm<14>();
  {
    const int d0 = cg * 4;
    char* hb = ws + O_H + (d0 >> 5) * 1024 + ((d0 >> 2) & 1) * 512 + ((d0 & 31) >> 3) * 128;
    const float dk[4] = {bflo(dsv.x), bfhi(dsv.x), bflo(dsv.y), bfhi(dsv.y)};
#pragma unroll
    for (int i = 0; i < 2; ++i) {
      const int l = w * 256 + i * 128 + lane * 2;
      pina(stv[i]);
      const float m0 = __uint_as_float(stv[i][0]), sd0 = __uint_as_float(stv[i][1]);
      const float m1 = __uint_as_float(stv[i][2]), sd1 = __uint_as_float(stv[i][3]);
      float v0[4], v1[4];
      const int eg = (l >> 2) & 3;
      const u32 eo = (u32)((l >> 8) * 1024 + eg * 256 + ((((l >> 4) & 15) ^ (4 * eg)) * 16) + (l & 3) * 4);
#pragma unroll
      for (int k = 0; k < 4; ++k) {
        const f16x2 u = lds_ld<f16x2>(sb + k * CV_CH + 4 * CV_KC + (2046 - l) * 2);
        const f32x2 ya = lds_ld<f32x2>(sb + k * CV_CH + eo), yb = lds_ld<f32x2>(sb + k * CV_CH + 8192 + eo);
        v0[k] = (ya.x + yb.x) * ksc[k] + (m0 + (sd0 + dk[k]) * (float)u[1]);
        v1[k] = (ya.y + yb.y) * ksc[k] + (m1 + (sd1 + dk[k]) * (float)u[0]);
      }
      gstore16(hb, (u32)((l >> 4) * 20480 + (l & 15) * 8),
               u32x4{pkbf(v0[0], v0[1]), pkbf(v0[2], v0[3]), pkbf(v1[0], v1[1]), pkbf(v1[2], v1[3])});
    }
  }
  DRAIN_BARRIER();
  post(c, 1, 248 + c.xcd, MB_AC, c.bi - 8);
#pragma unroll
  for (int i = 0; i < 5; ++i) asm volatile("ds_write_b128 %0, %1 offset:%2" :: "v"(sb + w * 5120 + c.vo), "a"(w1r[i]), "i"(i * 1024) : "memory");
  asm volatile("s_waitcnt lgkmcnt(0)" ::: "memory");
}

template <int MODE> struct GP {
  static constexpr int NB = MODE == 3 ? 7 : 5;
  static constexpr int KS = MODE == 4 ? 54 : 20;
  static constexpr int SPC = MODE == 3 ? 4 : 2;
  static constexpr int NS = MODE == 4 ? 27 : (MODE == 3 ? 20 : 10);
  static constexpr int LPX = MODE == 1 ? 2 : 1;
  static constexpr int A0 = MODE == 1 ? 3 : 4;
  static constexpr int AH = MODE <= 2 ? 6 : 10;
  static constexpr int hi(int s) {
    int h = A0 + 2 * s; h = h < s + AH ? h : s + AH; return h < NS ? h : NS;
  }
  static constexpr int pfm(int s) {
    return MODE == 1 ? ((s == 6 || s == 7) ? 2 : 0) : MODE == 2 ? ((s == 6 || s == 7) ? 3 : 0)
         : MODE == 3 ? ((s == 6 || s == 8 || s == 10 || s == 12 || s == 14) ? 4 : 0)
         : ((s >= 4 && s <= 14 && !(s & 1)) ? 4 : 0);
  }
  static constexpr int pfc(int s) { return MODE == 1 ? s - 3 : MODE == 2 ? s - 5 : MODE == 3 ? (s - 6) / 2 : 8 + (s - 4) / 2; }
  static constexpr int npf(int s) { return pfm(s) == 0 ? 0 : (pfm(s) == 3 ? 4 : 3); }
  static constexpr int nx(int s) { return (hi(s + 1) - hi(s)) * LPX; }
  static constexpr int before(int s) { int n = A0 * LPX; for (int i = 0; i < s; ++i) n += nx(i) + npf(i); return n; }
  static constexpr int xend(int x) {
    if (x < A0) return (x + 1) * LPX;
    int s = 0; while (hi(s + 1) <= x) ++s;
    return before(s) + (x - hi(s) + 1) * LPX;
  }
  static constexpr bool bar(int s) {
    return MODE == 1 ? (s == 4 || s == 6) : MODE == 2 ? s == 6 : MODE == 3 ? (s == 8 || s == 12)
         : (s == 4 || s == 8 || s == 12 || s == 15 || s == 19 || s == 23);
  }
  static constexpr int waitn(int s) {
    int n = before(s) - xend(s);
    if (MODE == 4 && (s == 15 || s == 19 || s == 23)) {
      const int lim = before(s) - before(s - 8);
      n = n < lim ? n : lim;
    }
    return n;
  }
  static constexpr int SCR = (MODE <= 2 ? 140 : 0) * KB;
};

struct Tile { int tb, ct, kh, Bp, Bs, oA, oS, oB; bool has_s; };
template <int MODE> __device__ __forceinline__ Tile tile_of(const Ctx& cx) {
  Tile t;
  t.kh = MODE == 3 ? 0 : cx.w >> 2;
  if (MODE == 3) {
    t.ct = cx.bi & 15;
    t.tb = (cx.xcd * 2 + (cx.bi >> 4)) * 8 + cx.w;
    const int c8 = t.ct & 7;
    const int B0 = (t.ct >> 3) * 54 + (c8 <= 6 ? c8 * 7 : 48);
    const int odd = B0 & 1;
    t.Bp = B0 + odd; t.Bs = odd ? B0 : B0 + 6; t.has_s = c8 < 6;
    t.oA = odd; t.oS = odd ? 0 : 6; t.oB = 0;
  } else {
    t.ct = cx.bi & 7;
    t.tb = (cx.xcd * 4 + (cx.bi >> 3)) * 4 + (cx.w & 3);
    const int odd = t.ct & 1;
    t.oA = odd + 2 * t.kh; t.oS = odd ? 0 : 4; t.oB = odd + 2 * (t.kh ^ 1);
    t.Bp = t.ct * 5 + t.oA; t.Bs = t.ct * 5 + t.oS; t.has_s = t.kh == 0;
  }
  return t;
}
template <int MODE>
__device__ __forceinline__ void gemm_phase(Ctx& cx) {
  using P = GP<MODE>;
  constexpr int NB = P::NB, KS = P::KS, NS = P::NS;
  char* ws = cx.ws;
  const int lane = cx.lane, w = cx.w;
  const u32 sb = cx.sb, vo = cx.vo;
  const int c = lane & 15, q = lane >> 4;
  const Tile T = tile_of<MODE>(cx);
  const int tb = T.tb, ct = T.ct, kh = T.kh, Bp = T.Bp, Bs = T.Bs;
  const bool has_s = T.has_s;
  const char* Xg = ws + (MODE == 1 ? O_H : MODE == 2 ? O_G1 : MODE == 3 ? O_X1 : O_G3) + (size_t)tb * (KS * 1024) + kh * 2048;
  constexpr int NE = MODE == 3 ? 7 : 3;

  u32x4 xf[NS];
  u32x2 xl[NS], xh[NS];
  const u32 vol = vo - kh * 1024;
  u32 vsh[4];
  if (MODE == 2) {
#pragma unroll
    for (int g = 0; g < 4; ++g) {
      const int l2 = 16 + c - (g == 0 ? 1 : g == 1 ? -1 : g == 2 ? 2 : -2);
      vsh[g] = (u32)((l2 >> 4) * 20480 + q * 256 + (l2 & 15) * 16);
    }
  }
  auto issueX = [&](int s, int i) __attribute__((always_inline)) {
    const int off = ((s / P::SPC) * 4 + s % P::SPC) * 1024;
    if (MODE == 2) {
      const int g0 = (off >> 10) / 5, g1 = ((off >> 10) + 2) / 5;
      xf[s] = gload16a(Xg - 20480, (g0 == g1 || !kh) ? vsh[g0] : vsh[g1], off);
      return;
    }
    if (MODE == 1) { if (i == 0) xl[s] = gload8(Xg, lane * 8, off); else xh[s] = gload8(Xg, lane * 8, off + 512); }
    else xf[s] = gload16a(Xg, (MODE == 4 && s == NS - 1) ? vol : vo, off);
  };
#pragma unroll
  for (int s = 0; s < P::A0; ++s)
#pragma unroll
    for (int i = 0; i < P::LPX; ++i) issueX(s, i);

  f32x4 acc[NB];
#pragma unroll
  for (int f = 0; f < NB; ++f) { acc[f] = f32x4{0.f, 0.f, 0.f, 0.f}; pina(acc[f]); }
  float s1 = 0.f, s2 = 0.f;
  const bf16x2 ones = {(__bf16)1.0f, (__bf16)1.0f};
  f32x4 asum = {0.f, 0.f, 0.f, 0.f}, asq = asum;
  u32x4 onesf = {0x3f803f80u, 0x3f803f80u, 0x3f803f80u, 0x3f803f80u};
  if (MODE == 1) { pina(asum); pina(asq); pin(onesf); }
  constexpr int NT = MODE == 1 ? 2 : 1, BS = MODE == 1 ? 32 : 1024;
  u32 rb[NT][3][3];
#pragma unroll
  for (int t = 0; t < NT; ++t)
#pragma unroll
    for (int k = 0; k < 3; ++k) {
      const u32 b0 = sb + k * 65536 + kh * (2 * NB * 1024) + (MODE == 1 ? (8 * q + 4 * (t ^ (q & 1)) + (c >> 2)) * 160 + (c & 3) * 8 : vo);
      rb[t][0][k] = b0 + T.oA * BS; rb[t][1][k] = b0 + T.oS * BS; rb[t][2][k] = b0 + T.oB * BS;
      pin(rb[t][0][k]); pin(rb[t][1][k]); if (MODE != 3) pin(rb[t][2][k]);
    }
  auto rdW = [&](int s, int f) __attribute__((always_inline)) -> u32x4 {
    const int off = wpos(MODE, s / P::SPC) + (s % P::SPC) * NB * 1024 + (f < NE - 1 ? f : (f == NE - 1 ? 0 : f - NE)) * BS;
    const int kind = f < NE - 1 ? 0 : (f == NE - 1 ? 1 : 2);
    if (MODE != 1) {
      u32 base = rb[0][kind][off >> 16];
      if (MODE == 4 && s == NS - 1) base -= kh * (NB * 1024);
      return lds_ld<u32x4>(base + (off & 65535));
    }
    const s16x4 a = __builtin_amdgcn_ds_read_tr16_b64_v4i16((LDS_AS s16x4*)(size_t)(rb[0][kind][off >> 16] + (off & 65535)));
    const s16x4 b = __builtin_amdgcn_ds_read_tr16_b64_v4i16((LDS_AS s16x4*)(size_t)(rb[NT - 1][kind][off >> 16] + (off & 65535)));
    const u32x2 ua = __builtin_bit_cast(u32x2, a), ub = __builtin_bit_cast(u32x2, b);
    return u32x4{ua.x, ua.y, ub.x, ub.y};
  };
  u32x4 wf[2][NB];
#pragma unroll
  for (int f = 0; f < NB; ++f) wf[0][f] = rdW(0, f);
  u32 csw[2];
  u32x2 psv[4];

  sfor<NS>([&](auto S_) __attribute__((always_inline)) {
    constexpr int s = decltype(S_)::value;
    if constexpr (P::bar(s)) wait_vm_bar<P::waitn(s)>();
    else wait_vm<P::waitn(s)>();
    if (MODE == 1) { pin(xl[s]); pin(xh[s]); xf[s] = u32x4{xl[s].x, xl[s].y, xh[s].x, xh[s].y}; }
    else pina(xf[s]);
    SCHED_FENCE();
#pragma unroll
    for (int f = 0; f < NB; ++f) {
      if (s + 1 < NS) wf[(s + 1) & 1][f] = rdW(s + 1, f);
      if (MODE == 1) asm volatile("v_mfma_f32_16x16x32_bf16 %0, %1, %2, %0" : "+a"(acc[f]) : "v"(wf[s & 1][f]), "v"(xf[s]));
      else asm volatile("v_mfma_f32_16x16x32_bf16 %0, %1, %2, %0" : "+a"(acc[f]) : "v"(wf[s & 1][f]), "a"(xf[s]));
      if (MODE == 1 && f == 1) asm volatile("v_mfma_f32_16x16x32_bf16 %0, %1, %2, %0" : "+a"(asum) : "v"(onesf), "v"(xf[s]));
      if (MODE == 1 && f == 3) asm volatile("v_mfma_f32_16x16x32_bf16 %0, %1, %1, %0" : "+a"(asq) : "v"(xf[s]));
      constexpr int nx = P::nx(s);
      if (f < nx) issueX(P::hi(s) + f / P::LPX, f % P::LPX);
      else if (f - nx < P::npf(s)) pf_piece<(P::pfm(s) ? P::pfm(s) : 1)>(cx, P::pfc(s), f - nx);
      if ((MODE == 1 || MODE == 3) && s == 2 && f == 4) {
        const char* cs = ws + (MODE == 1 ? O_CS1 : O_CS3);
#pragma unroll
        for (int k = 0; k < (MODE == 3 ? 2 : 1); ++k) {
          const int sl = k * 4 + (c >> 2);
          csw[k] = gload4s(cs, (u32)((sl >= NE - 1 ? Bs : Bp + sl) * 64 + q * 16 + (c & 3) * 4));
        }
      }
      if (MODE == 3 && s == 3 && f >= 3) psv[f - 3] = gload8(ws + O_PS + tb * 2048, lane * 8, (f - 3) * 512);
      if (MODE == 1 && s == NS - 1 && f < 3) {
        const int B = f < 2 ? Bp + f : Bs;
        cx.hv[f] = gload8a(ws + O_H + (size_t)tb * 20480 + (B >> 1) * 1024 + (B & 1) * 256, (u32)((q & 1) * 512 + (q >> 1) * 128 + c * 8), 0);
      }
      SCHED_FENCE();
    }
  });
  asm volatile("s_nop 7\n\ts_nop 7" ::: "memory");
#pragma unroll
  for (int f = 0; f < NB; ++f) pina(acc[f]);
  STAMP(1 + MODE, 14);
  asm volatile("s_barrier" ::: "memory");
  if (MODE != 3) {
    auto wr = [&](u32 a, f32x4& v) __attribute__((always_inline)) { asm volatile("ds_write_b128 %0, %1" :: "v"(a), "a"(v) : "memory"); };
    const u32 base = sb + P::SCR + (w & 3) * 5120 + vo;
    const u32 mine = base + kh * 2048, theirs = base + (kh ^ 1) * 2048;
    wr(mine, acc[3]); wr(mine + 1024, acc[4]);
    if (kh) wr(mine + 2048, acc[2]);
    if (MODE == 1) {
      pina(asum); pina(asq);
      const float dg = (c & 2) ? ((c & 1) ? asq[3] : asq[2]) : ((c & 1) ? asq[1] : asq[0]);
      s1 = asum[0]; s2 = qsum(q == (c >> 2) ? dg : 0.f);
      lds_st(sb + 28 * KB + w * 512 + lane * 8, f32x2{s1, s2});
    }
    WG_BARRIER();
    acc[0] += lds_ld<f32x4>(theirs); acc[1] += lds_ld<f32x4>(theirs + 1024); acc[2] += lds_ld<f32x4>(theirs + 2048);
    if (MODE == 1) { f32x2 o = lds_ld<f32x2>(sb + 28 * KB + (w ^ 4) * 512 + lane * 8); s1 += o.x; s2 += o.y; }
  }
  STAMP(1 + MODE, 13);
  const u32 sofs = (u32)(Bs * 512 + (q >> 1) * 256);
  const int gq = (q & 1) * 2 + (q >> 1);
  if (MODE == 1 || MODE == 3) {
    pin(csw[0]); if (MODE == 3) pin(csw[1]);
    float a = s1, bq = s2;
    if (MODE == 3) {
#pragma unroll
      for (int t = 0; t < 4; ++t) { pin(psv[t]); a += __uint_as_float(psv[t].x); bq += __uint_as_float(psv[t].y); }
      a = qsum(a); bq = qsum(bq);
    }
    const float m = a * (1.0f / D);
    const float var = fmaxf(bq * (1.0f / D) - m * m, 0.f);
    const float r = __builtin_amdgcn_rsqf(var + 1e-5f);
    float rr = r;
    if (MODE == 1) { rr = r * __builtin_amdgcn_rsqf(var * r * r + 1e-5f); cx.m = m; cx.r = r; }
    const float mr = -m * rr;
    const u32 tofs = (u32)(tb * (MODE == 1 ? 20480 : 54 * 1024) + c * 16);
    char* dst = ws + (MODE == 1 ? O_G1 : O_G3);
    auto act = [&](auto F_) __attribute__((always_inline)) -> u32x2 {
      constexpr int f = decltype(F_)::value;
      const f32x4 cs = {__uint_as_float(dpp<0x150 + (f & 3) * 4>(csw[f >> 2])), __uint_as_float(dpp<0x151 + (f & 3) * 4>(csw[f >> 2])),
                        __uint_as_float(dpp<0x152 + (f & 3) * 4>(csw[f >> 2])), __uint_as_float(dpp<0x153 + (f & 3) * 4>(csw[f >> 2]))};
      const f32x4 xx = acc[f] * rr + cs * mr;
      const f32x2 o0 = gelu2(f32x2{xx[0], xx[1]}), o1 = gelu2(f32x2{xx[2], xx[3]});
      return u32x2{pkbf(o0.x, o0.y), pkbf(o1.x, o1.y)};
    };
    sfor<(NE - 1) / 2>([&](auto J_) __attribute__((always_inline)) {
      constexpr int j = decltype(J_)::value;
      if (MODE == 3) { pf_piece<4>(cx, 5 + j, 0); pf_piece<4>(cx, 5 + j, 1); } else { pf_piece<3>(cx, 0, 0); pf_piece<3>(cx, 0, 1); }
      const u32x2 p0 = act(std::integral_constant<int, 2 * j>{});
      if (MODE == 3) pf_piece<4>(cx, 5 + j, 2); else { pf_piece<3>(cx, 0, 2); pf_piece<3>(cx, 0, 3); }
      const u32x2 p1 = act(std::integral_constant<int, 2 * j + 1>{});
      gstore16(dst, tofs + (u32)((Bp + 2 * j) * 512 + gq * 256), widen2(p0, p1));
    });
    if (has_s) { const u32x4 v = widen(act(std::integral_constant<int, NE - 1>{})); if (!(q & 1)) gstore16(dst, tofs + sofs, v); }
  } else if (MODE == 2) {
    const float m = cx.m, r = cx.r;
    pina(cx.hv[0]); pina(cx.hv[1]); pina(cx.hv[2]);
    u32 p[6];
#pragma unroll
    for (int k = 0; k < 6; ++k) {
      const u32 hv = cx.hv[k >> 1][k & 1];
      p[k] = pkbf((bflo(hv) - m) * r + acc[k >> 1][(k & 1) * 2], (bfhi(hv) - m) * r + acc[k >> 1][(k & 1) * 2 + 1]);
    }
#pragma unroll
    for (int k = 0; k < 3; ++k) cx.xv[k] = u32x2{p[2 * k], p[2 * k + 1]};
    gstore16(ws + O_X1 + (size_t)tb * 20480 + Bp * 512, (u32)(gq * 256 + c * 16), widen2(cx.xv[0], cx.xv[1]));
    if (has_s) { const u32x4 v = widen(cx.xv[2]); if (!(q & 1)) gstore16(ws + O_X1 + (size_t)tb * 20480, sofs + c * 16, v); }
    if (!has_s) { p[4] = 0u; p[5] = 0u; }
#pragma unroll
    for (int k = 0; k < 6; ++k) {
      s1 = __builtin_amdgcn_fdot2_f32_bf16(as_bf2(p[k]), ones, s1, false);
      s2 = __builtin_amdgcn_fdot2_f32_bf16(as_bf2(p[k]), as_bf2(p[k]), s2, false);
    }
    s1 = qsum(s1); s2 = qsum(s2);
    if (q == 0) gstore8(ws + O_PS + tb * 2048 + (ct * 2 + kh) * 128, c * 8, u32x2{__float_as_uint(s1), __float_as_uint(s2)});
  } else {
    u32 p[6];
#pragma unroll
    for (int k = 0; k < 6; ++k) {
      const u32 hv = cx.hv[k >> 1][k & 1], xv = cx.xv[k >> 1][k & 1];
      p[k] = pkbf(bflo(hv) + bflo(xv) + acc[k >> 1][(k & 1) * 2], bfhi(hv) + bfhi(xv) + acc[k >> 1][(k & 1) * 2 + 1]);
    }
    u16* orow = cx.out + (size_t)(tb * 16 + c) * D;
    *(u32x4*)(orow + Bp * 16 + gq * 8) = widen2(u32x2{p[0], p[1]}, u32x2{p[2], p[3]});
    if (has_s) *(u32x2*)(orow + Bs * 16 + q * 4) = u32x2{p[4], p[5]};
  }
  STAMP(1 + MODE, 15);
}

__global__ __launch_bounds__(512, 2) __attribute__((amdgpu_waves_per_eu(2, 2)))
void k_main(In in, char* __restrict__ ws, u16* __restrict__ out) {
  __shared__ __attribute__((aligned(16))) char smem[K2_LDS];
  STAMP(0, 0);
  asm volatile("" :: "s"(in.x), "s"(in.lar), "s"(in.aim), "s"(in.cre), "s"(in.cim), "s"(in.ldt), "s"(in.dsk), "s"(in.w1), "s"(in.w2), "s"(in.w3), "s"(in.w4), "s"(ws), "s"(out));
  Ctx c;
  c.ws = ws; c.out = out;
  c.lane = threadIdx.x & 63; c.w = __builtin_amdgcn_readfirstlane(threadIdx.x >> 6);
  c.sb = lds_addr(smem); c.vo = c.lane * 16;
  c.xcd = blockIdx.x & 7; c.bi = blockIdx.x >> 3;
  const int rt = c.xcd * 4 + (c.bi >> 3), ct = c.bi & 7, rt3 = c.xcd * 2 + (c.bi >> 4);
  auto setup = [&]() __attribute__((always_inline)) {
  c.Wg[1] = (const char*)(in.w1 + ct * 80 + c.w * 16 * D);
  c.Wg[2] = ws + O_W2 + (size_t)ct * (100 * KB) + c.w * 2560;
  c.Wg[3] = ws + O_W3 + (size_t)(c.bi & 15) * (140 * KB) + c.w * 3584;
  c.Wg[4] = ws + O_W4 + (size_t)ct * (270 * KB) + c.w * 2560;
#pragma unroll
  for (int i = 0; i < 3; ++i) {
    const int u = i * 64 + c.lane, r = (u / 10) & 15, cc = u % 10;
    c.wo[i] = (u32)((r ^ ((r >> 3) << 2)) * (D * 2) + cc * 16);
  }
#pragma unroll
  for (int i = 0; i < 3; ++i) pin(c.wo[i]);
  };
  const bool isconv = c.bi >= 8 && c.bi < 28;
  const int cg = c.xcd * 20 + c.bi - 8;
  const u32 epo = (c.w == 0 && c.lane >= 1 && c.lane <= 16) ? (u32)(2048 + blockIdx.x * 2048 + (c.lane - 1) * 128) : 0u;
  auto mktag = [&](u32x2& epv) __attribute__((always_inline)) {
    pin(epv);
    const unsigned long long ep = ((unsigned long long)epv.y << 32) | epv.x;
    unsigned long long t = (ep ^ (ep >> 31)) * 0x9E3779B97F4A7C15ull + 0x632BE59BD9B4E019ull;
    t ^= t >> 29;
    c.tlo = __builtin_amdgcn_readfirstlane((u32)t); c.thi = __builtin_amdgcn_readfirstlane((u32)(t >> 32));
  };
  const int tun = (c.bi < 8 ? c.bi : c.bi - 20) * 8 + c.xcd;
  if (c.bi < 8) {
    u32x4 xv[5];
    const char* xr = (const char*)(in.x + (size_t)(tun * 32 + c.w * 4) * D);
    const u32 xo = (u32)(c.lane >> 4) * (D * 2) + (u32)(c.lane & 15) * 16u;
#pragma unroll
    for (int i = 0; i < 5; ++i) xv[i] = gload16(xr, xo, i * 256);
    u32x2 epv = gload8(ws + O_EP, epo, 0);
    wait_vm<0>();
#pragma unroll
    for (int i = 0; i < 5; ++i) pin(xv[i]);
    mktag(epv);
    ln_compute(c, xv);
    WG_BARRIER();
    ln_store(c, tun);
    STAMP(0, 1);
    asm volatile("s_waitcnt vmcnt(0)\n\ts_barrier" ::: "memory");
    post(c, 160, 64 + threadIdx.x, MB_LN, tun);
    STAMP(0, 2);
  }
  if (__builtin_expect(isconv, 1)) {
    const int kch = c.w >> 1, set = c.w & 1, d = cg * 4 + kch;
    const u32 n4 = (u32)(d * NM * 2 + c.lane * 4);
    KP kp;
    kp.lar = gload4(in.lar, n4); kp.aim = gload4(in.aim, n4); kp.ldt = gload2(in.ldt, (u32)d * 2u);
    kp.cr = gload4(in.cre, n4); kp.ci = gload4(in.cim, n4);
    u32x2 dsv = gload8(in.dsk, (u32)cg * 8u, 0);
    u32x2 epv = gload8(ws + O_EP, epo, 0);
    setup();
    const u32 tab = c.sb + kch * KG_SZ;
    const KI ki = kg_index(tab, c.sb + KG_KT + kch * 2048, set, c.lane);
    u32 ix[11];
    const int gq = c.w >> 2;
    kg_lagidx(threadIdx.x & 255, c.sb + KG_KT + gq * 2048, ix);
    wait_vm<0>();
    pin(kp.lar); pin(kp.aim); pin(kp.ldt); pin(kp.cr); pin(kp.ci); pin(dsv);
    mktag(epv);
    {
      const int k = cg * 8 + c.w;
      const u16* p = k < 540 ? in.w4 : (k < 1080 ? in.w3 : in.w1);
      const int u = k < 540 ? k : (k < 1080 ? k - 540 : k - 1080);
      asm volatile("s_mov_b32 m0, %2\n\ts_nop 0\n\tglobal_load_lds_dword %0, %1" :: "v"((u32)u * 4096u + (u32)c.lane * 64u), "s"(p), "s"(c.sb + 150 * KB) : "memory");
    }
    STAMP(0, 1);
    const float ks = kg_tables(kp, tab, set, c.lane);
    if (set == 0 && c.lane == 0) lds_st(c.sb + KG_KT + 8192 + kch * 4, ks);
    WG_BARRIER();
    STAMP(0, 2);
    kg_mfma(ki);
    await(c, MB_LN, 64);
    WG_BARRIER();
    STAMP(0, 3);
    u32x4 uv0 = gload16(ws + O_UT + (size_t)cg * 16384 + c.w * 2048, c.vo, 0), uv1 = gload16(ws + O_UT + (size_t)cg * 16384 + c.w * 2048, c.vo, 1024);
    u32x4 stv[2];
#pragma unroll
    for (int i = 0; i < 2; ++i) stv[i] = gload16a(ws + O_ST0 + c.w * 2048, c.vo, i * 1024);
    u32 v0[11], v1[11];
#pragma unroll
    for (int k = 0; k < 11; ++k) { v0[k] = lds_ld<u16>(ix[k]); v1[k] = lds_ld<u16>(ix[k] + 4096); }
    SCHED_FENCE();
    kg_expand(v0, c.sb + gq * CV_CH, threadIdx.x & 255);
    kg_expand(v1, c.sb + (2 + gq) * CV_CH, threadIdx.x & 255);
    if (c.w < 4 && c.lane < 32) lds_st(c.sb + c.w * CV_CH + 4 * CV_KC + 4096 + c.lane * 16, u32x4{0u, 0u, 0u, 0u});
    const f32x4 ksc = lds_ld<f32x4>(c.sb + KG_KT + 8192);
    STAMP(1, 0);
    conv_phase(c, dsv, cg, ksc, uv0, uv1, stv);
  } else {
    u32x2 epv = gload8(ws + O_EP, epo, 0);
    if (c.bi == 30 && c.w >= 6) {
      const u32 o = (u32)c.lane * PGS;
      const char* p = c.w == 6 ? ws : (const char*)out;
      asm volatile("s_mov_b32 m0, %2\n\ts_nop 0\n\tglobal_load_lds_dword %0, %1" :: "v"(o < (c.w == 6 ? (u32)O_MB : (u32)(L * D * 2)) ? o : 0u), "s"(p), "s"(c.sb + 150 * KB) : "memory");
    }
    const int nu = (tun >= 64 || tun < 26) ? 2 : 1, u0 = tun < 64 ? 64 + tun : tun - 64, ud = tun < 64 ? 64 : 32;
    for (int k = 0; k < nu; ++k) tu_load(c, tu_of(in, ws, u0 + k * ud), c.sb + k * 40 * KB);
    setup();
    if (tun >= 94) {
      char* z = ws + (tun == 94 ? O_G1 - 20480 : O_G1 + (size_t)L * D * 2);
      for (int i = threadIdx.x; i < 1280; i += 512) gstore16(z, i * 16u, u32x4{0u, 0u, 0u, 0u});
    }
    asm volatile("s_waitcnt vmcnt(0)\n\ts_barrier" ::: "memory");
    mktag(epv);
    for (int k = 0; k < nu; ++k) tu_emit(c, tu_of(in, ws, u0 + k * ud), c.sb + k * 40 * KB, c.sb + (98 + k) * KB);
    STAMP(0, 3);
    DRAIN_BARRIER();
    post(c, 1, 248 + c.xcd, MB_AT, c.bi < 8 ? c.bi : c.bi - 20);
#pragma unroll
    for (int k = 0; k < 5; ++k) pf_chunk<1>(c, k);
    asm volatile("s_waitcnt vmcnt(0)" ::: "memory");
  }
  STAMP(1, 15);
  if (c.bi == 31) {
    await(c, MB_AT, 12);
#pragma unroll
    for (int k = 0; k < 4; ++k) post(c, 64, k * 64 + threadIdx.x, MB_T, c.xcd);
  }
  await(c, MB_T, 8);
  asm volatile("s_barrier" ::: "memory");
  if (c.w) { pf7_chunk<2>(c, 0); pf7_chunk<2>(c, 1); pf7_chunk<2>(c, 2); }
  if (c.bi == 31) {
    await(c, MB_AC, 20);
#pragma unroll
    for (int k = 0; k < 4; ++k) post(c, 64, k * 64 + threadIdx.x, MB_C, c.xcd);
  }
  await(c, MB_C, 8);
  if (blockIdx.x == 0 && threadIdx.x == 0) gstore8(ws + O_EP, 0u, u32x2{c.tlo, c.thi});
  asm volatile("s_barrier" ::: "memory");
  STAMP(2, 0);
  gemm_phase<1>(c);
  DRAIN_BARRIER();
  {
    const int t = threadIdx.x, r2 = (rt + (t >> 3) - 1) & 31;
    post(c, 24, (((r2 & 3) * 8 + (t & 7)) << 3) + (r2 >> 2), MB_1, (2 - (t >> 3)) * 8 + ct);
  }
  await(c, MB_1, 24);
  asm volatile("s_barrier" ::: "memory");
  STAMP(3, 0);
  gemm_phase<2>(c);
  DRAIN_BARRIER();
  post(c, 16, (((c.bi & 16) + threadIdx.x) << 3) + c.xcd, MB_2, c.bi & 15);
  if (c.w) { pf7_chunk<3>(c, 3); pf7_chunk<3>(c, 4); }
  await(c, MB_2, 16);
  asm volatile("s_barrier" ::: "memory");
  STAMP(4, 0);
  gemm_phase<3>(c);
  DRAIN_BARRIER();
  post(c, 16, (((c.bi & 16) + threadIdx.x) << 3) + c.xcd, MB_3, c.bi & 15);
  await(c, MB_3, 16);
  asm volatile("s_barrier" ::: "memory");
  STAMP(5, 0);
  gemm_phase<4>(c);
}

}

extern "C" void kernel_launch(void* const* d_in, const int* in_sizes, int n_in, void* d_out, int out_size, void* d_ws,
                              size_t ws_size, hipStream_t stream) {
  In in;
  in.x = (const u16*)d_in[0]; in.lar = (const u16*)d_in[1]; in.aim = (const u16*)d_in[2]; in.cre = (const u16*)d_in[3];
  in.cim = (const u16*)d_in[4]; in.ldt = (const u16*)d_in[5]; in.dsk = (const u16*)d_in[6];
  in.w1 = (const u16*)d_in[8]; in.w2 = (const u16*)d_in[9]; in.w3 = (const u16*)d_in[10]; in.w4 = (const u16*)d_in[11];
  k_main<<<256, 512, 0, stream>>>(in, (char*)d_ws, (u16*)d_out);
}
```

```cpp
#include <hip/hip_runtime.h>
#include <hip/hip_bf16.h>
#include <stdint.h>
#include <utility>

typedef unsigned short u16;
typedef unsigned u32;
typedef _Float16 f16;
typedef __attribute__((ext_vector_type(2))) _Float16 f16x2;
typedef __attribute__((ext_vector_type(8))) _Float16 f16x8;
typedef __attribute__((ext_vector_type(2))) __bf16 bf16x2;
typedef __attribute__((ext_vector_type(2))) float f32x2;
typedef __attribute__((ext_vector_type(4))) float f32x4;
typedef __attribute__((ext_vector_type(2))) unsigned u32x2;
typedef __attribute__((ext_vector_type(4))) unsigned u32x4;
typedef __attribute__((ext_vector_type(4))) short s16x4;
#define LDS_AS __attribute__((address_space(3)))

namespace {

constexpr int L = 2048, D = 640, NM = 128, F = 1728;
constexpr size_t O_UT  = 0;
constexpr size_t O_H   = O_UT  + (size_t)D * L * 2;
constexpr size_t O_G1  = O_H   + (size_t)L * D * 2 + 20480;
constexpr size_t O_X1  = O_G1  + (size_t)L * D * 2 + 20480;
constexpr size_t O_G3  = O_X1  + (size_t)L * D * 2;
constexpr size_t O_W2  = O_G3  + (size_t)L * F * 2;
constexpr size_t O_W3  = O_W2  + (size_t)D * D * 2;
constexpr size_t O_W4  = O_W3  + (size_t)16 * 20 * 7 * 1024;
constexpr size_t O_CS1 = O_W4  + (size_t)D * F * 2 + 16384;
constexpr size_t O_CS3 = O_CS1 + D * 4;
constexpr size_t O_ST0 = O_CS3 + 1792 * 4;
constexpr size_t O_PS  = O_ST0 + L * 8;
constexpr size_t O_EP  = O_PS + 128 * 2048;
constexpr size_t O_MB  = O_EP + 2048;
constexpr int MB_LN = 0, MB_1 = 512, MB_2 = 768, MB_3 = 896, MB_C = 1024, MB_T = 1152, MB_AC = 1280, MB_AT = 1536;

__device__ __forceinline__ float bflo(u32 v) { return __uint_as_float(v << 16); }
__device__ __forceinline__ float bfhi(u32 v) { return __uint_as_float(v & 0xffff0000u); }
__device__ __forceinline__ u32 pkbf(float a, float b) {
  f32x2 v = {a, b};
  return __builtin_bit_cast(u32, __builtin_convertvector(v, bf16x2));
}
__device__ __forceinline__ float rbf(float f) { return bflo(pkbf(f, 0.f)); }
__device__ __forceinline__ u32 pkh(float a, float b) {
  f32x2 v = {a, b};
  return __builtin_bit_cast(u32, __builtin_convertvector(v, f16x2));
}
__device__ __forceinline__ u32 umax(u32 a, u32 b) { return a > b ? a : b; }
__device__ __forceinline__ bf16x2 as_bf2(u32 v) { return __builtin_bit_cast(bf16x2, v); }
__device__ __forceinline__ u32 lds_addr(const void* p) { return (u32)(size_t)(LDS_AS const void*)p; }
template <class T> __device__ __forceinline__ T lds_ld(u32 a) { return *(LDS_AS const T*)(size_t)a; }
template <class T> __device__ __forceinline__ void lds_st(u32 a, T v) { *(LDS_AS T*)(size_t)a = v; }

template <bool SC1>
__device__ __forceinline__ void glds16(const void* sbase, u32 voff, int goff, u32 lbase, int loff) {
  u32 t;
  if (SC1) asm volatile("s_add_i32 m0, %4, %5\n\tv_add_u32 %0, %3, %1\n\tglobal_load_lds_dwordx4 %0, %2 sc1"
               : "=&v"(t) : "v"(voff), "s"(sbase), "i"(goff), "s"(lbase), "i"(loff) : "memory", "scc");
  else asm volatile("s_add_i32 m0, %4, %5\n\tv_add_u32 %0, %3, %1\n\tglobal_load_lds_dwordx4 %0, %2"
               : "=&v"(t) : "v"(voff), "s"(sbase), "i"(goff), "s"(lbase), "i"(loff) : "memory", "scc");
}
__device__ __forceinline__ void glds16r(const void* sbase, u32 voff, u32 goff, u32 laddr) {
  u32 t;
  asm volatile("s_nop 4\n\ts_mov_b32 m0, %4\n\tv_add_u32 %0, %3, %1\n\tglobal_load_lds_dwordx4 %0, %2"
               : "=&v"(t) : "v"(voff), "s"(sbase), "s"(goff), "s"(laddr) : "memory");
}
template <bool SC1>
__device__ __forceinline__ void glds16_half(const void* sbase, u32 voff, int goff, u32 lbase, int loff) {
  u32 t;
  if (SC1) asm volatile("s_add_i32 m0, %4, %5\n\tv_add_u32 %0, %3, %1\n\ts_mov_b32 exec_hi, 0\n\tglobal_load_lds_dwordx4 %0, %2 sc1\n\ts_mov_b32 exec_hi, -1"
               : "=&v"(t) : "v"(voff), "s"(sbase), "i"(goff), "s"(lbase), "i"(loff) : "memory", "scc");
  else asm volatile("s_add_i32 m0, %4, %5\n\tv_add_u32 %0, %3, %1\n\ts_mov_b32 exec_hi, 0\n\tglobal_load_lds_dwordx4 %0, %2\n\ts_mov_b32 exec_hi, -1"
               : "=&v"(t) : "v"(voff), "s"(sbase), "i"(goff), "s"(lbase), "i"(loff) : "memory", "scc");
}
__device__ __forceinline__ u32x4 gload16(const void* sbase, u32 voff, int goff) {
  u32x4 r; u32 t;
  asm volatile("v_add_u32 %1, %4, %2\n\tglobal_load_dwordx4 %0, %1, %3 sc1" : "=v"(r), "=&v"(t) : "v"(voff), "s"(sbase), "i"(goff) : "memory");
  return r;
}
__device__ __forceinline__ u32x2 gload8(const void* sbase, u32 voff, int goff) {
  u32x2 r; u32 t;
  asm volatile("v_add_u32 %1, %4, %2\n\tglobal_load_dwordx2 %0, %1, %3 sc1" : "=v"(r), "=&v"(t) : "v"(voff), "s"(sbase), "i"(goff) : "memory");
  return r;
}
__device__ __forceinline__ u32x4 gload16a(const void* sbase, u32 voff, int goff) {
  u32x4 r; u32 t;
  asm volatile("v_add_u32 %1, %4, %2\n\tglobal_load_dwordx4 %0, %1, %3 sc1" : "=a"(r), "=&v"(t) : "v"(voff), "s"(sbase), "i"(goff) : "memory");
  return r;
}
__device__ __forceinline__ u32x2 gload8a(const void* sbase, u32 voff, int goff) {
  u32x2 r; u32 t;
  asm volatile("v_add_u32 %1, %4, %2\n\tglobal_load_dwordx2 %0, %1, %3 sc1" : "=a"(r), "=&v"(t) : "v"(voff), "s"(sbase), "i"(goff) : "memory");
  return r;
}
__device__ __forceinline__ u32x4 gload16ap(const void* sbase, u32 voff) {
  u32x4 r;
  asm volatile("global_load_dwordx4 %0, %1, %2" : "=a"(r) : "v"(voff), "s"(sbase) : "memory");
  return r;
}
__device__ __forceinline__ void gstore16(void* sbase, u32 voff, u32x4 v) {
  asm volatile("global_store_dwordx4 %0, %1, %2 sc1\n\ts_nop 1" :: "v"(voff), "v"(v), "s"(sbase) : "memory");
}
__device__ __forceinline__ void gstore8(void* sbase, u32 voff, u32x2 v) {
  asm volatile("global_store_dwordx2 %0, %1, %2 sc1\n\ts_nop 0" :: "v"(voff), "v"(v), "s"(sbase) : "memory");
}
__device__ __forceinline__ void gstore4(void* sbase, u32 voff, u32 v) {
  asm volatile("global_store_dword %0, %1, %2 sc1\n\ts_nop 0" :: "v"(voff), "v"(v), "s"(sbase) : "memory");
}
constexpr int SPIN = 1 << 16;
__device__ __forceinline__ float qsum(float a) {
  auto r = __builtin_amdgcn_permlane16_swap(__float_as_uint(a), __float_as_uint(a), false, false);
  a = __uint_as_float(r[0]) + __uint_as_float(r[1]);
  auto r2 = __builtin_amdgcn_permlane32_swap(__float_as_uint(a), __float_as_uint(a), false, false);
  return __uint_as_float(r2[0]) + __uint_as_float(r2[1]);
}
template <class T> __device__ __forceinline__ void pin(T& x) { asm volatile("" : "+v"(x)); }
template <class T> __device__ __forceinline__ void pina(T& x) { asm volatile("" : "+a"(x)); }
template <int N> __device__ __forceinline__ void wait_vm() { asm volatile("s_waitcnt vmcnt(%0)" ::"i"(N) : "memory"); }
#define SCHED_FENCE() __builtin_amdgcn_sched_barrier(0)
template <class Fn, int... I> __device__ __forceinline__ void sfor_impl(std::integer_sequence<int, I...>, Fn&& f) {
  (f(std::integral_constant<int, I>{}), ...);
}
template <int N, class Fn> __device__ __forceinline__ void sfor(Fn&& f) { sfor_impl(std::make_integer_sequence<int, N>{}, f); }
template <int N> __device__ __forceinline__ void wait_vm_bar() { asm volatile("s_waitcnt vmcnt(%0)\n\ts_barrier" ::"i"(N) : "memory"); }

__device__ __forceinline__ f32x2 gelu2(f32x2 x) {
  const float c0 = -2.0f * 0.7978845608028654f * 1.4426950408889634f;
  const float c1 = c0 * 0.044715f;
  const f32x2 z = x * __builtin_elementwise_fma(x * x, f32x2{c1, c1}, f32x2{c0, c0});
  const f32x2 d = f32x2{__builtin_amdgcn_exp2f(z.x), __builtin_amdgcn_exp2f(z.y)} + 1.0f;
  return x * f32x2{__builtin_amdgcn_rcpf(d.x), __builtin_amdgcn_rcpf(d.y)};
}
__device__ __forceinline__ u32x4 widen(u32x2 p) {
  auto a = __builtin_amdgcn_permlane16_swap(p.x, p.x, false, false);
  auto b = __builtin_amdgcn_permlane16_swap(p.y, p.y, false, false);
  return u32x4{(u32)a[0], (u32)b[0], (u32)a[1], (u32)b[1]};
}

__device__ __forceinline__ u32x4 widen2(u32x2 p0, u32x2 p1) {
  auto a = __builtin_amdgcn_permlane16_swap(p0.x, p1.x, false, false);
  auto b = __builtin_amdgcn_permlane16_swap(p0.y, p1.y, false, false);
  return u32x4{(u32)a[0], (u32)b[0], (u32)a[1], (u32)b[1]};
}
__device__ __forceinline__ f32x2 cmul(f32x2 a, f32x2 b) {
  f32x2 r;
  asm("v_pk_mul_f32 %0, %1, %2 op_sel_hi:[1,0]\n\t"
      "v_pk_fma_f32 %0, %1, %2, %0 op_sel:[1,1,0] op_sel_hi:[0,1,1] neg_lo:[1,0,0]" : "=&v"(r) : "v"(a), "v"(b));
  return r;
}
__device__ __forceinline__ u32 cmulh(u32 a, u32 b) {
  u32 r;
  asm("v_pk_mul_f16 %0, %1, %2 op_sel_hi:[1,0]\n\t"
      "v_pk_fma_f16 %0, %1, %2, %0 op_sel:[1,1,0] op_sel_hi:[0,1,1] neg_lo:[1,0,0]" : "=&v"(r) : "v"(a), "v"(b));
  return r;
}
template <int CTRL> __device__ __forceinline__ u32 dpp(u32 v) { return (u32)__builtin_amdgcn_update_dpp(0, (int)v, CTRL, 0xf, 0xf, true); }
__device__ __forceinline__ float rowsum16(float v) {
  v += __uint_as_float(dpp<0xB1>(__float_as_uint(v)));
  v += __uint_as_float(dpp<0x4E>(__float_as_uint(v)));
  v += __uint_as_float(dpp<0x141>(__float_as_uint(v)));
  v += __uint_as_float(dpp<0x140>(__float_as_uint(v)));
  return v;
}
__device__ __forceinline__ u32 wavemax(u32 v) {
  v = umax(v, dpp<0xB1>(v)); v = umax(v, dpp<0x4E>(v)); v = umax(v, dpp<0x141>(v)); v = umax(v, dpp<0x140>(v));
  auto r = __builtin_amdgcn_permlane16_swap(v, v, false, false);
  v = umax((u32)r[0], (u32)r[1]);
  auto r2 = __builtin_amdgcn_permlane32_swap(v, v, false, false);
  return umax((u32)r2[0], (u32)r2[1]);
}
__device__ __forceinline__ f32x2 cexpp(float rho, float th, float p) {
  float mag = __builtin_amdgcn_exp2f(rho * p);
  float ph = __builtin_amdgcn_fractf(th * p);
  return f32x2{mag * __builtin_amdgcn_cosf(ph), mag * __builtin_amdgcn_sinf(ph)};
}

#define PGS (512u * 1024u)
constexpr int CV_KC = 4160;
constexpr int CV_U = 4608;
constexpr int CV_CH = 4 * CV_KC + CV_U + 32;
constexpr int K2_LDS = 160 * 1024;
constexpr int KB = 1024;

__device__ __forceinline__ u32x2 dsr64(u32 a, int off) {
  u32x2 r;
  asm volatile("ds_read_b64 %0, %1 offset:%2" : "=v"(r) : "v"(a), "i"(off));
  return r;
}
__device__ __forceinline__ u32x4 dsr128(u32 a, int off) {
  u32x4 r;
  asm volatile("ds_read_b128 %0, %1 offset:%2" : "=a"(r) : "v"(a), "i"(off));
  return r;
}

#define CV_ACC "+a"(acc[0]), "+a"(acc[1]), "+a"(acc[2]), "+a"(acc[3]), "+a"(acc[4]), "+a"(acc[5]), "+a"(acc[6]), "+a"(acc[7])
#define CV_CASE(S)                                                                                                                        \
  if constexpr (SI == S) {                                                                                                                \
    if constexpr (KIND == 0) asm volatile("v_mfma_f32_16x16x32_f16 %" #S ", %8, %9, %" #S : CV_ACC : "v"(a), "a"(b));                     \
    else if constexpr (KIND == 1)                                                                                                         \
      asm volatile("v_mfma_f32_16x16x32_f16 %" #S ", %9, %10, %" #S "\n\tds_read_b64 %8, %11 offset:%12"                                 \
                   : CV_ACC, "=&v"(d2) : "v"(a), "a"(b), "v"(ad), "i"(OFF));                                                              \
    else                                                                                                                                  \
      asm volatile("v_mfma_f32_16x16x32_f16 %" #S ", %9, %10, %" #S "\n\tds_read_b128 %8, %11 offset:%12"                                \
                   : CV_ACC, "=&a"(d4) : "v"(a), "a"(b), "v"(ad), "i"(OFF));                                                              \
  }
template <int SI, int KIND, int OFF>
__device__ __forceinline__ void cv_step(f32x4 (&acc)[8], u32x4 a, u32x4 b, u32 ad, u32x2& d2, u32x4& d4) {
  CV_CASE(0) CV_CASE(1) CV_CASE(2) CV_CASE(3) CV_CASE(4) CV_CASE(5) CV_CASE(6) CV_CASE(7)
}
constexpr int cv_a(int cnt) { if (cnt == 34) return 7; if (cnt == 35) return 6; int a = 0; while (cnt >= 8 - a) { cnt -= 8 - a; ++a; } return a; }
constexpr int cv_s(int cnt) { if (cnt == 33 || cnt == 34) return cnt == 33 ? 6 : 7; if (cnt == 35) return 7; int a = 0; while (cnt >= 8 - a) { cnt -= 8 - a; ++a; } return a + cnt; }

constexpr int wpos(int m, int ch) {
  return m == 1 ? (ch < 3 ? (100 + 20 * ch) * KB : 20 * (ch - 3) * KB)
       : m == 2 ? (40 + 20 * ch) * KB
       : m == 3 ? 28 * ch * KB
       : ((ch + 7) & 7) * 20 * KB;
}

struct Ctx {
  char* ws; u16* out;
  u32 sb, vo;
  int lane, w, xcd, bi;
  u32 tlo, thi;
  const char* Wg[5];
  u32 wo[3];

  float m, r;
  u32x2 hv[3], xv[3];
};

template <int M> __device__ __forceinline__ void pf_piece(const Ctx& c, int ch, int i) {
  constexpr int CHB = (M == 3 ? 28 : 20) * KB, NW = M == 3 ? 4 : 3;
  const u32 wl = c.sb + c.w * (CHB / 8);
  const u32 v = M == 1 ? c.wo[i] : c.vo;
  const int go = M == 1 ? ch * 128 * D * 2 : ch * CHB + i * 1024;
  if (i == NW - 1) glds16_half<(M != 1)>(c.Wg[M], v, go, wl, wpos(M, ch) + i * 1024);
  else glds16<(M != 1)>(c.Wg[M], v, go, wl, wpos(M, ch) + i * 1024);
}
template <int M> __device__ __forceinline__ void pf_chunk(const Ctx& c, int ch) {
#pragma unroll
  for (int i = 0; i < (M == 3 ? 4 : 3); ++i) pf_piece<M>(c, ch, i);
}

__device__ __forceinline__ void pf_pair4(const Ctx& c, int ch, int i) {
  const u32 lb = ch == 8 ? c.sb + (c.w < 4 ? 140 * KB + c.w * 5120 : (c.w - 4) * 5120) : c.sb + c.w * 5120;
  glds16<true>(c.Wg[4], c.vo + c.w * 2560, ch * 20 * KB + i * 1024, lb, (ch == 8 ? 0 : wpos(4, ch)) + i * 1024);
}
template <int M> __device__ __forceinline__ void pf7_chunk(const Ctx& c, int ch) {
  constexpr int CHB = (M == 3 ? 28 : 20) * KB, NU = M == 3 ? 4 : 3;
  const char* base = c.Wg[M] - (M == 1 ? c.w * 16 * D * 2 : c.w * (CHB / 8));
#pragma unroll
  for (int i = 0; i < NU; ++i) {
    int u = (c.w - 1) * NU + i; u = u < CHB / 1024 ? u : CHB / 1024 - 1;
    u32 v = c.vo + u * 1024;
    if (M == 1) { const int x = u * 64 + c.lane, r = x / 10, cc = x - r * 10; v = (u32)((r ^ ((r >> 1) & 4)) * (D * 2) + cc * 16); }
    glds16<(M != 1)>(base, v, M == 1 ? ch * 128 * D * 2 : ch * CHB, c.sb + u * 1024, wpos(M, ch));
  }
}
#define DRAIN_BARRIER() asm volatile("s_waitcnt vmcnt(0) lgkmcnt(0)\n\ts_barrier" ::: "memory")
__device__ __forceinline__ void post(const Ctx& c, int n, int wg, int sec, int slot) {
  if ((int)threadIdx.x < n) gstore8(c.ws + O_MB, (u32)(wg * 2048 + sec + slot * 8), u32x2{c.tlo, c.thi});
}
__device__ __forceinline__ void await(const Ctx& c, int sec, int n) {
  if (c.w == 0) {
    const u32 off = (u32)(sec + (c.lane < n ? c.lane : 0) * 8);
    for (int sp = 0; sp < SPIN; ++sp) {
      u32x2 v;
      asm volatile("global_load_dwordx2 %0, %1, %2 sc1\n\ts_waitcnt vmcnt(0)" : "=v"(v) : "v"(off), "s"(c.ws + O_MB + (size_t)blockIdx.x * 2048) : "memory");
      if (__all(v.x == c.tlo && v.y == c.thi)) break;
    }
  }
}
#define WG_BARRIER() asm volatile("s_waitcnt lgkmcnt(0)\n\ts_barrier" ::: "memory")

struct In { const u16 *x, *lar, *aim, *cre, *cim, *ldt, *dsk, *w1, *w2, *w3, *w4; };

constexpr int LN_T = 100 * KB, LN_P = 1280 + 16, LN_ST = LN_T + 32 * LN_P;
__device__ __forceinline__ void ln_compute(const Ctx& c, u32x4 (&v)[5]) {
  const int row = c.w * 4 + (c.lane >> 4), i = c.lane & 15;
  const bf16x2 ones = {(__bf16)1.0f, (__bf16)1.0f};
  float s1 = 0.f, s2 = 0.f;
#pragma unroll
  for (int s = 0; s < 5; ++s)
#pragma unroll
    for (int k = 0; k < 4; ++k) {
      s1 = __builtin_amdgcn_fdot2_f32_bf16(as_bf2(v[s][k]), ones, s1, false);
      s2 = __builtin_amdgcn_fdot2_f32_bf16(as_bf2(v[s][k]), as_bf2(v[s][k]), s2, false);
    }
  const float m = rowsum16(s1) * (1.0f / D);
  const float ve = fmaxf(rowsum16(s2) * (1.0f / D) - m * m, 0.f) + 1e-5f;
  const float r = __builtin_amdgcn_rsqf(ve), mr = -m * r;
  if (i == 0) lds_st(c.sb + LN_ST + row * 8, f32x2{m, ve * r});
#pragma unroll
  for (int s = 0; s < 5; ++s) {
    u32x4 o;
#pragma unroll
    for (int k = 0; k < 4; ++k) o[k] = pkh(bflo(v[s][k]) * r + mr, bfhi(v[s][k]) * r + mr);
    lds_st(c.sb + LN_T + row * LN_P + (i + 16 * s) * 16, o);
  }
}
__device__ __forceinline__ void ln_store(const Ctx& c, int j) {
  const int lane = c.lane, gi = lane >> 4, qq = (lane & 15) >> 2, p = lane & 3;
  char* dst = c.ws + O_UT + (size_t)(252 - 4 * j) * 64;
#pragma unroll
  for (int it = 0; it < 5; ++it) {
    const int cgp = (it * 8 + c.w) * 4 + gi;
    const u32 base = c.sb + LN_T + (31 - 8 * p - qq) * LN_P + cgp * 8;
    s16x4 a = __builtin_amdgcn_ds_read_tr16_b64_v4i16((LDS_AS s16x4*)(size_t)base);
    s16x4 b = __builtin_amdgcn_ds_read_tr16_b64_v4i16((LDS_AS s16x4*)(size_t)(base - 4 * LN_P));
    u32x2 ua = __builtin_bit_cast(u32x2, a), ub = __builtin_bit_cast(u32x2, b);
    gstore16(dst, (u32)(cgp * 16384 + (lane & 15) * 16), u32x4{ua.x, ua.y, ub.x, ub.y});
  }
  if (threadIdx.x < 16) gstore16(c.ws + O_ST0 + j * 256, threadIdx.x * 16, lds_ld<u32x4>(c.sb + LN_ST + threadIdx.x * 16));
}

struct TU { const u16* W; char* Wt; int N, KS, sb2, strip, ks0, nks, wk; };
__device__ __forceinline__ TU tu_of(const In& in, char* ws, int u) {
  TU t;
  if (u < 20)      t = TU{in.w2, ws + O_W2, D, 20, 2, u >> 1, (u & 1) * 10, 10, 0};
  else if (u < 74) t = TU{in.w3, ws + O_W3, F, 20, 1, u - 20, 0, 20, 1};
  else if (u < 134) t = TU{in.w4, ws + O_W4, D, 54, 2, (u - 74) / 6, ((u - 74) % 6) * 9, 9, 0};
  else             t = TU{in.w1, nullptr, D, 20, 1, u - 134, 0, 20, 2};
  return t;
}
__device__ __forceinline__ void tu_load(const Ctx& c, const TU& t, u32 lds) {
  const int lpr2 = t.sb2 + 1;
  const char* src = (const char*)(t.W + (size_t)t.ks0 * 32 * t.N + (t.strip << (t.sb2 + 4)));
  const u32 voff = (u32)(c.lane >> lpr2) * (u32)t.N * 2u + (u32)(c.lane & ((1 << lpr2) - 1)) * 16u;
  const int ni = t.nks << t.sb2;
  const u32 step = (u32)(64 >> lpr2) * (u32)t.N * 2u;
#pragma unroll
  for (int j = 0; j < 5; ++j) {
    int i = c.w + j * 8; i = i < ni ? i : c.w;
    glds16r(src, voff, (u32)i * step, lds + i * 1024);
  }
}
__device__ __forceinline__ void tu_emit(const Ctx& c, const TU& t, u32 lds, u32 red) {
  const int lane = c.lane, w = c.w;
  const int RB = 32 << t.sb2;
  const int e = w & ((1 << t.sb2) - 1);
  const int g = lane >> 4, q = (lane & 15) >> 2, p = lane & 3;
  const int B = (t.strip << t.sb2) + e;
  int ct, f;
  if (t.wk == 1) { const int hh = B >= 54 ? 1 : 0, b2 = B - hh * 54, c8 = b2 < 48 ? b2 / 7 : 7; ct = hh * 8 + c8; f = c8 <= 6 ? b2 - c8 * 7 : b2 - 48; }
  else { ct = B / 5; f = B - ct * 5; }
  const int kstr = t.wk == 1 ? 7168 : 5120;
  char* dst = t.Wt + (size_t)(ct * t.KS + t.ks0) * kstr + f * 1024;
  const int k0 = w >> t.sb2, kst = 8 >> t.sb2;
  float acc = 0.f;
  const bf16x2 ones = {(__bf16)1.0f, (__bf16)1.0f};
  u32x4 v[5];
#pragma unroll
  for (int j = 0; j < 5; ++j) {
    int ks = k0 + j * kst; ks = ks < t.nks ? ks : k0;
    const u32 base = lds + (ks * 32 + 8 * g + q) * RB + e * 32 + p * 8;
    s16x4 a = __builtin_amdgcn_ds_read_tr16_b64_v4i16((LDS_AS s16x4*)(size_t)base);
    s16x4 b = __builtin_amdgcn_ds_read_tr16_b64_v4i16((LDS_AS s16x4*)(size_t)(base + 4 * RB));
    u32x2 ua = __builtin_bit_cast(u32x2, a), ub = __builtin_bit_cast(u32x2, b);
    v[j] = u32x4{ua.x, ua.y, ub.x, ub.y};
  }
#pragma unroll
  for (int j = 0; j < 5; ++j) {
    int ks = k0 + j * kst; ks = ks < t.nks ? ks : k0;
    if (t.wk != 2) gstore16(dst, (u32)(ks * kstr + lane * 16), v[j]);
#pragma unroll
    for (int k = 0; k < 4; ++k) acc = __builtin_amdgcn_fdot2_f32_bf16(as_bf2(v[j][k]), ones, acc, false);
  }
  if (t.wk) {
    acc = qsum(acc);
    if (lane < 16) lds_st(red + (w * 16 + lane) * 4, acc);
    WG_BARRIER();
    if (threadIdx.x < 32) {
      const u32 a = red + threadIdx.x * 4;
      gstore4(c.ws + (t.wk == 1 ? O_CS3 : O_CS1) + t.strip * 128, threadIdx.x * 4,
              __float_as_uint((lds_ld<float>(a) + lds_ld<float>(a + 128)) + (lds_ld<float>(a + 256) + lds_ld<float>(a + 384))));
    }
  }
}

constexpr int KG_SZ = 28 * KB, KG_KT = 112 * KB, TP = 528;
struct KP { u32 ldt, lar, aim, cr, ci; };
__device__ __forceinline__ u32 gload2(const void* sbase, u32 voff) {
  u32 r;
  asm volatile("global_load_ushort %0, %1, %2" : "=v"(r) : "v"(voff), "s"(sbase) : "memory");
  return r;
}
__device__ __forceinline__ u32 gload4s(const void* sbase, u32 voff) {
  u32 r;
  asm volatile("global_load_dword %0, %1, %2 sc1" : "=v"(r) : "v"(voff), "s"(sbase) : "memory");
  return r;
}
__device__ __forceinline__ u32 gload4(const void* sbase, u32 voff) {
  u32 r;
  asm volatile("global_load_dword %0, %1, %2" : "=v"(r) : "v"(voff), "s"(sbase) : "memory");
  return r;
}
__device__ __forceinline__ void kg_setA(float rho, float th, f32x2 C, u32 (&a)[21]) {
  f32x2 A[8];
  A[0] = C;
  const f32x2 E1 = cexpp(rho, th, 1.f);
  const f32x2 G1 = cexpp(rho, th, 256.f);
  const f32x2 G4 = cexpp(rho, th, 1024.f);
  const f32x2 E2 = cmul(E1, E1), E4 = cmul(E2, E2);
  A[1] = cmul(A[0], E1); A[2] = cmul(A[0], E2); A[3] = cmul(A[1], E2);
  A[4] = cmul(A[0], E4); A[5] = cmul(A[1], E4); A[6] = cmul(A[2], E4); A[7] = cmul(A[3], E4);
  const f32x2 G2 = cmul(G1, G1), G3 = cmul(G2, G1), A16 = cmul(G4, A[0]);
#pragma unroll
  for (int k = 0; k < 8; ++k) a[k] = pkh(A[k].x, A[k].y);
  const u32 g1 = pkh(G1.x, G1.y), g2 = pkh(G2.x, G2.y), g3 = pkh(G3.x, G3.y), g4 = pkh(G4.x, G4.y);
  a[16] = pkh(A16.x, A16.y);
  a[8] = cmulh(g1, a[0]); a[9] = cmulh(g1, a[2]); a[10] = cmulh(g1, a[4]); a[11] = cmulh(g1, a[6]);
  a[12] = cmulh(g2, a[0]); a[13] = cmulh(g2, a[4]); a[14] = cmulh(g3, a[0]); a[15] = cmulh(g3, a[4]);
  a[17] = cmulh(g1, a[16]); a[18] = cmulh(g2, a[16]); a[19] = cmulh(g3, a[16]); a[20] = cmulh(g4, a[16]);
}
__device__ __forceinline__ void kg_setB(float rho, float th, u32 (&b)[32]) {
  f32x2 lo[8], hi[4];
  lo[1] = cexpp(rho, -th, 8.f);
  hi[1] = cexpp(rho, -th, 64.f);
  lo[2] = cmul(lo[1], lo[1]); lo[3] = cmul(lo[2], lo[1]); lo[4] = cmul(lo[2], lo[2]);
  lo[5] = cmul(lo[4], lo[1]); lo[6] = cmul(lo[4], lo[2]); lo[7] = cmul(lo[4], lo[3]);
  hi[2] = cmul(hi[1], hi[1]); hi[3] = cmul(hi[2], hi[1]);
  b[0] = 0x00003c00u;
#pragma unroll
  for (int k = 1; k < 8; ++k) b[k] = pkh(lo[k].x, lo[k].y);
#pragma unroll
  for (int j = 1; j < 4; ++j) {
    b[8 * j] = pkh(hi[j].x, hi[j].y);
#pragma unroll
    for (int k = 1; k < 8; ++k) b[8 * j + k] = cmulh(b[8 * j], b[k]);
  }
}
__device__ __forceinline__ float kg_tables(const KP& p, u32 tab, int set, int lane) {
  const float LOG2E = 1.4426950408889634f;
  const int sh = set ? 0 : 16;
  const float dt = rbf(__builtin_amdgcn_exp2f(bflo(p.ldt) * LOG2E));
  const float rho = (-rbf(__builtin_amdgcn_exp2f(bfhi(p.lar << sh) * LOG2E)) * dt) * LOG2E;
  const float th = (bfhi(p.aim << sh) * dt) * 0.15915494309189535f;
  const u32 mx = wavemax(umax(umax(p.cr & 0x7fffu, (p.cr >> 16) & 0x7fffu), umax(p.ci & 0x7fffu, (p.ci >> 16) & 0x7fffu)));
  u32 eb = (mx >> 7) + 1u;
  eb = eb < 2u ? 2u : (eb > 252u ? 252u : eb);
  const float sc = __uint_as_float(eb << 23), isc = __uint_as_float((254u - eb) << 23);
  const u32 ta = tab + set * 256 + lane * 4;
  u32 a[21], b[32];
  kg_setA(rho, th, f32x2{bfhi(p.cr << sh) * isc, bfhi(p.ci << sh) * isc}, a);
  kg_setB(rho, th, b);
#pragma unroll
  for (int k = 0; k < 21; ++k) asm volatile("ds_write_b32 %0, %1 offset:%2" :: "v"(ta), "v"(a[k]), "i"(k * TP) : "memory");
#pragma unroll
  for (int k = 0; k < 32; ++k) asm volatile("ds_write_b32 %0, %1 offset:%2" :: "v"(ta), "v"(b[k]), "i"((21 + k) * TP) : "memory");
  return 2.0f * dt * sc;
}
struct KI { u32 pa, pb, o[4], ob; };
__device__ __forceinline__ KI kg_index(u32 tab, u32 ktb, int at, int lane) {
  const int r = lane & 15, g = lane >> 4;
  int ra = at * 16 + r; ra = ra > 20 ? 20 : ra;
  KI k;
  k.pa = tab + ra * TP + g * 16; k.pb = tab + (21 + r) * TP + g * 16;
  const int sbs = at == 0 ? (g < 2 ? 8 : (g == 2 ? 4 : 2)) : 1;
  const int base = at == 0 ? (g < 2 ? 4 * g : (g == 2 ? 256 : 384)) : (g == 0 ? 512 : 640 + (g > 1 ? 64 : 0));
  const int sk = at == 0 ? 1 : (g == 0 ? 32 : 64), x2 = (at == 0 && g == 3) ? 62 : 0;
  const u32 o = ktb + (base + r * sbs) * 2;
  k.o[0] = o; k.o[1] = o + sk * 2; k.o[2] = o + (2 * sk + x2) * 2; k.o[3] = o + (3 * sk + x2) * 2;
  k.ob = sbs * 32;
  pin(k.pa); pin(k.pb); pin(k.o[0]); pin(k.o[1]); pin(k.o[2]); pin(k.o[3]); pin(k.ob);
  return k;
}
__device__ __forceinline__ void kg_mfma(const KI& k) {
  f32x4 acc0 = {0.f, 0.f, 0.f, 0.f}, acc1 = acc0;
  f16x8 a[8], b0[8], b1[8];
#pragma unroll
  for (int s = 0; s < 8; ++s) { a[s] = lds_ld<f16x8>(k.pa + s * 64); b0[s] = lds_ld<f16x8>(k.pb + s * 64); b1[s] = lds_ld<f16x8>(k.pb + 16 * TP + s * 64); }
  SCHED_FENCE();
#pragma unroll
  for (int s = 0; s < 8; ++s) {
    acc0 = __builtin_amdgcn_mfma_f32_16x16x32_f16(a[s], b0[s], acc0, 0, 0, 0);
    acc1 = __builtin_amdgcn_mfma_f32_16x16x32_f16(a[s], b1[s], acc1, 0, 0, 0);
  }
  asm volatile("s_nop 7\n\ts_nop 7" : "+a"(acc0), "+a"(acc1));
#pragma unroll
  for (int e = 0; e < 4; ++e) { lds_st(k.o[e], (f16)acc0[e]); lds_st(k.o[e] + k.ob, (f16)acc1[e]); }
}
__device__ __forceinline__ void kg_lagidx(int t, u32 kt, u32 (&ix)[11]) {
#pragma unroll
  for (int k = 0; k < 11; ++k) {
    const int lag = t * 8 + k;
    const int tt = k < 8 ? t : t + 1;
    const int s = tt < 32 ? 0 : (tt < 64 ? 1 : (tt < 128 ? 2 : 3));
    const int add = ((1 << s) >> 1) - (s > 0 ? 1 : 0);
    ix[k] = kt + (128 * s + ((lag + add + ((lag >> s) & (s > 0 ? 1 : 0))) >> s)) * 2;
    pin(ix[k]);
  }
}
__device__ __forceinline__ void kg_expand(const u32 (&v)[11], u32 kc, int t) {
  u32 e[5], o[5];
#pragma unroll
  for (int k = 0; k < 5; ++k) { e[k] = v[2 * k] | (v[2 * k + 1] << 16); o[k] = v[2 * k + 1] | (v[2 * k + 2] << 16); }
  const u32 a = kc + 32 + t * 16;
  lds_st(a, u32x4{e[0], e[1], e[2], e[3]});
  lds_st(a + CV_KC, u32x4{o[0], o[1], o[2], o[3]});
  lds_st(a + 2 * CV_KC, u32x4{e[1], e[2], e[3], e[4]});
  lds_st(a + 3 * CV_KC, u32x4{o[1], o[2], o[3], o[4]});
  if (t == 0) {
    const u32x4 z = {0u, 0u, 0u, 0u};
#pragma unroll
    for (int cc = 0; cc < 4; ++cc) lds_st(kc + cc * CV_KC, z);
    lds_st(kc + 16, z);
    lds_st(kc + CV_KC + 16, u32x4{0u, 0u, 0u, v[0] << 16});
    lds_st(kc + 2 * CV_KC + 16, u32x4{0u, 0u, 0u, e[0]});
    lds_st(kc + 3 * CV_KC + 16, u32x4{0u, 0u, v[0] << 16, o[0]});
  }
}

__device__ __forceinline__ void conv_phase(Ctx& c, u32x2 dsv, int cg, f32x4 ksc, u32x4 uv0, u32x4 uv1, u32x4 (&stv)[2]) {
  char* ws = c.ws;
  const int lane = c.lane, w = c.w;
  const int ch = w & 3, hh = w >> 2;
  const u32 sb = c.sb;
  const u32 mya = sb + ch * CV_CH;
  const int r = lane & 15, g = lane >> 4;
  wait_vm<2>();
  pin(uv0); pin(uv1);
  {
    const u32 ua = sb + (lane & 3) * CV_CH + 4 * CV_KC + (w * 32 + (lane >> 2)) * 16;
    lds_st(ua, uv0); lds_st(ua + 256, uv1);
  }
  WG_BARRIER();

  const u32 pa = mya + ((r + 1) & 3) * CV_KC + (((r + 1) & ~3) + 8 * g) * 2 + hh * 256;
  const u32 pb = mya + 4 * CV_KC + (2032 - 16 * r + 8 * g) * 2 - 7 * 512 + hh * 256;
  f32x4 acc[8];
#pragma unroll
  for (int i = 0; i < 8; ++i) { acc[i] = f32x4{0.f, 0.f, 0.f, 0.f}; pina(acc[i]); }
  u32x2 Al[2][8], Ah[2][8];
  u32x4 B[2][8], w1r[5];
#pragma unroll
  for (int a = 0; a < 8; ++a) {
    Al[0][a] = dsr64(pa, a * 512); Ah[0][a] = dsr64(pa, a * 512 + 8);
    B[0][a] = dsr128(pb, (7 - a) * 512);
    if (a == 3) asm volatile("s_waitcnt lgkmcnt(6)" ::: "memory");
  }
  sfor<4>([&](auto R_) __attribute__((always_inline)) {
    constexpr int rho = decltype(R_)::value, cur = rho & 1, nxt = cur ^ 1;
    asm volatile("s_waitcnt lgkmcnt(0)" ::: "memory");
#pragma unroll
    for (int a = 0; a < 8; ++a) { pin(Al[cur][a]); pin(Ah[cur][a]); pina(B[cur][a]); }
    SCHED_FENCE();
    sfor<36>([&](auto C_) __attribute__((always_inline)) {
      constexpr int cnt = decltype(C_)::value, s = cv_s(cnt), a = cv_a(cnt), i = cnt < 24 ? cnt / 3 : 0, kind = cnt % 3;
      constexpr bool rd = rho < 3 && cnt < 24;
      const u32x4 av = {Al[cur][a].x, Al[cur][a].y, Ah[cur][a].x, Ah[cur][a].y};
      if constexpr (!rd) cv_step<s, 0, 0>(acc, av, B[cur][s - a], pa, Al[nxt][0], B[nxt][0]);
      else if constexpr (kind == 0) cv_step<s, 1, i * 512 + (rho + 1) * 64>(acc, av, B[cur][s - a], pa, Al[nxt][i], B[nxt][i]);
      else if constexpr (kind == 1) cv_step<s, 1, i * 512 + (rho + 1) * 64 + 8>(acc, av, B[cur][s - a], pa, Ah[nxt][i], B[nxt][i]);
      else cv_step<s, 2, (7 - i) * 512 + (rho + 1) * 64>(acc, av, B[cur][s - a], pb, Al[nxt][i], B[nxt][i]);
      if (rho < 3 && cnt >= 25 && cnt < 34 && (cnt - 25) % 3 == 0) pf_piece<1>(c, rho, (cnt - 25) / 3);
      if constexpr (rho == 3 && cnt < 25 && cnt % 5 == 0) {
        const int x = (w * 5 + cnt / 5) * 64 + lane, rr = x / 10, cc = x - rr * 10;
        w1r[cnt / 5] = gload16ap(c.Wg[1] - w * 16 * D * 2 + 3 * 128 * D * 2, (u32)((rr ^ ((rr >> 1) & 4)) * (D * 2) + cc * 16));
      }
      SCHED_FENCE();
    });
  });
  asm volatile("s_nop 7\n\ts_nop 7" : "+a"(acc[0]), "+a"(acc[1]), "+a"(acc[2]), "+a"(acc[3]), "+a"(acc[4]), "+a"(acc[5]), "+a"(acc[6]), "+a"(acc[7]));
  asm volatile("s_barrier" ::: "memory");
  {
    const u32 ex = mya + hh * 8192 + g * 256 + ((r ^ (4 * g)) * 16);
#pragma unroll
    for (int i = 0; i < 8; ++i) asm volatile("ds_write_b128 %0, %1 offset:%2" :: "v"(ex), "a"(acc[i]), "i"(i * 1024) : "memory");
  }
  WG_BARRIER();
  wait_vm<14>();
  {
    const int d0 = cg * 4;
    char* hb = ws + O_H + (d0 >> 5) * 1024 + ((d0 >> 2) & 1) * 512 + ((d0 & 31) >> 3) * 128;
    const float dk[4] = {bflo(dsv.x), bfhi(dsv.x), bflo(dsv.y), bfhi(dsv.y)};
#pragma unroll
    for (int i = 0; i < 2; ++i) {
      const int l = w * 256 + i * 128 + lane * 2;
      pina(stv[i]);
      const float m0 = __uint_as_float(stv[i][0]), sd0 = __uint_as_float(stv[i][1]);
      const float m1 = __uint_as_float(stv[i][2]), sd1 = __uint_as_float(stv[i][3]);
      float v0[4], v1[4];
      const int eg = (l >> 2) & 3;
      const u32 eo = (u32)((l >> 8) * 1024 + eg * 256 + ((((l >> 4) & 15) ^ (4 * eg)) * 16) + (l & 3) * 4);
#pragma unroll
      for (int k = 0; k < 4; ++k) {
        const f16x2 u = lds_ld<f16x2>(sb + k * CV_CH + 4 * CV_KC + (2046 - l) * 2);
        const f32x2 ya = lds_ld<f32x2>(sb + k * CV_CH + eo), yb = lds_ld<f32x2>(sb + k * CV_CH + 8192 + eo);
        v0[k] = (ya.x + yb.x) * ksc[k] + (m0 + (sd0 + dk[k]) * (float)u[1]);
        v1[k] = (ya.y + yb.y) * ksc[k] + (m1 + (sd1 + dk[k]) * (float)u[0]);
      }
      gstore16(hb, (u32)((l >> 4) * 20480 + (l & 15) * 8),
               u32x4{pkbf(v0[0], v0[1]), pkbf(v0[2], v0[3]), pkbf(v1[0], v1[1]), pkbf(v1[2], v1[3])});
    }
  }
  DRAIN_BARRIER();
  post(c, 1, 248 + c.xcd, MB_AC, c.bi - 8);
#pragma unroll
  for (int i = 0; i < 5; ++i) asm volatile("ds_write_b128 %0, %1 offset:%2" :: "v"(sb + w * 5120 + c.vo), "a"(w1r[i]), "i"(i * 1024) : "memory");
  asm volatile("s_waitcnt lgkmcnt(0)" ::: "memory");
}

template <int MODE> struct GP {
  static constexpr int NB = MODE == 3 ? 7 : 5;
  static constexpr int KS = MODE == 4 ? 54 : 20;
  static constexpr int SPC = MODE == 3 ? 4 : 2;
  static constexpr int NS = MODE == 4 ? 27 : (MODE == 3 ? 20 : 10);
  static constexpr int LPX = MODE == 1 ? 2 : 1;
  static constexpr int A0 = MODE == 1 ? 3 : 4;
  static constexpr int AH = MODE <= 2 ? 6 : 10;
  static constexpr int hi(int s) {
    int h = A0 + 2 * s; h = h < s + AH ? h : s + AH; return h < NS ? h : NS;
  }
  static constexpr int pfm(int s) { return MODE == 1 ? ((s == 6 || s == 7) ? 2 : 0) : MODE == 2 ? ((s == 6 || s == 7) ? 3 : 0) : 4; }
  static constexpr int pfc(int s) { return MODE == 1 ? s - 3 : MODE == 2 ? s - 5 : 0; }
  static constexpr int npf(int s) {
    return MODE <= 2 ? (pfm(s) == 0 ? 0 : (pfm(s) == 3 ? 4 : 3))
         : MODE == 3 ? (s == 6 ? 3 : ((s >= 8 && s <= 17) ? 1 : 0))
         : ((s >= 4 && s < 16) ? ((s & 3) == 0 ? 2 : 1) : 0);
  }
  static constexpr int nx(int s) { return (hi(s + 1) - hi(s)) * LPX; }
  static constexpr int before(int s) { int n = A0 * LPX; for (int i = 0; i < s; ++i) n += nx(i) + npf(i); return n; }
  static constexpr int xend(int x) {
    if (x < A0) return (x + 1) * LPX;
    int s = 0; while (hi(s + 1) <= x) ++s;
    return before(s) + (x - hi(s) + 1) * LPX;
  }
  static constexpr bool bar(int s) {
    return MODE == 1 ? (s == 4 || s == 6) : MODE == 2 ? s == 6 : MODE == 3 ? (s == 8 || s == 12)
         : (s == 4 || s == 8 || s == 12 || s == 15 || s == 19 || s == 23);
  }
  static constexpr int waitn(int s) {
    int n = before(s) - xend(s);
    if (MODE == 4 && (s == 15 || s == 19 || s == 23)) {
      const int lim = before(s) - before(s - 7);
      n = n < lim ? n : lim;
    }
    return n;
  }
  static constexpr int SCR = (MODE <= 2 ? 140 : 0) * KB;
};

struct Tile { int tb, ct, kh, Bp, Bs, oA, oS, oB; bool has_s; };
template <int MODE> __device__ __forceinline__ Tile tile_of(const Ctx& cx) {
  Tile t;
  t.kh = MODE == 3 ? 0 : cx.w >> 2;
  if (MODE == 3) {
    t.ct = cx.bi & 15;
    t.tb = (cx.xcd * 2 + (cx.bi >> 4)) * 8 + cx.w;
    const int c8 = t.ct & 7;
    const int B0 = (t.ct >> 3) * 54 + (c8 <= 6 ? c8 * 7 : 48);
    const int odd = B0 & 1;
    t.has_s = c8 < 6;
    t.oA = odd; t.oS = (odd || !t.has_s) ? 0 : 6; t.oB = 0;
    t.Bp = B0 + odd; t.Bs = B0 + t.oS;
  } else {
    t.ct = cx.bi & 7;
    t.tb = (cx.xcd * 4 + (cx.bi >> 3)) * 4 + (cx.w & 3);
    const int odd = t.ct & 1;
    t.oA = odd + 2 * t.kh; t.oS = odd ? 0 : 4; t.oB = odd + 2 * (t.kh ^ 1);
    t.Bp = t.ct * 5 + t.oA; t.Bs = t.ct * 5 + t.oS; t.has_s = t.kh == 0;
  }
  return t;
}
template <int MODE>
__device__ __forceinline__ void gemm_phase(Ctx& cx) {
  using P = GP<MODE>;
  constexpr int NB = P::NB, KS = P::KS, NS = P::NS;
  char* ws = cx.ws;
  const int lane = cx.lane, w = cx.w;
  const u32 sb = cx.sb, vo = cx.vo;
  const int c = lane & 15, q = lane >> 4;
  const Tile T = tile_of<MODE>(cx);
  const int tb = T.tb, ct = T.ct, kh = T.kh, Bp = T.Bp, Bs = T.Bs;
  const bool has_s = T.has_s;
  const char* Xg = ws + (MODE == 1 ? O_H : MODE == 2 ? O_G1 : MODE == 3 ? O_X1 : O_G3) + (size_t)tb * (KS * 1024) + kh * 2048;
  constexpr int NE = MODE == 3 ? 7 : 3;

  u32x4 xf[NS];
  u32x2 xl[NS], xh[NS];
  const u32 vol = vo - kh * 1024;
  u32 vsh[4];
  if (MODE == 2) {
#pragma unroll
    for (int g = 0; g < 4; ++g) {
      const int l2 = 16 + c - (g == 0 ? 1 : g == 1 ? -1 : g == 2 ? 2 : -2);
      vsh[g] = (u32)((l2 >> 4) * 20480 + q * 256 + (l2 & 15) * 16);
    }
  }
  auto issueX = [&](int s, int i) __attribute__((always_inline)) {
    const int off = ((s / P::SPC) * 4 + s % P::SPC) * 1024;
    if (MODE == 2) {
      const int g0 = (off >> 10) / 5, g1 = ((off >> 10) + 2) / 5;
      xf[s] = gload16a(Xg - 20480, (g0 == g1 || !kh) ? vsh[g0] : vsh[g1], off);
      return;
    }
    if (MODE == 1) { if (i == 0) xl[s] = gload8(Xg, lane * 8, off); else xh[s] = gload8(Xg, lane * 8, off + 512); }
    else xf[s] = gload16a(Xg, (MODE == 4 && s == NS - 1) ? vol : vo, off);
  };
#pragma unroll
  for (int s = 0; s < P::A0; ++s)
#pragma unroll
    for (int i = 0; i < P::LPX; ++i) issueX(s, i);

  f32x4 acc[NB];
#pragma unroll
  for (int f = 0; f < NB; ++f) { acc[f] = f32x4{0.f, 0.f, 0.f, 0.f}; pina(acc[f]); }
  float s1 = 0.f, s2 = 0.f;
  const bf16x2 ones = {(__bf16)1.0f, (__bf16)1.0f};
  f32x4 asum = {0.f, 0.f, 0.f, 0.f}, asq = asum;
  u32x4 onesf = {0x3f803f80u, 0x3f803f80u, 0x3f803f80u, 0x3f803f80u};
  if (MODE == 1) { pina(asum); pina(asq); pin(onesf); }
  constexpr int NT = MODE == 1 ? 2 : 1, BS = MODE == 1 ? 32 : 1024;
  u32 rb[NT][3][3];
#pragma unroll
  for (int t = 0; t < NT; ++t)
#pragma unroll
    for (int k = 0; k < 3; ++k) {
      const u32 b0 = sb + k * 65536 + kh * (2 * NB * 1024) + (MODE == 1 ? (8 * q + 4 * (t ^ (q & 1)) + (c >> 2)) * 160 + (c & 3) * 8 : vo);
      rb[t][0][k] = b0 + T.oA * BS; rb[t][1][k] = b0 + T.oS * BS; rb[t][2][k] = b0 + T.oB * BS;
      pin(rb[t][0][k]); pin(rb[t][1][k]); if (MODE != 3) pin(rb[t][2][k]);
    }
  auto rdW = [&](int s, int f) __attribute__((always_inline)) -> u32x4 {
    const int off = wpos(MODE, s / P::SPC) + (s % P::SPC) * NB * 1024 + (f < NE - 1 ? f : (f == NE - 1 ? 0 : f - NE)) * BS;
    const int kind = f < NE - 1 ? 0 : (f == NE - 1 ? 1 : 2);
    if (MODE != 1) {
      u32 base = rb[0][kind][off >> 16];
      if (MODE == 4 && s == NS - 1) base -= kh * (NB * 1024);
      return lds_ld<u32x4>(base + (off & 65535));
    }
    const s16x4 a = __builtin_amdgcn_ds_read_tr16_b64_v4i16((LDS_AS s16x4*)(size_t)(rb[0][kind][off >> 16] + (off & 65535)));
    const s16x4 b = __builtin_amdgcn_ds_read_tr16_b64_v4i16((LDS_AS s16x4*)(size_t)(rb[NT - 1][kind][off >> 16] + (off & 65535)));
    const u32x2 ua = __builtin_bit_cast(u32x2, a), ub = __builtin_bit_cast(u32x2, b);
    return u32x4{ua.x, ua.y, ub.x, ub.y};
  };
  u32x4 wf[2][NB];
#pragma unroll
  for (int f = 0; f < NB; ++f) wf[0][f] = rdW(0, f);
  u32 csw[2];
  u32x2 psv[4];

  sfor<NS>([&](auto S_) __attribute__((always_inline)) {
    constexpr int s = decltype(S_)::value;
    if constexpr (P::bar(s)) wait_vm_bar<P::waitn(s)>();
    else wait_vm<P::waitn(s)>();
    if (MODE == 1) { pin(xl[s]); pin(xh[s]); xf[s] = u32x4{xl[s].x, xl[s].y, xh[s].x, xh[s].y}; }
    else pina(xf[s]);
    SCHED_FENCE();
#pragma unroll
    for (int f = 0; f < NB; ++f) {
      if (s + 1 < NS) wf[(s + 1) & 1][f] = rdW(s + 1, f);
      if (MODE == 1) asm volatile("v_mfma_f32_16x16x32_bf16 %0, %1, %2, %0" : "+a"(acc[f]) : "v"(wf[s & 1][f]), "v"(xf[s]));
      else asm volatile("v_mfma_f32_16x16x32_bf16 %0, %1, %2, %0" : "+a"(acc[f]) : "v"(wf[s & 1][f]), "a"(xf[s]));
      if (MODE == 1 && f == 1) asm volatile("v_mfma_f32_16x16x32_bf16 %0, %1, %2, %0" : "+a"(asum) : "v"(onesf), "v"(xf[s]));
      if (MODE == 1 && f == 3) asm volatile("v_mfma_f32_16x16x32_bf16 %0, %1, %1, %0" : "+a"(asq) : "v"(xf[s]));
      constexpr int nx = P::nx(s);
      if (f < nx) issueX(P::hi(s) + f / P::LPX, f % P::LPX);
      else if (f - nx < P::npf(s)) {
        if (MODE == 3 && s != 6) pf_pair4(cx, s < 13 ? 1 : 3, (s - 8) % 5);
        else if (MODE == 4) pf_pair4(cx, 8 + (s - 4) / 4 * 2, (s & 3) == 0 ? f - nx : (s & 3) + 1);
        else pf_piece<(P::pfm(s) ? P::pfm(s) : 1)>(cx, P::pfc(s), f - nx);
      }
      if ((MODE == 1 || MODE == 3) && s == 2 && f == 4) {
        const char* cs = ws + (MODE == 1 ? O_CS1 : O_CS3);
#pragma unroll
        for (int k = 0; k < (MODE == 3 ? 2 : 1); ++k) {
          const int sl = k * 4 + (c >> 2);
          csw[k] = gload4s(cs, (u32)((sl >= NE - 1 ? Bs : Bp + sl) * 64 + q * 16 + (c & 3) * 4));
        }
      }
      if (MODE == 3 && s == 3 && f >= 3) psv[f - 3] = gload8(ws + O_PS + tb * 2048, lane * 8, (f - 3) * 512);
      if (MODE == 1 && s == NS - 1 && f < 3) {
        const int B = f < 2 ? Bp + f : Bs;
        cx.hv[f] = gload8a(ws + O_H + (size_t)tb * 20480 + (B >> 1) * 1024 + (B & 1) * 256, (u32)((q & 1) * 512 + (q >> 1) * 128 + c * 8), 0);
      }
      SCHED_FENCE();
    }
  });
  asm volatile("s_nop 7\n\ts_nop 7" ::: "memory");
#pragma unroll
  for (int f = 0; f < NB; ++f) pina(acc[f]);
  asm volatile("s_barrier" ::: "memory");
  if (MODE != 3) {
    auto wr = [&](u32 a, f32x4& v) __attribute__((always_inline)) { asm volatile("ds_write_b128 %0, %1" :: "v"(a), "a"(v) : "memory"); };
    const u32 base = sb + P::SCR + (w & 3) * 5120 + vo;
    const u32 mine = base + kh * 2048, theirs = base + (kh ^ 1) * 2048;
    wr(mine, acc[3]); wr(mine + 1024, acc[4]);
    if (kh) wr(mine + 2048, acc[2]);
    if (MODE == 1) {
      pina(asum); pina(asq);
      const float dg = (c & 2) ? ((c & 1) ? asq[3] : asq[2]) : ((c & 1) ? asq[1] : asq[0]);
      s1 = asum[0]; s2 = qsum(q == (c >> 2) ? dg : 0.f);
      lds_st(sb + 28 * KB + w * 512 + lane * 8, f32x2{s1, s2});
    }
    WG_BARRIER();
    acc[0] += lds_ld<f32x4>(theirs); acc[1] += lds_ld<f32x4>(theirs + 1024); acc[2] += lds_ld<f32x4>(theirs + 2048);
    if (MODE == 1) { f32x2 o = lds_ld<f32x2>(sb + 28 * KB + (w ^ 4) * 512 + lane * 8); s1 += o.x; s2 += o.y; }
  }
  const u32 sofs = (u32)(Bs * 512 + (q >> 1) * 256);
  const int gq = (q & 1) * 2 + (q >> 1);
  if (MODE == 1 || MODE == 3) {
    pin(csw[0]); if (MODE == 3) pin(csw[1]);
    float a = s1, bq = s2;
    if (MODE == 3) {
#pragma unroll
      for (int t = 0; t < 4; ++t) { pin(psv[t]); a += __uint_as_float(psv[t].x); bq += __uint_as_float(psv[t].y); }
      a = qsum(a); bq = qsum(bq);
    }
    const float m = a * (1.0f / D);
    const float var = fmaxf(bq * (1.0f / D) - m * m, 0.f);
    const float r = __builtin_amdgcn_rsqf(var + 1e-5f);
    float rr = r;
    if (MODE == 1) { rr = r * __builtin_amdgcn_rsqf(var * r * r + 1e-5f); cx.m = m; cx.r = r; }
    const float mr = -m * rr;
    const u32 tofs = (u32)(tb * (MODE == 1 ? 20480 : 54 * 1024) + c * 16);
    char* dst = ws + (MODE == 1 ? O_G1 : O_G3);
    auto act = [&](auto F_) __attribute__((always_inline)) -> u32x2 {
      constexpr int f = decltype(F_)::value;
      const f32x4 cs = {__uint_as_float(dpp<0x150 + (f & 3) * 4>(csw[f >> 2])), __uint_as_float(dpp<0x151 + (f & 3) * 4>(csw[f >> 2])),
                        __uint_as_float(dpp<0x152 + (f & 3) * 4>(csw[f >> 2])), __uint_as_float(dpp<0x153 + (f & 3) * 4>(csw[f >> 2]))};
      const f32x4 xx = acc[f] * rr + cs * mr;
      const f32x2 o0 = gelu2(f32x2{xx[0], xx[1]}), o1 = gelu2(f32x2{xx[2], xx[3]});
      return u32x2{pkbf(o0.x, o0.y), pkbf(o1.x, o1.y)};
    };
    sfor<(NE - 1) / 2>([&](auto J_) __attribute__((always_inline)) {
      constexpr int j = decltype(J_)::value;
      if (MODE == 3) { pf_pair4(cx, 5, 2 * j); if (j < 2) pf_pair4(cx, 5, 2 * j + 1); } else { pf_piece<3>(cx, 0, 0); pf_piece<3>(cx, 0, 1); }
      const u32x2 p0 = act(std::integral_constant<int, 2 * j>{});
      if (MODE == 3) pf_piece<4>(cx, 7, j); else { pf_piece<3>(cx, 0, 2); pf_piece<3>(cx, 0, 3); }
      const u32x2 p1 = act(std::integral_constant<int, 2 * j + 1>{});
      gstore16(dst, tofs + (u32)((Bp + 2 * j) * 512 + gq * 256), widen2(p0, p1));
    });
    if (has_s) { const u32x4 v = widen(act(std::integral_constant<int, NE - 1>{})); if (!(q & 1)) gstore16(dst, tofs + sofs, v); }
  } else if (MODE == 2) {
    const float m = cx.m, r = cx.r;
    pina(cx.hv[0]); pina(cx.hv[1]); pina(cx.hv[2]);
    u32 p[6];
#pragma unroll
    for (int k = 0; k < 6; ++k) {
      const u32 hv = cx.hv[k >> 1][k & 1];
      p[k] = pkbf((bflo(hv) - m) * r + acc[k >> 1][(k & 1) * 2], (bfhi(hv) - m) * r + acc[k >> 1][(k & 1) * 2 + 1]);
    }
#pragma unroll
    for (int k = 0; k < 3; ++k) cx.xv[k] = u32x2{p[2 * k], p[2 * k + 1]};
    gstore16(ws + O_X1 + (size_t)tb * 20480 + Bp * 512, (u32)(gq * 256 + c * 16), widen2(cx.xv[0], cx.xv[1]));
    if (has_s) { const u32x4 v = widen(cx.xv[2]); if (!(q & 1)) gstore16(ws + O_X1 + (size_t)tb * 20480, sofs + c * 16, v); }
    if (!has_s) { p[4] = 0u; p[5] = 0u; }
#pragma unroll
    for (int k = 0; k < 6; ++k) {
      s1 = __builtin_amdgcn_fdot2_f32_bf16(as_bf2(p[k]), ones, s1, false);
      s2 = __builtin_amdgcn_fdot2_f32_bf16(as_bf2(p[k]), as_bf2(p[k]), s2, false);
    }
    s1 = qsum(s1); s2 = qsum(s2);
    if (q == 0) gstore8(ws + O_PS + tb * 2048 + (ct * 2 + kh) * 128, c * 8, u32x2{__float_as_uint(s1), __float_as_uint(s2)});
  } else {
    u32 p[6];
#pragma unroll
    for (int k = 0; k < 6; ++k) {
      const u32 hv = cx.hv[k >> 1][k & 1], xv = cx.xv[k >> 1][k & 1];
      p[k] = pkbf(bflo(hv) + bflo(xv) + acc[k >> 1][(k & 1) * 2], bfhi(hv) + bfhi(xv) + acc[k >> 1][(k & 1) * 2 + 1]);
    }
    u16* orow = cx.out + (size_t)(tb * 16 + c) * D;
    *(u32x4*)(orow + Bp * 16 + gq * 8) = widen2(u32x2{p[0], p[1]}, u32x2{p[2], p[3]});
    if (has_s) *(u32x2*)(orow + Bs * 16 + q * 4) = u32x2{p[4], p[5]};
  }
}

__global__ __launch_bounds__(512, 2) __attribute__((amdgpu_waves_per_eu(2, 2)))
void k_main(In in, char* __restrict__ ws, u16* __restrict__ out) {
  __shared__ __attribute__((aligned(16))) char smem[K2_LDS];
  asm volatile("" :: "s"(in.x), "s"(in.lar), "s"(in.aim), "s"(in.cre), "s"(in.cim), "s"(in.ldt), "s"(in.dsk), "s"(in.w1), "s"(in.w2), "s"(in.w3), "s"(in.w4), "s"(ws), "s"(out));
  Ctx c;
  c.ws = ws; c.out = out;
  c.lane = threadIdx.x & 63; c.w = __builtin_amdgcn_readfirstlane(threadIdx.x >> 6);
  c.sb = lds_addr(smem); c.vo = c.lane * 16;
  c.xcd = blockIdx.x & 7; c.bi = blockIdx.x >> 3;
  const int rt = c.xcd * 4 + (c.bi >> 3), ct = c.bi & 7;
  auto setup = [&]() __attribute__((always_inline)) {
  c.Wg[1] = (const char*)(in.w1 + ct * 80 + c.w * 16 * D);
  c.Wg[2] = ws + O_W2 + (size_t)ct * (100 * KB) + c.w * 2560;
  c.Wg[3] = ws + O_W3 + (size_t)(c.bi & 15) * (140 * KB) + c.w * 3584;
  c.Wg[4] = ws + O_W4 + (size_t)ct * (270 * KB) + c.w * 2560;
#pragma unroll
  for (int i = 0; i < 3; ++i) {
    const int u = i * 64 + c.lane, r = (u / 10) & 15, cc = u % 10;
    c.wo[i] = (u32)((r ^ ((r >> 3) << 2)) * (D * 2) + cc * 16);
  }
#pragma unroll
  for (int i = 0; i < 3; ++i) pin(c.wo[i]);
  };
  const bool isconv = c.bi >= 8 && c.bi < 28;
  const int cg = c.xcd * 20 + c.bi - 8;
  const u32 epo = (c.w == 0 && c.lane >= 1 && c.lane <= 16) ? (u32)(2048 + blockIdx.x * 2048 + (c.lane - 1) * 128) : 0u;
  auto mktag = [&](u32x2& epv) __attribute__((always_inline)) {
    pin(epv);
    const unsigned long long ep = ((unsigned long long)epv.y << 32) | epv.x;
    unsigned long long t = (ep ^ (ep >> 31)) * 0x9E3779B97F4A7C15ull + 0x632BE59BD9B4E019ull;
    t ^= t >> 29;
    c.tlo = __builtin_amdgcn_readfirstlane((u32)t); c.thi = __builtin_amdgcn_readfirstlane((u32)(t >> 32));
  };
  const int tun = (c.bi < 8 ? c.bi : c.bi - 20) * 8 + c.xcd;
  if (c.bi < 8) {
    u32x4 xv[5];
    const char* xr = (const char*)(in.x + (size_t)(tun * 32 + c.w * 4) * D);
    const u32 xo = (u32)(c.lane >> 4) * (D * 2) + (u32)(c.lane & 15) * 16u;
#pragma unroll
    for (int i = 0; i < 5; ++i) xv[i] = gload16(xr, xo, i * 256);
    u32x2 epv = gload8(ws + O_EP, epo, 0);
    wait_vm<0>();
#pragma unroll
    for (int i = 0; i < 5; ++i) pin(xv[i]);
    mktag(epv);
    ln_compute(c, xv);
    WG_BARRIER();
    ln_store(c, tun);
    asm volatile("s_waitcnt vmcnt(0)\n\ts_barrier" ::: "memory");
    post(c, 160, 64 + threadIdx.x, MB_LN, tun);
  }
  if (__builtin_expect(isconv, 1)) {
    const int kch = c.w >> 1, set = c.w & 1, d = cg * 4 + kch;
    const u32 n4 = (u32)(d * NM * 2 + c.lane * 4);
    KP kp;
    kp.lar = gload4(in.lar, n4); kp.aim = gload4(in.aim, n4); kp.ldt = gload2(in.ldt, (u32)d * 2u);
    kp.cr = gload4(in.cre, n4); kp.ci = gload4(in.cim, n4);
    u32x2 dsv = gload8(in.dsk, (u32)cg * 8u, 0);
    u32x2 epv = gload8(ws + O_EP, epo, 0);
    setup();
    const u32 tab = c.sb + kch * KG_SZ;
    const KI ki = kg_index(tab, c.sb + KG_KT + kch * 2048, set, c.lane);
    u32 ix[11];
    const int gq = c.w >> 2;
    kg_lagidx(threadIdx.x & 255, c.sb + KG_KT + gq * 2048, ix);
    wait_vm<0>();
    pin(kp.lar); pin(kp.aim); pin(kp.ldt); pin(kp.cr); pin(kp.ci); pin(dsv);
    mktag(epv);
    {
      const int k = cg * 8 + c.w;
      const u16* p = k < 540 ? in.w4 : (k < 1080 ? in.w3 : in.w1);
      const int u = k < 540 ? k : (k < 1080 ? k - 540 : k - 1080);
      asm volatile("s_mov_b32 m0, %2\n\ts_nop 0\n\tglobal_load_lds_dword %0, %1" :: "v"((u32)u * 4096u + (u32)c.lane * 64u), "s"(p), "s"(c.sb + 150 * KB) : "memory");
    }
    const float ks = kg_tables(kp, tab, set, c.lane);
    if (set == 0 && c.lane == 0) lds_st(c.sb + KG_KT + 8192 + kch * 4, ks);
    WG_BARRIER();
    kg_mfma(ki);
    await(c, MB_LN, 64);
    WG_BARRIER();
    u32x4 uv0 = gload16(ws + O_UT + (size_t)cg * 16384 + c.w * 2048, c.vo, 0), uv1 = gload16(ws + O_UT + (size_t)cg * 16384 + c.w * 2048, c.vo, 1024);
    u32x4 stv[2];
#pragma unroll
    for (int i = 0; i < 2; ++i) stv[i] = gload16a(ws + O_ST0 + c.w * 2048, c.vo, i * 1024);
    u32 v0[11], v1[11];
#pragma unroll
    for (int k = 0; k < 11; ++k) { v0[k] = lds_ld<u16>(ix[k]); v1[k] = lds_ld<u16>(ix[k] + 4096); }
    SCHED_FENCE();
    kg_expand(v0, c.sb + gq * CV_CH, threadIdx.x & 255);
    kg_expand(v1, c.sb + (2 + gq) * CV_CH, threadIdx.x & 255);
    if (c.w < 4 && c.lane < 32) lds_st(c.sb + c.w * CV_CH + 4 * CV_KC + 4096 + c.lane * 16, u32x4{0u, 0u, 0u, 0u});
    const f32x4 ksc = lds_ld<f32x4>(c.sb + KG_KT + 8192);
    conv_phase(c, dsv, cg, ksc, uv0, uv1, stv);
  } else {
    u32x2 epv = gload8(ws + O_EP, epo, 0);
    if (c.bi == 30 && c.w >= 6) {
      const u32 o = (u32)c.lane * PGS;
      const char* p = c.w == 6 ? ws : (const char*)out;
      asm volatile("s_mov_b32 m0, %2\n\ts_nop 0\n\tglobal_load_lds_dword %0, %1 sc1" :: "v"(o < (c.w == 6 ? (u32)O_MB : (u32)(L * D * 2)) ? o : 0u), "s"(p), "s"(c.sb + 150 * KB) : "memory");
    }
    const int nu = (tun >= 64 || tun < 26) ? 2 : 1, u0 = tun < 64 ? 64 + tun : tun - 64, ud = tun < 64 ? 64 : 32;
    for (int k = 0; k < nu; ++k) tu_load(c, tu_of(in, ws, u0 + k * ud), c.sb + k * 40 * KB);
    setup();
    if (tun >= 94) {
      char* z = ws + (tun == 94 ? O_G1 - 20480 : O_G1 + (size_t)L * D * 2);
      for (int i = threadIdx.x; i < 1280; i += 512) gstore16(z, i * 16u, u32x4{0u, 0u, 0u, 0u});
    }
    asm volatile("s_waitcnt vmcnt(0)\n\ts_barrier" ::: "memory");
    mktag(epv);
    for (int k = 0; k < nu; ++k) tu_emit(c, tu_of(in, ws, u0 + k * ud), c.sb + k * 40 * KB, c.sb + (98 + k) * KB);
    DRAIN_BARRIER();
    post(c, 1, 248 + c.xcd, MB_AT, c.bi < 8 ? c.bi : c.bi - 20);
#pragma unroll
    for (int k = 0; k < 5; ++k) pf_chunk<1>(c, k);
    asm volatile("s_waitcnt vmcnt(0)" ::: "memory");
  }
  if (c.bi == 31) {
    await(c, MB_AT, 12);
#pragma unroll
    for (int k = 0; k < 4; ++k) post(c, 64, k * 64 + threadIdx.x, MB_T, c.xcd);
  }
  await(c, MB_T, 8);
  asm volatile("s_barrier" ::: "memory");
  if (c.w) { pf7_chunk<2>(c, 0); pf7_chunk<2>(c, 1); pf7_chunk<2>(c, 2); }
  if (c.bi == 31) {
    await(c, MB_AC, 20);
#pragma unroll
    for (int k = 0; k < 4; ++k) post(c, 64, k * 64 + threadIdx.x, MB_C, c.xcd);
  }
  await(c, MB_C, 8);
  if (blockIdx.x == 0 && threadIdx.x == 0) gstore8(ws + O_EP, 0u, u32x2{c.tlo, c.thi});
  asm volatile("s_barrier" ::: "memory");
  gemm_phase<1>(c);
  DRAIN_BARRIER();
  {
    const int t = threadIdx.x, r2 = (rt + (t >> 3) - 1) & 31;
    post(c, 24, (((r2 & 3) * 8 + (t & 7)) << 3) + (r2 >> 2), MB_1, (2 - (t >> 3)) * 8 + ct);
  }
  await(c, MB_1, 24);
  asm volatile("s_barrier" ::: "memory");
  gemm_phase<2>(c);
  DRAIN_BARRIER();
  post(c, 16, (((c.bi & 16) + threadIdx.x) << 3) + c.xcd, MB_2, c.bi & 15);
  if (c.w) { pf7_chunk<3>(c, 3); pf7_chunk<3>(c, 4); }
  await(c, MB_2, 16);
  asm volatile("s_barrier" ::: "memory");
  gemm_phase<3>(c);
  DRAIN_BARRIER();
  post(c, 16, (((c.bi & 16) + threadIdx.x) << 3) + c.xcd, MB_3, c.bi & 15);
  await(c, MB_3, 16);
  asm volatile("s_barrier" ::: "memory");
  gemm_phase<4>(c);
}

}

extern "C" void kernel_launch(void* const* d_in, const int* in_sizes, int n_in, void* d_out, int out_size, void* d_ws,
                              size_t ws_size, hipStream_t stream) {
  In in;
  in.x = (const u16*)d_in[0]; in.lar = (const u16*)d_in[1]; in.aim = (const u16*)d_in[2]; in.cre = (const u16*)d_in[3];
  in.cim = (const u16*)d_in[4]; in.ldt = (const u16*)d_in[5]; in.dsk = (const u16*)d_in[6];
  in.w1 = (const u16*)d_in[8]; in.w2 = (const u16*)d_in[9]; in.w3 = (const u16*)d_in[10]; in.w4 = (const u16*)d_in[11];
  k_main<<<256, 512, 0, stream>>>(in, (char*)d_ws, (u16*)d_out);
}
```
